```python
import torch

def _softmax(x, axis=-1):
    return torch.softmax(x, dim=axis)

def _gelu(x):
    return torch.nn.functional.gelu(x, approximate='tanh')

def _silu(x):
    return torch.nn.functional.silu(x)

def _sigmoid(x):
    return torch.sigmoid(x)

def _layernorm(x, axis=-1, eps=1e-05):
    m = x.mean(dim=axis, keepdim=True)
    v = x.var(dim=axis, keepdim=True, unbiased=False)
    return (x - m) / torch.sqrt(v + eps)

def _scatter_add(tgt, idx, src, axis=0):
    return tgt.clone().index_add_(axis, idx, src)

def _segment_sum(src, seg, n):
    out = torch.zeros((n,) + src.shape[1:], dtype=src.dtype)
    return out.index_add_(0, seg, src)

def _pad(x, pw):
    flat = tuple((v for pair in reversed(pw) for v in pair))
    return torch.nn.functional.pad(x, flat)
kernel_name = 'adaLN_cond__mha_block_cached__dense_activation_silu__prompt'

def setup_inputs():

    _w = lambda a: (lambda t: t.to(torch.bfloat16) if t.is_floating_point() else t)(torch.as_tensor(a))

    def _A_setup():
        B, Sq, D = (1, 2048, 576)
        H, Sk, Dh = (32, 2048, 128)
        return {'x': _w((lambda *s: torch.randn(*s, dtype=torch.bfloat16) * 0.1)(B, Sq, D)), 'k_cache': _w((lambda *s: torch.randn(*s, dtype=torch.bfloat16) * 0.8)(B, H, Sk, Dh)), 'v_cache': _w((lambda *s: torch.randn(*s, dtype=torch.bfloat16) * 3.2)(B, H, Sk, Dh)), 'wq': _w((lambda *s: torch.randn(*s, dtype=torch.bfloat16) * 0.8)(D, H * Dh) * D ** (-0.5)), 'wo': _w((lambda *s: torch.randn(*s, dtype=torch.bfloat16) * 3.2)(H * Dh, D) * (H * Dh) ** (-0.5))}
    _A_in = _A_setup()

    def _B_setup():
        B, S, D, F = (1, 2048, 576, 4608)
        return {'x': _w((lambda *s: torch.randn(*s, dtype=torch.bfloat16) * 0.1)(B, S, D)), 'w1': _w((lambda *s: torch.randn(*s, dtype=torch.bfloat16) * 3.2)(D, F) * D ** (-0.5)), 'w2': _w((lambda *s: torch.randn(*s, dtype=torch.bfloat16) * 3.2)(F, D) * F ** (-0.5))}
    _B_in = _B_setup()
    return {'A_x': _A_in['x'], 'A_k_cache': _A_in['k_cache'], 'A_v_cache': _A_in['v_cache'], 'A_wq': _A_in['wq'], 'A_wo': _A_in['wo'], 'B_x': _B_in['x'], 'B_w1': _B_in['w1'], 'B_w2': _B_in['w2'], 'c': _w((lambda *s: torch.randn(*s, dtype=torch.bfloat16) * 0.2)(1, 576) * 8.0), 'w_mod': _w((lambda *s: torch.randn(*s, dtype=torch.bfloat16) * 0.2)(576, 6 * 576) * 576 ** (-0.5) * 8.0)}

def reference(A_x, A_k_cache, A_v_cache, A_wq, A_wo, B_x, B_w1, B_w2, c, w_mod):

    def _A_ref(x, k_cache, v_cache, wq, wo):
        B, Sq, D = x.shape
        H, Dh = (32, 128)
        q = (lambda x, *d: x.permute(*d))((x @ wq).reshape(B, Sq, H, Dh), 0, 2, 1, 3)
        scores = torch.einsum('bhqd,bhkd->bhqk', q, k_cache) * Dh ** (-0.5)
        p = _softmax(scores, axis=-1)
        o = torch.einsum('bhqk,bhkd->bhqd', p, v_cache)
        return (lambda x, *d: x.permute(*d))(o, 0, 2, 1, 3).reshape(B, Sq, H * Dh) @ wo

    def _B_ref(x, w1, w2):
        h = torch.einsum('bsd,df->bsf', x, w1)
        h = _silu(h)
        out = torch.einsum('bsf,fd->bsd', h, w2)
        return out
    x = A_x
    mod = (_silu(c) @ w_mod).reshape(c.shape[0], 6, 1, -1)
    sa, ca, ga, sb, cb, gb = (mod[:, 0].clone(), mod[:, 1].clone(), mod[:, 2].clone(), mod[:, 3].clone(), mod[:, 4].clone(), mod[:, 5].clone())
    h = _layernorm(x, axis=-1) * (1 + ca) + sa
    x = x + ga * _A_ref(h, A_k_cache, A_v_cache, A_wq, A_wo)
    h = _layernorm(x, axis=-1) * (1 + cb) + sb
    x = x + gb * _B_ref(h, B_w1, B_w2)
    return x
```

```cpp
#include <hip/hip_runtime.h>
typedef unsigned short u16;

extern "C" __device__ unsigned long long get_dispatch_id() __asm("llvm.amdgcn.dispatch.id");
__global__ __launch_bounds__(256,1) void k_main(const u16* __restrict__ x, const u16* __restrict__ k, const u16* __restrict__ v, const u16* __restrict__ wq, const u16* __restrict__ wo,
                                                const u16* __restrict__ w1, const u16* __restrict__ w2, const u16* __restrict__ c, const u16* __restrict__ wm,
                                                u16* __restrict__ out, char* __restrict__ ws) {
  __shared__ __attribute__((aligned(16))) char lds[158720];
  unsigned tid = threadIdx.x;
  const unsigned long long did = get_dispatch_id(), dpt = (unsigned long long)__builtin_amdgcn_dispatch_ptr();
  const unsigned taglo = (unsigned)did * 0x9E3779B1u + 0x7F4A7C15u, taghi = ((unsigned)dpt ^ (unsigned)(dpt >> 32) ^ (unsigned)(did >> 32)) * 0x85EBCA6Bu + 0x165667B1u;
  asm volatile(
".macro mA d,a,b,c\n"
"v_mfma_f32_16x16x32_bf16 v[\\d:\\d+3], a[\\a:\\a+3], a[\\b:\\b+3], v[\\c:\\c+3]\n"
".endm\n"
".macro mZ d,a,b\n"
"v_mfma_f32_16x16x32_bf16 v[\\d:\\d+3], a[\\a:\\a+3], a[\\b:\\b+3], 0\n"
".endm\n"
".macro mB d,a,b,c\n"
"v_mfma_f32_16x16x32_bf16 v[\\d:\\d+3], a[\\a:\\a+3], v[\\b:\\b+3], v[\\c:\\c+3]\n"
".endm\n"
".macro dT d,x,o\n"
"ds_read_b64_tr_b16 a[\\d:\\d+1], v\\x offset:\\o\n"
".endm\n"
".macro dK d,x,o\n"
"ds_read_b128 a[\\d:\\d+3], v\\x offset:\\o\n"
".endm\n"
".macro eX d,s\n"
"v_exp_f32 v\\d, v\\s\n"
".endm\n"
".macro cP d,a,b\n"
"v_cvt_pk_bf16_f32 v\\d, v\\a, v\\b\n"
".endm\n"
".macro aD d,a,b\n"
"v_add_f32 v\\d, v\\a, v\\b\n"
".endm\n"
".macro mU d,a,b\n"
"v_mul_f32 v\\d, v\\a, v\\b\n"
".endm\n"
"v_and_b32 v1, 63, %[tid]\n"
"v_lshrrev_b32 v15, 6, %[tid]\n"
"s_nop 0\n"
"v_readfirstlane_b32 s56, v15\n"
"s_mov_b32 s57, %[lds]\n"
"s_and_b32 s59, %[bid], 7\n"
"s_lshr_b32 s62, %[bid], 3\n"
"s_lshr_b32 s58, s62, 2\n"
"s_and_b32 s60, s62, 3\n"
"s_lshl_b32 s61, s60, 3\n"
"s_add_u32 s61, s61, s59\n"
"v_lshlrev_b32 v7, 4, v1\n"
"s_mov_b32 s98, %[taglo]\n"
"s_mov_b32 s99, %[taghi]\n"
"s_cmp_lt_u32 %[bid], 216\n"
"s_cbranch_scc0 L_m0_%=\n"
"s_lshl_b32 s62, %[bid], 5\n"
"s_add_u32 s44, %[wmlo], 0\n"
"s_addc_u32 s45, %[wmhi], 0\n"
"s_add_u32 s44, s44, s62\n"
"s_addc_u32 s45, s45, 0\n"
"s_and_b32 s45, s45, 0xffff\n"
"s_mov_b32 s46, 3981312\n"
"s_mov_b32 s47, 0x20000\n"
"s_sub_u32 s46, s46, s62\n"
"s_add_u32 s52, %[clo], 0\n"
"s_addc_u32 s53, %[chi], 0\n"
"s_and_b32 s53, s53, 0xffff\n"
"s_mov_b32 s54, 1152\n"
"s_mov_b32 s55, 0x20000\n"
"s_lshl_b32 s62, s56, 5\n"
"v_lshrrev_b32 v96, 1, v1\n"
"v_add_u32 v96, s62, v96\n"
"v_mul_u32_u24 v97, 6912, v96\n"
"v_and_b32 v98, 1, v1\n"
"v_lshl_add_u32 v97, v98, 4, v97\n"
"v_lshlrev_b32 v96, 1, v96\n"
"s_mov_b32 s62, 0\n"
"buffer_load_dwordx4 v[56:59], v97, s[44:47], s62 offen\n"
"s_mov_b32 s62, 884736\n"
"buffer_load_dwordx4 v[60:63], v97, s[44:47], s62 offen\n"
"s_mov_b32 s62, 1769472\n"
"buffer_load_dwordx4 v[64:67], v97, s[44:47], s62 offen\n"
"s_mov_b32 s62, 2654208\n"
"buffer_load_dwordx4 v[68:71], v97, s[44:47], s62 offen\n"
"s_mov_b32 s62, 3538944\n"
"buffer_load_dwordx4 v[72:75], v97, s[44:47], s62 offen\n"
"buffer_load_ushort v80, v96, s[52:55], 0 offen offset:0\n"
"buffer_load_ushort v81, v96, s[52:55], 0 offen offset:256\n"
"buffer_load_ushort v82, v96, s[52:55], 0 offen offset:512\n"
"buffer_load_ushort v83, v96, s[52:55], 0 offen offset:768\n"
"buffer_load_ushort v84, v96, s[52:55], 0 offen offset:1024\n"
"L_m0_%=:\n"
"v_and_b32 v36, 7, v1\n"
"v_lshrrev_b32 v37, 4, v1\n"
"v_bfe_u32 v38, v1, 3, 1\n"
"v_lshl_add_u32 v16, v37, 4, v36\n"
"v_lshlrev_b32 v16, 4, v16\n"
"v_lshl_add_u32 v16, v38, 10, v16\n"
"s_lshr_b32 s62, s61, 3\n"
"s_mul_i32 s93, s62, 73728\n"
"s_bfe_u32 s62, s61, 0x20001\n"
"s_lshl_b32 s62, s62, 11\n"
"s_add_u32 s93, s93, s62\n"
"s_and_b32 s62, s61, 1\n"
"s_lshl_b32 s62, s62, 7\n"
"s_add_u32 s93, s93, s62\n"
"s_lshl_b32 s62, s56, 13\n"
"s_add_u32 s93, s93, s62\n"
"s_mul_i32 s63, s58, 294912\n"
"s_add_u32 s48, %[wslo], 262144\n"
"s_addc_u32 s49, %[wshi], 0\n"
"s_add_u32 s48, s48, s63\n"
"s_addc_u32 s49, s49, 0\n"
"s_and_b32 s49, s49, 0xffff\n"
"s_mov_b32 s50, 294912\n"
"s_mov_b32 s51, 0x20000\n"
"s_lshl_b32 s62, s56, 1\n"
"v_add_u32 v39, s62, v38\n"
"v_lshlrev_b32 v17, 7, v39\n"
"v_lshl_add_u32 v17, v37, 5, v17\n"
"s_add_u32 s40, %[wslo], 0\n"
"s_addc_u32 s41, %[wshi], 0\n"
"s_and_b32 s41, s41, 0xffff\n"
"s_mov_b32 s42, 13824\n"
"s_mov_b32 s43, 0x20000\n"
"s_lshl_b32 s62, s58, 5\n"
"s_add_u32 s62, s62, s61\n"
"s_mul_i32 s62, s62, 9216\n"
"s_add_u32 s36, %[xlo], 0\n"
"s_addc_u32 s37, %[xhi], 0\n"
"s_add_u32 s36, s36, s62\n"
"s_addc_u32 s37, s37, 0\n"
"s_and_b32 s37, s37, 0xffff\n"
"s_mov_b32 s38, 9216\n"
"s_mov_b32 s39, 0x20000\n"
"s_lshl_b32 s62, s56, 6\n"
"s_add_u32 s62, s62, s57\n"
"s_add_u32 s62, s62, 157696\n"
"v_lshlrev_b32 v19, 3, v36\n"
"v_add_u32 v18, s62, v19\n"
"s_add_u32 s62, s57, 157696\n"
"v_add_u32 v19, s62, v19\n"
"v_mul_u32_u24 v36, 1152, v36\n"
"v_lshl_add_u32 v36, v37, 3, v36\n"
"v_lshl_add_u32 v26, v39, 6, v36\n"
"buffer_load_dwordx2 v[152:153], v26, s[36:39], 0 offen\n"
"buffer_load_dwordx2 v[154:155], v26, s[36:39], 0 offen offset:32\n"
"buffer_load_dwordx2 v[156:157], v26, s[36:39], 0 offen offset:384\n"
"buffer_load_dwordx2 v[158:159], v26, s[36:39], 0 offen offset:416\n"
"buffer_load_dwordx2 v[160:161], v26, s[36:39], 0 offen offset:768\n"
"buffer_load_dwordx2 v[162:163], v26, s[36:39], 0 offen offset:800\n"
"s_getpc_b64 s[72:73]\n"
"L_pc1_%=:\n"
"s_add_u32 s72, s72, L_early_%=-L_pc1_%=\n"
"s_addc_u32 s73, s73, 0\n"
"s_and_b32 s73, s73, 0xffff\n"
"s_mov_b32 s74, L_fin_%=-L_early_%=\n"
"s_mov_b32 s75, 0x20000\n"
"s_lshr_b32 s62, %[bid], 3\n"
"s_lshl_b32 s62, s62, 2\n"
"s_add_u32 s62, s62, s56\n"
"s_lshl_b32 s62, s62, 10\n"
"s_add_u32 m0, s57, 156672\n"
"s_nop 0\n"
"buffer_load_dwordx4 v7, s[72:75], s62 offen lds\n"
"L_early_%=:\n"
"s_cmp_lt_u32 %[bid], 216\n"
"s_cbranch_scc0 L_m1_%=\n"
"s_mul_i32 s68, %[bid], 1821\n"
"s_lshr_b32 s68, s68, 16\n"
"s_mul_i32 s62, s68, 36\n"
"s_sub_u32 s65, %[bid], s62\n"
"s_cmp_ge_u32 s65, 18\n"
"s_cselect_b32 s62, 18, 0\n"
"s_cselect_b32 s69, 9, 0\n"
"s_sub_u32 s62, s65, s62\n"
"s_cmp_ge_u32 s62, 9\n"
"s_cselect_b32 s63, 1, 0\n"
"s_cselect_b32 s64, 9, 0\n"
"s_sub_u32 s62, s62, s64\n"
"s_lshr_b32 s64, s62, 1\n"
"s_lshl_b32 s70, s63, 2\n"
"s_add_u32 s64, s64, s70\n"
"s_and_b32 s70, s62, 1\n"
"s_cmp_eq_u32 s62, 8\n"
"s_cselect_b32 s64, 8, s64\n"
"s_cselect_b32 s70, s63, s70\n"
"s_add_u32 s69, s69, s64\n"
"s_lshr_b32 s62, s65, 1\n"
"s_and_b32 s63, s65, 1\n"
"s_cmp_lt_u32 s68, 2\n"
"s_cselect_b32 s69, s62, s69\n"
"s_cselect_b32 s70, s63, s70\n"
"s_lshl_b32 s69, s69, 7\n"
"s_lshl_b32 s70, s70, 4\n"
"s_add_u32 s69, s69, s70\n"
"s_mul_i32 s62, s68, 2304\n"
"s_add_u32 s69, s69, s62\n"
"v_bfe_u32 v104, v1, 2, 2\n"
"v_lshlrev_b32 v104, 5, v104\n"
"v_and_b32 v96, 3, v1\n"
"v_lshl_add_u32 v104, v96, 2, v104\n"
"s_add_u32 s62, s57, 131072\n"
"v_and_b32 v105, 15, v1\n"
"v_lshl_add_u32 v105, v105, 2, s62\n"
"s_lshl_b32 s63, s56, 6\n"
"s_add_u32 s62, s62, s63\n"
"v_and_b32 v106, 1, v1\n"
"v_lshl_add_u32 v106, v106, 5, s62\n"
"s_cmp_eq_u32 s68, 1\n"
"s_cselect_b32 s70, -1, 0\n"
"s_cmp_eq_u32 s68, 4\n"
"s_cselect_b32 s70, -1, s70\n"
"s_mov_b32 s64, 0xbfb8aa3b\n"
"s_waitcnt vmcnt(11)\n"
"v_lshlrev_b32 v80, 16, v80\n"
"s_waitcnt vmcnt(10)\n"
"v_lshlrev_b32 v81, 16, v81\n"
"s_waitcnt vmcnt(9)\n"
"v_lshlrev_b32 v82, 16, v82\n"
"s_waitcnt vmcnt(8)\n"
"v_lshlrev_b32 v83, 16, v83\n"
"s_waitcnt vmcnt(7)\n"
"v_lshlrev_b32 v84, 16, v84\n"
"v_mul_f32 v96, s64, v80\n"
"v_mul_f32 v97, s64, v81\n"
"v_mul_f32 v98, s64, v82\n"
"v_mul_f32 v99, s64, v83\n"
"v_mul_f32 v100, s64, v84\n"
"eX 96,96\n"
"eX 97,97\n"
"eX 98,98\n"
"eX 99,99\n"
"eX 100,100\n"
"v_add_f32 v96, 1.0, v96\n"
"v_add_f32 v97, 1.0, v97\n"
"v_add_f32 v98, 1.0, v98\n"
"v_add_f32 v99, 1.0, v99\n"
"v_add_f32 v100, 1.0, v100\n"
"v_rcp_f32 v96, v96\n"
"v_rcp_f32 v97, v97\n"
"v_rcp_f32 v98, v98\n"
"v_rcp_f32 v99, v99\n"
"v_rcp_f32 v100, v100\n"
"mU 80,80,96\n"
"mU 81,81,97\n"
"mU 82,82,98\n"
"mU 83,83,99\n"
"mU 84,84,100\n"
"cP 80,80,80\n"
"v_and_b32 v80, 0xffff0000, v80\n"
"cP 81,81,81\n"
"v_and_b32 v81, 0xffff0000, v81\n"
"cP 82,82,82\n"
"v_and_b32 v82, 0xffff0000, v82\n"
"cP 83,83,83\n"
"v_and_b32 v83, 0xffff0000, v83\n"
"cP 84,84,84\n"
"v_and_b32 v84, 0xffff0000, v84\n"
"v_lshlrev_b32 v96, 16, v56\n"
"v_and_b32 v97, 0xffff0000, v56\n"
"v_lshlrev_b32 v98, 16, v57\n"
"v_and_b32 v99, 0xffff0000, v57\n"
"v_lshlrev_b32 v100, 16, v58\n"
"v_and_b32 v101, 0xffff0000, v58\n"
"v_lshlrev_b32 v102, 16, v59\n"
"v_and_b32 v103, 0xffff0000, v59\n"
"mU 88,80,96\n"
"mU 89,80,97\n"
"mU 90,80,98\n"
"mU 91,80,99\n"
"mU 92,80,100\n"
"mU 93,80,101\n"
"mU 94,80,102\n"
"mU 95,80,103\n"
"v_lshlrev_b32 v96, 16, v60\n"
"v_and_b32 v97, 0xffff0000, v60\n"
"v_lshlrev_b32 v98, 16, v61\n"
"v_and_b32 v99, 0xffff0000, v61\n"
"v_lshlrev_b32 v100, 16, v62\n"
"v_and_b32 v101, 0xffff0000, v62\n"
"v_lshlrev_b32 v102, 16, v63\n"
"v_and_b32 v103, 0xffff0000, v63\n"
"v_fmac_f32 v88, v81, v96\n"
"v_fmac_f32 v89, v81, v97\n"
"v_fmac_f32 v90, v81, v98\n"
"v_fmac_f32 v91, v81, v99\n"
"v_fmac_f32 v92, v81, v100\n"
"v_fmac_f32 v93, v81, v101\n"
"v_fmac_f32 v94, v81, v102\n"
"v_fmac_f32 v95, v81, v103\n"
"v_lshlrev_b32 v96, 16, v64\n"
"v_and_b32 v97, 0xffff0000, v64\n"
"v_lshlrev_b32 v98, 16, v65\n"
"v_and_b32 v99, 0xffff0000, v65\n"
"v_lshlrev_b32 v100, 16, v66\n"
"v_and_b32 v101, 0xffff0000, v66\n"
"v_lshlrev_b32 v102, 16, v67\n"
"v_and_b32 v103, 0xffff0000, v67\n"
"v_fmac_f32 v88, v82, v96\n"
"v_fmac_f32 v89, v82, v97\n"
"v_fmac_f32 v90, v82, v98\n"
"v_fmac_f32 v91, v82, v99\n"
"v_fmac_f32 v92, v82, v100\n"
"v_fmac_f32 v93, v82, v101\n"
"v_fmac_f32 v94, v82, v102\n"
"v_fmac_f32 v95, v82, v103\n"
"v_lshlrev_b32 v96, 16, v68\n"
"v_and_b32 v97, 0xffff0000, v68\n"
"v_lshlrev_b32 v98, 16, v69\n"
"v_and_b32 v99, 0xffff0000, v69\n"
"v_lshlrev_b32 v100, 16, v70\n"
"v_and_b32 v101, 0xffff0000, v70\n"
"v_lshlrev_b32 v102, 16, v71\n"
"v_and_b32 v103, 0xffff0000, v71\n"
"v_fmac_f32 v88, v83, v96\n"
"v_fmac_f32 v89, v83, v97\n"
"v_fmac_f32 v90, v83, v98\n"
"v_fmac_f32 v91, v83, v99\n"
"v_fmac_f32 v92, v83, v100\n"
"v_fmac_f32 v93, v83, v101\n"
"v_fmac_f32 v94, v83, v102\n"
"v_fmac_f32 v95, v83, v103\n"
"v_lshlrev_b32 v96, 16, v72\n"
"v_and_b32 v97, 0xffff0000, v72\n"
"v_lshlrev_b32 v98, 16, v73\n"
"v_and_b32 v99, 0xffff0000, v73\n"
"v_lshlrev_b32 v100, 16, v74\n"
"v_and_b32 v101, 0xffff0000, v74\n"
"v_lshlrev_b32 v102, 16, v75\n"
"v_and_b32 v103, 0xffff0000, v75\n"
"v_fmac_f32 v88, v84, v96\n"
"v_fmac_f32 v89, v84, v97\n"
"v_fmac_f32 v90, v84, v98\n"
"v_fmac_f32 v91, v84, v99\n"
"v_fmac_f32 v92, v84, v100\n"
"v_fmac_f32 v93, v84, v101\n"
"v_fmac_f32 v94, v84, v102\n"
"v_fmac_f32 v95, v84, v103\n"
"v_add_f32_dpp v88, v88, v88 row_ror:2 row_mask:0xf bank_mask:0xf\n"
"v_add_f32_dpp v89, v89, v89 row_ror:2 row_mask:0xf bank_mask:0xf\n"
"v_add_f32_dpp v90, v90, v90 row_ror:2 row_mask:0xf bank_mask:0xf\n"
"v_add_f32_dpp v91, v91, v91 row_ror:2 row_mask:0xf bank_mask:0xf\n"
"v_add_f32_dpp v92, v92, v92 row_ror:2 row_mask:0xf bank_mask:0xf\n"
"v_add_f32_dpp v93, v93, v93 row_ror:2 row_mask:0xf bank_mask:0xf\n"
"v_add_f32_dpp v94, v94, v94 row_ror:2 row_mask:0xf bank_mask:0xf\n"
"v_add_f32_dpp v95, v95, v95 row_ror:2 row_mask:0xf bank_mask:0xf\n"
"v_add_f32_dpp v88, v88, v88 row_ror:4 row_mask:0xf bank_mask:0xf\n"
"v_add_f32_dpp v89, v89, v89 row_ror:4 row_mask:0xf bank_mask:0xf\n"
"v_add_f32_dpp v90, v90, v90 row_ror:4 row_mask:0xf bank_mask:0xf\n"
"v_add_f32_dpp v91, v91, v91 row_ror:4 row_mask:0xf bank_mask:0xf\n"
"v_add_f32_dpp v92, v92, v92 row_ror:4 row_mask:0xf bank_mask:0xf\n"
"v_add_f32_dpp v93, v93, v93 row_ror:4 row_mask:0xf bank_mask:0xf\n"
"v_add_f32_dpp v94, v94, v94 row_ror:4 row_mask:0xf bank_mask:0xf\n"
"v_add_f32_dpp v95, v95, v95 row_ror:4 row_mask:0xf bank_mask:0xf\n"
"v_add_f32_dpp v88, v88, v88 row_ror:8 row_mask:0xf bank_mask:0xf\n"
"v_add_f32_dpp v89, v89, v89 row_ror:8 row_mask:0xf bank_mask:0xf\n"
"v_add_f32_dpp v90, v90, v90 row_ror:8 row_mask:0xf bank_mask:0xf\n"
"v_add_f32_dpp v91, v91, v91 row_ror:8 row_mask:0xf bank_mask:0xf\n"
"v_add_f32_dpp v92, v92, v92 row_ror:8 row_mask:0xf bank_mask:0xf\n"
"v_add_f32_dpp v93, v93, v93 row_ror:8 row_mask:0xf bank_mask:0xf\n"
"v_add_f32_dpp v94, v94, v94 row_ror:8 row_mask:0xf bank_mask:0xf\n"
"v_add_f32_dpp v95, v95, v95 row_ror:8 row_mask:0xf bank_mask:0xf\n"
"v_mov_b32 v96, v88\n"
"v_mov_b32 v97, v89\n"
"v_mov_b32 v98, v90\n"
"v_mov_b32 v99, v91\n"
"v_mov_b32 v100, v92\n"
"v_mov_b32 v101, v93\n"
"v_mov_b32 v102, v94\n"
"v_mov_b32 v103, v95\n"
"v_permlane16_swap_b32 v88, v96\n"
"v_permlane16_swap_b32 v89, v97\n"
"v_permlane16_swap_b32 v90, v98\n"
"v_permlane16_swap_b32 v91, v99\n"
"v_permlane16_swap_b32 v92, v100\n"
"v_permlane16_swap_b32 v93, v101\n"
"v_permlane16_swap_b32 v94, v102\n"
"v_permlane16_swap_b32 v95, v103\n"
"aD 88,88,96\n"
"aD 89,89,97\n"
"aD 90,90,98\n"
"aD 91,91,99\n"
"aD 92,92,100\n"
"aD 93,93,101\n"
"aD 94,94,102\n"
"aD 95,95,103\n"
"v_mov_b32 v96, v88\n"
"v_mov_b32 v97, v89\n"
"v_mov_b32 v98, v90\n"
"v_mov_b32 v99, v91\n"
"v_mov_b32 v100, v92\n"
"v_mov_b32 v101, v93\n"
"v_mov_b32 v102, v94\n"
"v_mov_b32 v103, v95\n"
"v_permlane32_swap_b32 v88, v96\n"
"v_permlane32_swap_b32 v89, v97\n"
"v_permlane32_swap_b32 v90, v98\n"
"v_permlane32_swap_b32 v91, v99\n"
"v_permlane32_swap_b32 v92, v100\n"
"v_permlane32_swap_b32 v93, v101\n"
"v_permlane32_swap_b32 v94, v102\n"
"v_permlane32_swap_b32 v95, v103\n"
"aD 88,88,96\n"
"aD 89,89,97\n"
"aD 90,90,98\n"
"aD 91,91,99\n"
"aD 92,92,100\n"
"aD 93,93,101\n"
"aD 94,94,102\n"
"aD 95,95,103\n"
"ds_write_b128 v106, v[88:91]\n"
"ds_write_b128 v106, v[92:95] offset:16\n"
"s_waitcnt lgkmcnt(0)\n"
"s_barrier\n"
"ds_read_b32 v88, v105\n"
"ds_read_b32 v89, v105 offset:64\n"
"ds_read_b32 v90, v105 offset:128\n"
"ds_read_b32 v91, v105 offset:192\n"
"s_mov_b32 vcc_lo, s70\n"
"s_mov_b32 vcc_hi, s70\n"
"s_waitcnt lgkmcnt(2)\n"
"aD 88,88,89\n"
"s_waitcnt lgkmcnt(0)\n"
"aD 90,90,91\n"
"aD 88,88,90\n"
"cP 88,88,88\n"
"v_and_b32 v88, 0xffff0000, v88\n"
"v_add_f32 v96, 1.0, v88\n"
"cP 96,96,96\n"
"v_and_b32 v96, 0xffff0000, v96\n"
"v_cndmask_b32 v88, v88, v96, vcc\n"
"buffer_store_dword v88, v104, s[40:43], s69 offen sc1\n"
"s_add_u32 s66, %[wslo], 77824\n"
"s_addc_u32 s67, %[wshi], 0\n"
"s_waitcnt vmcnt(0) lgkmcnt(0)\n"
"s_barrier\n"
"s_cmp_eq_u32 s56, 0\n"
"s_cbranch_scc0 L_sg2_%=\n"
"s_lshl_b32 s62, %[bid], 3\n"
"v_mov_b32 v234, s62\n"
"v_mov_b32 v232, s98\n"
"v_mov_b32 v233, s99\n"
"s_mov_b64 exec, 1\n"
"global_store_dwordx2 v234, v[232:233], s[66:67] sc1\n"
"s_mov_b64 exec, -1\n"
"L_sg2_%=:\n"
"L_m1_%=:\n"
"s_nop 11\n"
"s_lshl_b32 s72, s56, 12\n"
"s_add_u32 s73, s57, s72\n"
"s_add_u32 s73, s73, 0\n"
"s_add_u32 s77, s57, s72\n"
"s_add_u32 s77, s77, 65536\n"
"s_add_u32 s74, s57, s72\n"
"s_add_u32 s74, s74, 16384\n"
"s_add_u32 s78, s57, s72\n"
"s_add_u32 s78, s78, 81920\n"
"s_add_u32 s75, s57, s72\n"
"s_add_u32 s75, s75, 32768\n"
"s_add_u32 s79, s57, s72\n"
"s_add_u32 s79, s79, 98304\n"
"s_add_u32 s76, s57, s72\n"
"s_add_u32 s76, s76, 49152\n"
"s_add_u32 s80, s57, s72\n"
"s_add_u32 s80, s80, 114688\n"
"s_mov_b32 s81, s72\n"
"s_mov_b32 s85, 0x3e0293ee\n"
"s_mov_b32 s86, 0x4e800000\n"
"v_bfe_u32 v212, v1, 2, 2\n"
"v_lshlrev_b32 v2, 10, v212\n"
"v_and_b32 v212, 3, v1\n"
"v_lshl_add_u32 v2, v212, 6, v2\n"
"v_lshrrev_b32 v213, 4, v1\n"
"v_bfe_u32 v214, v1, 3, 1\n"
"v_lshlrev_b32 v214, 1, v214\n"
"v_xor_b32 v214, v213, v214\n"
"v_lshl_add_u32 v2, v214, 4, v2\n"
"v_add_u32 v2, s57, v2\n"
"v_lshlrev_b32 v214, 10, v213\n"
"v_bfe_u32 v215, v1, 2, 2\n"
"v_lshl_add_u32 v214, v215, 6, v214\n"
"v_lshl_add_u32 v214, v212, 3, v214\n"
"v_add_u32 v214, s57, v214\n"
"v_and_b32 v215, 1, v213\n"
"v_lshl_add_u32 v10, v215, 5, v214\n"
"v_xor_b32 v215, 1, v215\n"
"v_lshl_add_u32 v11, v215, 5, v214\n"
"v_add_u32 v3, 65536, v10\n"
"v_add_u32 v12, 131072, v10\n"
"v_add_u32 v4, 65536, v11\n"
"v_add_u32 v13, 131072, v11\n"
"v_bfe_u32 v215, v1, 2, 2\n"
"v_xor_b32 v214, 0, v212\n"
"v_lshlrev_b32 v214, 4, v214\n"
"v_lshl_add_u32 v214, v213, 6, v214\n"
"v_lshl_add_u32 v5, v215, 8, v214\n"
"v_lshl_add_u32 v8, v215, 13, v214\n"
"v_xor_b32 v214, 2, v212\n"
"v_lshlrev_b32 v214, 4, v214\n"
"v_lshl_add_u32 v214, v213, 6, v214\n"
"v_lshl_add_u32 v6, v215, 8, v214\n"
"v_lshl_add_u32 v9, v215, 13, v214\n"
"v_mov_b32 v14, s57\n"
"v_add_u32 v14, 158208, v14\n"
"v_mov_b32 v212, 0\n"
"ds_write_b32 v14, v212\n"
"s_lshl_b32 s62, s59, 2\n"
"s_add_u32 s62, s62, s60\n"
"s_lshl_b32 s62, s62, 8\n"
"s_add_u32 s52, %[wqlo], 0\n"
"s_addc_u32 s53, %[wqhi], 0\n"
"s_add_u32 s52, s52, s62\n"
"s_addc_u32 s53, s53, 0\n"
"s_and_b32 s53, s53, 0xffff\n"
"s_mov_b32 s54, 4718592\n"
"s_mov_b32 s55, 0x20000\n"
"s_sub_u32 s54, s54, s62\n"
"s_cmp_eq_u32 s56, 3\n"
"s_cbranch_scc1 L_r33_%=\n"
"s_waitcnt vmcnt(6)\n"
"v_lshlrev_b32 v32, 16, v152\n"
"v_and_b32 v33, 0xffff0000, v152\n"
"v_lshlrev_b32 v34, 16, v153\n"
"v_and_b32 v35, 0xffff0000, v153\n"
"s_waitcnt vmcnt(5)\n"
"v_lshlrev_b32 v36, 16, v154\n"
"v_and_b32 v37, 0xffff0000, v154\n"
"v_lshlrev_b32 v38, 16, v155\n"
"v_and_b32 v39, 0xffff0000, v155\n"
"s_waitcnt vmcnt(4)\n"
"v_lshlrev_b32 v40, 16, v156\n"
"v_and_b32 v41, 0xffff0000, v156\n"
"v_lshlrev_b32 v42, 16, v157\n"
"v_and_b32 v43, 0xffff0000, v157\n"
"s_waitcnt vmcnt(3)\n"
"v_lshlrev_b32 v44, 16, v158\n"
"v_and_b32 v45, 0xffff0000, v158\n"
"v_lshlrev_b32 v46, 16, v159\n"
"v_and_b32 v47, 0xffff0000, v159\n"
"s_waitcnt vmcnt(2)\n"
"v_lshlrev_b32 v48, 16, v160\n"
"v_and_b32 v49, 0xffff0000, v160\n"
"v_lshlrev_b32 v50, 16, v161\n"
"v_and_b32 v51, 0xffff0000, v161\n"
"s_waitcnt vmcnt(1)\n"
"v_lshlrev_b32 v52, 16, v162\n"
"v_and_b32 v53, 0xffff0000, v162\n"
"v_lshlrev_b32 v54, 16, v163\n"
"v_and_b32 v55, 0xffff0000, v163\n"
"aD 56,32,33\n"
"aD 58,34,35\n"
"aD 56,56,36\n"
"aD 58,58,37\n"
"aD 56,56,38\n"
"aD 58,58,39\n"
"aD 56,56,40\n"
"aD 58,58,41\n"
"aD 56,56,42\n"
"aD 58,58,43\n"
"aD 56,56,44\n"
"aD 58,58,45\n"
"aD 56,56,46\n"
"aD 58,58,47\n"
"aD 56,56,48\n"
"aD 58,58,49\n"
"aD 56,56,50\n"
"aD 58,58,51\n"
"aD 56,56,52\n"
"aD 58,58,53\n"
"aD 56,56,54\n"
"aD 58,58,55\n"
"aD 56,56,58\n"
"s_nop 1\n"
"v_add_f32_dpp v56, v56, v56 row_ror:8 row_mask:0xf bank_mask:0xf\n"
"v_mov_b32 v58, v56\n"
"s_nop 1\n"
"v_permlane16_swap_b32 v56, v58\n"
"aD 56,56,58\n"
"v_mov_b32 v58, v56\n"
"s_nop 1\n"
"v_permlane32_swap_b32 v56, v58\n"
"aD 56,56,58\n"
"v_mul_f32 v56, 0x3baaaaab, v56\n"
"v_sub_f32 v68, v32, v56\n"
"v_sub_f32 v69, v33, v56\n"
"v_sub_f32 v70, v34, v56\n"
"v_sub_f32 v71, v35, v56\n"
"v_sub_f32 v72, v36, v56\n"
"v_sub_f32 v73, v37, v56\n"
"v_sub_f32 v74, v38, v56\n"
"v_sub_f32 v75, v39, v56\n"
"v_sub_f32 v76, v40, v56\n"
"v_sub_f32 v77, v41, v56\n"
"v_sub_f32 v78, v42, v56\n"
"v_sub_f32 v79, v43, v56\n"
"v_sub_f32 v80, v44, v56\n"
"v_sub_f32 v81, v45, v56\n"
"v_sub_f32 v82, v46, v56\n"
"v_sub_f32 v83, v47, v56\n"
"v_sub_f32 v84, v48, v56\n"
"v_sub_f32 v85, v49, v56\n"
"v_sub_f32 v86, v50, v56\n"
"v_sub_f32 v87, v51, v56\n"
"v_sub_f32 v88, v52, v56\n"
"v_sub_f32 v89, v53, v56\n"
"v_sub_f32 v90, v54, v56\n"
"v_sub_f32 v91, v55, v56\n"
"mU 57,68,68\n"
"mU 58,69,69\n"
"v_fmac_f32 v57, v70, v70\n"
"v_fmac_f32 v58, v71, v71\n"
"v_fmac_f32 v57, v72, v72\n"
"v_fmac_f32 v58, v73, v73\n"
"v_fmac_f32 v57, v74, v74\n"
"v_fmac_f32 v58, v75, v75\n"
"v_fmac_f32 v57, v76, v76\n"
"v_fmac_f32 v58, v77, v77\n"
"v_fmac_f32 v57, v78, v78\n"
"v_fmac_f32 v58, v79, v79\n"
"v_fmac_f32 v57, v80, v80\n"
"v_fmac_f32 v58, v81, v81\n"
"v_fmac_f32 v57, v82, v82\n"
"v_fmac_f32 v58, v83, v83\n"
"v_fmac_f32 v57, v84, v84\n"
"v_fmac_f32 v58, v85, v85\n"
"v_fmac_f32 v57, v86, v86\n"
"v_fmac_f32 v58, v87, v87\n"
"v_fmac_f32 v57, v88, v88\n"
"v_fmac_f32 v58, v89, v89\n"
"v_fmac_f32 v57, v90, v90\n"
"v_fmac_f32 v58, v91, v91\n"
"aD 57,57,58\n"
"s_nop 1\n"
"v_add_f32_dpp v57, v57, v57 row_ror:8 row_mask:0xf bank_mask:0xf\n"
"v_mov_b32 v58, v57\n"
"s_nop 1\n"
"v_permlane16_swap_b32 v57, v58\n"
"aD 57,57,58\n"
"v_mov_b32 v58, v57\n"
"s_nop 1\n"
"v_permlane32_swap_b32 v57, v58\n"
"aD 57,57,58\n"
"ds_write_b64 v18, v[56:57]\n"
"s_waitcnt lgkmcnt(0)\n"
"s_barrier\n"
"ds_read_b64 v[60:61], v19\n"
"ds_read_b64 v[62:63], v19 offset:64\n"
"ds_read_b64 v[64:65], v19 offset:128\n"
"s_waitcnt lgkmcnt(1)\n"
"aD 56,60,62\n"
"s_waitcnt lgkmcnt(0)\n"
"aD 56,56,64\n"
"v_mul_f32 v56, 0x3eaaaaab, v56\n"
"aD 57,61,63\n"
"aD 57,57,65\n"
"v_sub_f32 v58, v60, v56\n"
"mU 59,58,58\n"
"v_sub_f32 v58, v62, v56\n"
"v_fmac_f32 v59, v58, v58\n"
"v_sub_f32 v58, v64, v56\n"
"v_fmac_f32 v59, v58, v58\n"
"v_mul_f32 v59, 0x3eaaaaab, v59\n"
"s_mov_b32 s62, 0x3ae38e39\n"
"v_fma_f32 v59, v57, s62, v59\n"
"cP 59,59,59\n"
"v_and_b32 v59, 0xffff0000, v59\n"
"v_add_f32 v59, 0x3727c5ac, v59\n"
"cP 59,59,59\n"
"v_and_b32 v59, 0xffff0000, v59\n"
"v_sqrt_f32 v59, v59\n"
"s_nop 0\n"
"cP 59,59,59\n"
"v_and_b32 v59, 0xffff0000, v59\n"
"v_rcp_f32 v57, v59\n"
"cP 56,56,56\n"
"v_and_b32 v56, 0xffff0000, v56\n"
"v_sub_f32 v32, v32, v56\n"
"v_sub_f32 v33, v33, v56\n"
"v_sub_f32 v34, v34, v56\n"
"v_sub_f32 v35, v35, v56\n"
"v_sub_f32 v36, v36, v56\n"
"v_sub_f32 v37, v37, v56\n"
"v_sub_f32 v38, v38, v56\n"
"v_sub_f32 v39, v39, v56\n"
"cP 68,32,33\n"
"v_lshlrev_b32 v32, 16, v68\n"
"v_and_b32 v33, 0xffff0000, v68\n"
"cP 69,34,35\n"
"v_lshlrev_b32 v34, 16, v69\n"
"v_and_b32 v35, 0xffff0000, v69\n"
"cP 70,36,37\n"
"v_lshlrev_b32 v36, 16, v70\n"
"v_and_b32 v37, 0xffff0000, v70\n"
"cP 71,38,39\n"
"v_lshlrev_b32 v38, 16, v71\n"
"v_and_b32 v39, 0xffff0000, v71\n"
"mU 32,32,57\n"
"mU 33,33,57\n"
"mU 34,34,57\n"
"mU 35,35,57\n"
"mU 36,36,57\n"
"mU 37,37,57\n"
"mU 38,38,57\n"
"mU 39,39,57\n"
"cP 68,32,33\n"
"v_lshlrev_b32 v32, 16, v68\n"
"v_and_b32 v33, 0xffff0000, v68\n"
"cP 69,34,35\n"
"v_lshlrev_b32 v34, 16, v69\n"
"v_and_b32 v35, 0xffff0000, v69\n"
"cP 70,36,37\n"
"v_lshlrev_b32 v36, 16, v70\n"
"v_and_b32 v37, 0xffff0000, v70\n"
"cP 71,38,39\n"
"v_lshlrev_b32 v38, 16, v71\n"
"v_and_b32 v39, 0xffff0000, v71\n"
"v_sub_f32 v40, v40, v56\n"
"v_sub_f32 v41, v41, v56\n"
"v_sub_f32 v42, v42, v56\n"
"v_sub_f32 v43, v43, v56\n"
"v_sub_f32 v44, v44, v56\n"
"v_sub_f32 v45, v45, v56\n"
"v_sub_f32 v46, v46, v56\n"
"v_sub_f32 v47, v47, v56\n"
"cP 76,40,41\n"
"v_lshlrev_b32 v40, 16, v76\n"
"v_and_b32 v41, 0xffff0000, v76\n"
"cP 77,42,43\n"
"v_lshlrev_b32 v42, 16, v77\n"
"v_and_b32 v43, 0xffff0000, v77\n"
"cP 78,44,45\n"
"v_lshlrev_b32 v44, 16, v78\n"
"v_and_b32 v45, 0xffff0000, v78\n"
"cP 79,46,47\n"
"v_lshlrev_b32 v46, 16, v79\n"
"v_and_b32 v47, 0xffff0000, v79\n"
"mU 40,40,57\n"
"mU 41,41,57\n"
"mU 42,42,57\n"
"mU 43,43,57\n"
"mU 44,44,57\n"
"mU 45,45,57\n"
"mU 46,46,57\n"
"mU 47,47,57\n"
"cP 76,40,41\n"
"v_lshlrev_b32 v40, 16, v76\n"
"v_and_b32 v41, 0xffff0000, v76\n"
"cP 77,42,43\n"
"v_lshlrev_b32 v42, 16, v77\n"
"v_and_b32 v43, 0xffff0000, v77\n"
"cP 78,44,45\n"
"v_lshlrev_b32 v44, 16, v78\n"
"v_and_b32 v45, 0xffff0000, v78\n"
"cP 79,46,47\n"
"v_lshlrev_b32 v46, 16, v79\n"
"v_and_b32 v47, 0xffff0000, v79\n"
"v_sub_f32 v48, v48, v56\n"
"v_sub_f32 v49, v49, v56\n"
"v_sub_f32 v50, v50, v56\n"
"v_sub_f32 v51, v51, v56\n"
"v_sub_f32 v52, v52, v56\n"
"v_sub_f32 v53, v53, v56\n"
"v_sub_f32 v54, v54, v56\n"
"v_sub_f32 v55, v55, v56\n"
"cP 84,48,49\n"
"v_lshlrev_b32 v48, 16, v84\n"
"v_and_b32 v49, 0xffff0000, v84\n"
"cP 85,50,51\n"
"v_lshlrev_b32 v50, 16, v85\n"
"v_and_b32 v51, 0xffff0000, v85\n"
"cP 86,52,53\n"
"v_lshlrev_b32 v52, 16, v86\n"
"v_and_b32 v53, 0xffff0000, v86\n"
"cP 87,54,55\n"
"v_lshlrev_b32 v54, 16, v87\n"
"v_and_b32 v55, 0xffff0000, v87\n"
"mU 48,48,57\n"
"mU 49,49,57\n"
"mU 50,50,57\n"
"mU 51,51,57\n"
"mU 52,52,57\n"
"mU 53,53,57\n"
"mU 54,54,57\n"
"mU 55,55,57\n"
"cP 84,48,49\n"
"v_lshlrev_b32 v48, 16, v84\n"
"v_and_b32 v49, 0xffff0000, v84\n"
"cP 85,50,51\n"
"v_lshlrev_b32 v50, 16, v85\n"
"v_and_b32 v51, 0xffff0000, v85\n"
"cP 86,52,53\n"
"v_lshlrev_b32 v52, 16, v86\n"
"v_and_b32 v53, 0xffff0000, v86\n"
"cP 87,54,55\n"
"v_lshlrev_b32 v54, 16, v87\n"
"v_and_b32 v55, 0xffff0000, v87\n"
"s_add_u32 s66, %[wslo], 77824\n"
"s_addc_u32 s67, %[wshi], 0\n"
"s_cmp_eq_u32 s56, 0\n"
"s_cbranch_scc0 L_wd4_%=\n"
"v_min_u32 v236, 35, v1\n"
"v_lshlrev_b32 v236, 4, v236\n"
"s_mov_b32 s63, 131072\n"
"L_wp4_%=:\n"
"global_load_dwordx4 v[232:235], v236, s[66:67] sc1\n"
"s_waitcnt vmcnt(0)\n"
"v_cmp_eq_u64 vcc, s[98:99], v[232:233]\n"
"v_cmp_eq_u64 s[64:65], s[98:99], v[234:235]\n"
"s_nop 1\n"
"s_and_b64 vcc, vcc, s[64:65]\n"
"s_cmp_eq_u64 vcc, -1\n"
"s_cbranch_scc1 L_wd4_%=\n"
"s_sleep 2\n"
"s_sub_u32 s63, s63, 1\n"
"s_cmp_lg_u32 s63, 0\n"
"s_cbranch_scc1 L_wp4_%=\n"
"L_wd4_%=:\n"
"s_barrier\n"
"s_mov_b32 s62, 0\n"
"buffer_load_dwordx4 v[164:167], v17, s[40:43], s62 offen sc1\n"
"buffer_load_dwordx4 v[168:171], v17, s[40:43], s62 offen offset:16 sc1\n"
"buffer_load_dwordx4 v[172:175], v17, s[40:43], s62 offen offset:768 sc1\n"
"buffer_load_dwordx4 v[176:179], v17, s[40:43], s62 offen offset:784 sc1\n"
"buffer_load_dwordx4 v[180:183], v17, s[40:43], s62 offen offset:1536 sc1\n"
"buffer_load_dwordx4 v[184:187], v17, s[40:43], s62 offen offset:1552 sc1\n"
"s_mov_b32 s62, 2304\n"
"buffer_load_dwordx4 v[188:191], v17, s[40:43], s62 offen sc1\n"
"buffer_load_dwordx4 v[192:195], v17, s[40:43], s62 offen offset:16 sc1\n"
"buffer_load_dwordx4 v[196:199], v17, s[40:43], s62 offen offset:768 sc1\n"
"buffer_load_dwordx4 v[200:203], v17, s[40:43], s62 offen offset:784 sc1\n"
"buffer_load_dwordx4 v[204:207], v17, s[40:43], s62 offen offset:1536 sc1\n"
"buffer_load_dwordx4 v[208:211], v17, s[40:43], s62 offen offset:1552 sc1\n"
"s_waitcnt vmcnt(5)\n"
"mU 32,32,188\n"
"mU 33,33,189\n"
"mU 34,34,190\n"
"mU 35,35,191\n"
"s_waitcnt vmcnt(4)\n"
"mU 36,36,192\n"
"mU 37,37,193\n"
"mU 38,38,194\n"
"mU 39,39,195\n"
"cP 72,32,33\n"
"v_lshlrev_b32 v32, 16, v72\n"
"v_and_b32 v33, 0xffff0000, v72\n"
"cP 73,34,35\n"
"v_lshlrev_b32 v34, 16, v73\n"
"v_and_b32 v35, 0xffff0000, v73\n"
"cP 74,36,37\n"
"v_lshlrev_b32 v36, 16, v74\n"
"v_and_b32 v37, 0xffff0000, v74\n"
"cP 75,38,39\n"
"v_lshlrev_b32 v38, 16, v75\n"
"v_and_b32 v39, 0xffff0000, v75\n"
"aD 32,32,164\n"
"aD 33,33,165\n"
"aD 34,34,166\n"
"aD 35,35,167\n"
"aD 36,36,168\n"
"aD 37,37,169\n"
"aD 38,38,170\n"
"aD 39,39,171\n"
"cP 72,32,33\n"
"cP 73,34,35\n"
"cP 74,36,37\n"
"cP 75,38,39\n"
"s_add_u32 s62, s93, 0\n"
"buffer_store_dwordx4 v[72:75], v16, s[48:51], s62 offen sc1\n"
"s_waitcnt vmcnt(4)\n"
"mU 40,40,196\n"
"mU 41,41,197\n"
"mU 42,42,198\n"
"mU 43,43,199\n"
"s_waitcnt vmcnt(3)\n"
"mU 44,44,200\n"
"mU 45,45,201\n"
"mU 46,46,202\n"
"mU 47,47,203\n"
"cP 80,40,41\n"
"v_lshlrev_b32 v40, 16, v80\n"
"v_and_b32 v41, 0xffff0000, v80\n"
"cP 81,42,43\n"
"v_lshlrev_b32 v42, 16, v81\n"
"v_and_b32 v43, 0xffff0000, v81\n"
"cP 82,44,45\n"
"v_lshlrev_b32 v44, 16, v82\n"
"v_and_b32 v45, 0xffff0000, v82\n"
"cP 83,46,47\n"
"v_lshlrev_b32 v46, 16, v83\n"
"v_and_b32 v47, 0xffff0000, v83\n"
"aD 40,40,172\n"
"aD 41,41,173\n"
"aD 42,42,174\n"
"aD 43,43,175\n"
"aD 44,44,176\n"
"aD 45,45,177\n"
"aD 46,46,178\n"
"aD 47,47,179\n"
"cP 80,40,41\n"
"cP 81,42,43\n"
"cP 82,44,45\n"
"cP 83,46,47\n"
"s_add_u32 s62, s93, 24576\n"
"buffer_store_dwordx4 v[80:83], v16, s[48:51], s62 offen sc1\n"
"s_waitcnt vmcnt(3)\n"
"mU 48,48,204\n"
"mU 49,49,205\n"
"mU 50,50,206\n"
"mU 51,51,207\n"
"s_waitcnt vmcnt(2)\n"
"mU 52,52,208\n"
"mU 53,53,209\n"
"mU 54,54,210\n"
"mU 55,55,211\n"
"cP 88,48,49\n"
"v_lshlrev_b32 v48, 16, v88\n"
"v_and_b32 v49, 0xffff0000, v88\n"
"cP 89,50,51\n"
"v_lshlrev_b32 v50, 16, v89\n"
"v_and_b32 v51, 0xffff0000, v89\n"
"cP 90,52,53\n"
"v_lshlrev_b32 v52, 16, v90\n"
"v_and_b32 v53, 0xffff0000, v90\n"
"cP 91,54,55\n"
"v_lshlrev_b32 v54, 16, v91\n"
"v_and_b32 v55, 0xffff0000, v91\n"
"aD 48,48,180\n"
"aD 49,49,181\n"
"aD 50,50,182\n"
"aD 51,51,183\n"
"aD 52,52,184\n"
"aD 53,53,185\n"
"aD 54,54,186\n"
"aD 55,55,187\n"
"cP 88,48,49\n"
"cP 89,50,51\n"
"cP 90,52,53\n"
"cP 91,54,55\n"
"s_add_u32 s62, s93, 49152\n"
"buffer_store_dwordx4 v[88:91], v16, s[48:51], s62 offen sc1\n"
"s_waitcnt vmcnt(0) lgkmcnt(0)\n"
"s_branch L_rj3_%=\n"
"L_r33_%=:\n"
"s_add_u32 m0, s57, 0\n"
"s_mov_b32 s63, 0\n"
"buffer_load_dwordx4 v8, s[52:55], s63 offen lds\n"
"s_add_u32 m0, s57, 1024\n"
"s_mov_b32 s63, 32768\n"
"buffer_load_dwordx4 v9, s[52:55], s63 offen lds\n"
"s_add_u32 m0, s57, 2048\n"
"s_mov_b32 s63, 65536\n"
"buffer_load_dwordx4 v8, s[52:55], s63 offen lds\n"
"s_add_u32 m0, s57, 3072\n"
"s_mov_b32 s63, 98304\n"
"buffer_load_dwordx4 v9, s[52:55], s63 offen lds\n"
"s_add_u32 m0, s57, 4096\n"
"s_mov_b32 s63, 131072\n"
"buffer_load_dwordx4 v8, s[52:55], s63 offen lds\n"
"s_add_u32 m0, s57, 5120\n"
"s_mov_b32 s63, 163840\n"
"buffer_load_dwordx4 v9, s[52:55], s63 offen lds\n"
"s_add_u32 m0, s57, 6144\n"
"s_mov_b32 s63, 196608\n"
"buffer_load_dwordx4 v8, s[52:55], s63 offen lds\n"
"s_add_u32 m0, s57, 7168\n"
"s_mov_b32 s63, 229376\n"
"buffer_load_dwordx4 v9, s[52:55], s63 offen lds\n"
"s_add_u32 m0, s57, 8192\n"
"s_mov_b32 s63, 262144\n"
"buffer_load_dwordx4 v8, s[52:55], s63 offen lds\n"
"s_add_u32 m0, s57, 9216\n"
"s_mov_b32 s63, 294912\n"
"buffer_load_dwordx4 v9, s[52:55], s63 offen lds\n"
"s_add_u32 m0, s57, 10240\n"
"s_mov_b32 s63, 327680\n"
"buffer_load_dwordx4 v8, s[52:55], s63 offen lds\n"
"s_add_u32 m0, s57, 11264\n"
"s_mov_b32 s63, 360448\n"
"buffer_load_dwordx4 v9, s[52:55], s63 offen lds\n"
"s_add_u32 m0, s57, 12288\n"
"s_mov_b32 s63, 393216\n"
"buffer_load_dwordx4 v8, s[52:55], s63 offen lds\n"
"s_add_u32 m0, s57, 13312\n"
"s_mov_b32 s63, 425984\n"
"buffer_load_dwordx4 v9, s[52:55], s63 offen lds\n"
"s_add_u32 m0, s57, 14336\n"
"s_mov_b32 s63, 458752\n"
"buffer_load_dwordx4 v8, s[52:55], s63 offen lds\n"
"s_add_u32 m0, s57, 15360\n"
"s_mov_b32 s63, 491520\n"
"buffer_load_dwordx4 v9, s[52:55], s63 offen lds\n"
"s_add_u32 m0, s57, 65536\n"
"s_mov_b32 s63, 524288\n"
"buffer_load_dwordx4 v8, s[52:55], s63 offen lds\n"
"s_add_u32 m0, s57, 66560\n"
"s_mov_b32 s63, 557056\n"
"buffer_load_dwordx4 v9, s[52:55], s63 offen lds\n"
"s_add_u32 m0, s57, 67584\n"
"s_mov_b32 s63, 589824\n"
"buffer_load_dwordx4 v8, s[52:55], s63 offen lds\n"
"s_add_u32 m0, s57, 68608\n"
"s_mov_b32 s63, 622592\n"
"buffer_load_dwordx4 v9, s[52:55], s63 offen lds\n"
"s_add_u32 m0, s57, 69632\n"
"s_mov_b32 s63, 655360\n"
"buffer_load_dwordx4 v8, s[52:55], s63 offen lds\n"
"s_add_u32 m0, s57, 70656\n"
"s_mov_b32 s63, 688128\n"
"buffer_load_dwordx4 v9, s[52:55], s63 offen lds\n"
"s_add_u32 m0, s57, 71680\n"
"s_mov_b32 s63, 720896\n"
"buffer_load_dwordx4 v8, s[52:55], s63 offen lds\n"
"s_add_u32 m0, s57, 72704\n"
"s_mov_b32 s63, 753664\n"
"buffer_load_dwordx4 v9, s[52:55], s63 offen lds\n"
"s_add_u32 m0, s57, 73728\n"
"s_mov_b32 s63, 786432\n"
"buffer_load_dwordx4 v8, s[52:55], s63 offen lds\n"
"s_add_u32 m0, s57, 74752\n"
"s_mov_b32 s63, 819200\n"
"buffer_load_dwordx4 v9, s[52:55], s63 offen lds\n"
"s_add_u32 m0, s57, 75776\n"
"s_mov_b32 s63, 851968\n"
"buffer_load_dwordx4 v8, s[52:55], s63 offen lds\n"
"s_add_u32 m0, s57, 76800\n"
"s_mov_b32 s63, 884736\n"
"buffer_load_dwordx4 v9, s[52:55], s63 offen lds\n"
"s_add_u32 m0, s57, 77824\n"
"s_mov_b32 s63, 917504\n"
"buffer_load_dwordx4 v8, s[52:55], s63 offen lds\n"
"s_add_u32 m0, s57, 78848\n"
"s_mov_b32 s63, 950272\n"
"buffer_load_dwordx4 v9, s[52:55], s63 offen lds\n"
"s_add_u32 m0, s57, 79872\n"
"s_mov_b32 s63, 983040\n"
"buffer_load_dwordx4 v8, s[52:55], s63 offen lds\n"
"s_add_u32 m0, s57, 80896\n"
"s_mov_b32 s63, 1015808\n"
"buffer_load_dwordx4 v9, s[52:55], s63 offen lds\n"
"s_add_u32 m0, s57, 16384\n"
"s_mov_b32 s63, 1048576\n"
"buffer_load_dwordx4 v8, s[52:55], s63 offen lds\n"
"s_add_u32 m0, s57, 17408\n"
"s_mov_b32 s63, 1081344\n"
"buffer_load_dwordx4 v9, s[52:55], s63 offen lds\n"
"s_add_u32 m0, s57, 18432\n"
"s_mov_b32 s63, 1114112\n"
"buffer_load_dwordx4 v8, s[52:55], s63 offen lds\n"
"s_add_u32 m0, s57, 19456\n"
"s_mov_b32 s63, 1146880\n"
"buffer_load_dwordx4 v9, s[52:55], s63 offen lds\n"
"s_add_u32 m0, s57, 20480\n"
"s_mov_b32 s63, 1179648\n"
"buffer_load_dwordx4 v8, s[52:55], s63 offen lds\n"
"s_add_u32 m0, s57, 21504\n"
"s_mov_b32 s63, 1212416\n"
"buffer_load_dwordx4 v9, s[52:55], s63 offen lds\n"
"s_add_u32 m0, s57, 22528\n"
"s_mov_b32 s63, 1245184\n"
"buffer_load_dwordx4 v8, s[52:55], s63 offen lds\n"
"s_add_u32 m0, s57, 23552\n"
"s_mov_b32 s63, 1277952\n"
"buffer_load_dwordx4 v9, s[52:55], s63 offen lds\n"
"s_add_u32 m0, s57, 24576\n"
"s_mov_b32 s63, 1310720\n"
"buffer_load_dwordx4 v8, s[52:55], s63 offen lds\n"
"s_add_u32 m0, s57, 25600\n"
"s_mov_b32 s63, 1343488\n"
"buffer_load_dwordx4 v9, s[52:55], s63 offen lds\n"
"s_add_u32 m0, s57, 26624\n"
"s_mov_b32 s63, 1376256\n"
"buffer_load_dwordx4 v8, s[52:55], s63 offen lds\n"
"s_add_u32 m0, s57, 27648\n"
"s_mov_b32 s63, 1409024\n"
"buffer_load_dwordx4 v9, s[52:55], s63 offen lds\n"
"s_add_u32 m0, s57, 28672\n"
"s_mov_b32 s63, 1441792\n"
"buffer_load_dwordx4 v8, s[52:55], s63 offen lds\n"
"s_add_u32 m0, s57, 29696\n"
"s_mov_b32 s63, 1474560\n"
"buffer_load_dwordx4 v9, s[52:55], s63 offen lds\n"
"s_add_u32 m0, s57, 30720\n"
"s_mov_b32 s63, 1507328\n"
"buffer_load_dwordx4 v8, s[52:55], s63 offen lds\n"
"s_add_u32 m0, s57, 31744\n"
"s_mov_b32 s63, 1540096\n"
"buffer_load_dwordx4 v9, s[52:55], s63 offen lds\n"
"s_barrier\n"
"s_add_u32 m0, s57, 81920\n"
"s_mov_b32 s63, 1572864\n"
"buffer_load_dwordx4 v8, s[52:55], s63 offen lds\n"
"s_add_u32 m0, s57, 82944\n"
"s_mov_b32 s63, 1605632\n"
"buffer_load_dwordx4 v9, s[52:55], s63 offen lds\n"
"s_add_u32 m0, s57, 83968\n"
"s_mov_b32 s63, 1638400\n"
"buffer_load_dwordx4 v8, s[52:55], s63 offen lds\n"
"s_add_u32 m0, s57, 84992\n"
"s_mov_b32 s63, 1671168\n"
"buffer_load_dwordx4 v9, s[52:55], s63 offen lds\n"
"s_add_u32 m0, s57, 86016\n"
"s_mov_b32 s63, 1703936\n"
"buffer_load_dwordx4 v8, s[52:55], s63 offen lds\n"
"s_add_u32 m0, s57, 87040\n"
"s_mov_b32 s63, 1736704\n"
"buffer_load_dwordx4 v9, s[52:55], s63 offen lds\n"
"s_add_u32 m0, s57, 88064\n"
"s_mov_b32 s63, 1769472\n"
"buffer_load_dwordx4 v8, s[52:55], s63 offen lds\n"
"s_add_u32 m0, s57, 89088\n"
"s_mov_b32 s63, 1802240\n"
"s_waitcnt vmcnt(46)\n"
"buffer_load_dwordx4 v9, s[52:55], s63 offen lds\n"
"s_add_u32 m0, s57, 90112\n"
"s_mov_b32 s63, 1835008\n"
"buffer_load_dwordx4 v8, s[52:55], s63 offen lds\n"
"s_add_u32 m0, s57, 91136\n"
"s_mov_b32 s63, 1867776\n"
"buffer_load_dwordx4 v9, s[52:55], s63 offen lds\n"
"s_add_u32 m0, s57, 92160\n"
"s_mov_b32 s63, 1900544\n"
"buffer_load_dwordx4 v8, s[52:55], s63 offen lds\n"
"s_add_u32 m0, s57, 93184\n"
"s_mov_b32 s63, 1933312\n"
"buffer_load_dwordx4 v9, s[52:55], s63 offen lds\n"
"s_add_u32 m0, s57, 94208\n"
"s_mov_b32 s63, 1966080\n"
"buffer_load_dwordx4 v8, s[52:55], s63 offen lds\n"
"s_add_u32 m0, s57, 95232\n"
"s_mov_b32 s63, 1998848\n"
"buffer_load_dwordx4 v9, s[52:55], s63 offen lds\n"
"s_add_u32 m0, s57, 96256\n"
"s_mov_b32 s63, 2031616\n"
"buffer_load_dwordx4 v8, s[52:55], s63 offen lds\n"
"s_add_u32 m0, s57, 97280\n"
"s_mov_b32 s63, 2064384\n"
"buffer_load_dwordx4 v9, s[52:55], s63 offen lds\n"
"s_add_u32 m0, s57, 32768\n"
"s_mov_b32 s63, 2097152\n"
"buffer_load_dwordx4 v8, s[52:55], s63 offen lds\n"
"s_add_u32 m0, s57, 33792\n"
"s_mov_b32 s63, 2129920\n"
"buffer_load_dwordx4 v9, s[52:55], s63 offen lds\n"
"s_add_u32 m0, s57, 34816\n"
"s_mov_b32 s63, 2162688\n"
"buffer_load_dwordx4 v8, s[52:55], s63 offen lds\n"
"s_add_u32 m0, s57, 35840\n"
"s_mov_b32 s63, 2195456\n"
"buffer_load_dwordx4 v9, s[52:55], s63 offen lds\n"
"s_add_u32 m0, s57, 36864\n"
"s_mov_b32 s63, 2228224\n"
"buffer_load_dwordx4 v8, s[52:55], s63 offen lds\n"
"s_add_u32 m0, s57, 37888\n"
"s_mov_b32 s63, 2260992\n"
"buffer_load_dwordx4 v9, s[52:55], s63 offen lds\n"
"s_add_u32 m0, s57, 38912\n"
"s_mov_b32 s63, 2293760\n"
"buffer_load_dwordx4 v8, s[52:55], s63 offen lds\n"
"s_add_u32 m0, s57, 39936\n"
"s_mov_b32 s63, 2326528\n"
"s_waitcnt vmcnt(46)\n"
"buffer_load_dwordx4 v9, s[52:55], s63 offen lds\n"
"s_add_u32 m0, s57, 40960\n"
"s_mov_b32 s63, 2359296\n"
"buffer_load_dwordx4 v8, s[52:55], s63 offen lds\n"
"s_add_u32 m0, s57, 41984\n"
"s_mov_b32 s63, 2392064\n"
"buffer_load_dwordx4 v9, s[52:55], s63 offen lds\n"
"s_add_u32 m0, s57, 43008\n"
"s_mov_b32 s63, 2424832\n"
"buffer_load_dwordx4 v8, s[52:55], s63 offen lds\n"
"s_add_u32 m0, s57, 44032\n"
"s_mov_b32 s63, 2457600\n"
"buffer_load_dwordx4 v9, s[52:55], s63 offen lds\n"
"s_add_u32 m0, s57, 45056\n"
"s_mov_b32 s63, 2490368\n"
"buffer_load_dwordx4 v8, s[52:55], s63 offen lds\n"
"s_add_u32 m0, s57, 46080\n"
"s_mov_b32 s63, 2523136\n"
"buffer_load_dwordx4 v9, s[52:55], s63 offen lds\n"
"s_add_u32 m0, s57, 47104\n"
"s_mov_b32 s63, 2555904\n"
"buffer_load_dwordx4 v8, s[52:55], s63 offen lds\n"
"s_add_u32 m0, s57, 48128\n"
"s_mov_b32 s63, 2588672\n"
"buffer_load_dwordx4 v9, s[52:55], s63 offen lds\n"
"s_add_u32 m0, s57, 98304\n"
"s_mov_b32 s63, 2621440\n"
"buffer_load_dwordx4 v8, s[52:55], s63 offen lds\n"
"s_add_u32 m0, s57, 99328\n"
"s_mov_b32 s63, 2654208\n"
"buffer_load_dwordx4 v9, s[52:55], s63 offen lds\n"
"s_add_u32 m0, s57, 100352\n"
"s_mov_b32 s63, 2686976\n"
"buffer_load_dwordx4 v8, s[52:55], s63 offen lds\n"
"s_add_u32 m0, s57, 101376\n"
"s_mov_b32 s63, 2719744\n"
"buffer_load_dwordx4 v9, s[52:55], s63 offen lds\n"
"s_add_u32 m0, s57, 102400\n"
"s_mov_b32 s63, 2752512\n"
"buffer_load_dwordx4 v8, s[52:55], s63 offen lds\n"
"s_add_u32 m0, s57, 103424\n"
"s_mov_b32 s63, 2785280\n"
"buffer_load_dwordx4 v9, s[52:55], s63 offen lds\n"
"s_add_u32 m0, s57, 104448\n"
"s_mov_b32 s63, 2818048\n"
"buffer_load_dwordx4 v8, s[52:55], s63 offen lds\n"
"s_add_u32 m0, s57, 105472\n"
"s_mov_b32 s63, 2850816\n"
"s_waitcnt vmcnt(46)\n"
"buffer_load_dwordx4 v9, s[52:55], s63 offen lds\n"
"s_add_u32 m0, s57, 106496\n"
"s_mov_b32 s63, 2883584\n"
"buffer_load_dwordx4 v8, s[52:55], s63 offen lds\n"
"s_add_u32 m0, s57, 107520\n"
"s_mov_b32 s63, 2916352\n"
"buffer_load_dwordx4 v9, s[52:55], s63 offen lds\n"
"s_add_u32 m0, s57, 108544\n"
"s_mov_b32 s63, 2949120\n"
"buffer_load_dwordx4 v8, s[52:55], s63 offen lds\n"
"s_add_u32 m0, s57, 109568\n"
"s_mov_b32 s63, 2981888\n"
"buffer_load_dwordx4 v9, s[52:55], s63 offen lds\n"
"s_add_u32 m0, s57, 110592\n"
"s_mov_b32 s63, 3014656\n"
"buffer_load_dwordx4 v8, s[52:55], s63 offen lds\n"
"s_add_u32 m0, s57, 111616\n"
"s_mov_b32 s63, 3047424\n"
"buffer_load_dwordx4 v9, s[52:55], s63 offen lds\n"
"s_add_u32 m0, s57, 112640\n"
"s_mov_b32 s63, 3080192\n"
"buffer_load_dwordx4 v8, s[52:55], s63 offen lds\n"
"s_add_u32 m0, s57, 113664\n"
"s_mov_b32 s63, 3112960\n"
"buffer_load_dwordx4 v9, s[52:55], s63 offen lds\n"
"s_barrier\n"
"L_rj3_%=:\n"
"s_nop 11\n"
"s_lshl_b32 s62, s58, 8\n"
"s_add_u32 s62, s62, 65536\n"
"s_add_u32 s66, %[wslo], s62\n"
"s_addc_u32 s67, %[wshi], 0\n"
"s_barrier\n"
"s_cmp_eq_u32 s56, 0\n"
"s_cbranch_scc0 L_sg5_%=\n"
"s_lshl_b32 s62, s59, 2\n"
"s_add_u32 s62, s62, s60\n"
"s_lshl_b32 s62, s62, 3\n"
"v_mov_b32 v234, s62\n"
"v_mov_b32 v232, s98\n"
"v_mov_b32 v233, s99\n"
"s_mov_b64 exec, 1\n"
"global_store_dwordx2 v234, v[232:233], s[66:67] sc1\n"
"s_mov_b64 exec, -1\n"
"L_sg5_%=:\n"
"s_lshl_b32 s64, s56, 17\n"
"s_add_u32 m0, s73, 49152\n"
"s_add_u32 s63, s64, 3145728\n"
"buffer_load_dwordx4 v8, s[52:55], s63 offen lds\n"
"s_add_u32 m0, s73, 50176\n"
"s_add_u32 s63, s64, 3178496\n"
"buffer_load_dwordx4 v9, s[52:55], s63 offen lds\n"
"s_add_u32 m0, s73, 51200\n"
"s_add_u32 s63, s64, 3211264\n"
"buffer_load_dwordx4 v8, s[52:55], s63 offen lds\n"
"s_add_u32 m0, s73, 52224\n"
"s_add_u32 s63, s64, 3244032\n"
"buffer_load_dwordx4 v9, s[52:55], s63 offen lds\n"
"s_add_u32 m0, s73, 114688\n"
"s_add_u32 s63, s64, 3670016\n"
"buffer_load_dwordx4 v8, s[52:55], s63 offen lds\n"
"s_add_u32 m0, s73, 115712\n"
"s_add_u32 s63, s64, 3702784\n"
"buffer_load_dwordx4 v9, s[52:55], s63 offen lds\n"
"s_add_u32 m0, s73, 116736\n"
"s_add_u32 s63, s64, 3735552\n"
"buffer_load_dwordx4 v8, s[52:55], s63 offen lds\n"
"s_add_u32 m0, s73, 117760\n"
"s_add_u32 s63, s64, 3768320\n"
"s_waitcnt vmcnt(46)\n"
"buffer_load_dwordx4 v9, s[52:55], s63 offen lds\n"
"s_add_u32 m0, s73, 131072\n"
"s_add_u32 s63, s64, 4194304\n"
"buffer_load_dwordx4 v8, s[52:55], s63 offen lds\n"
"s_add_u32 m0, s73, 132096\n"
"s_add_u32 s63, s64, 4227072\n"
"buffer_load_dwordx4 v9, s[52:55], s63 offen lds\n"
"s_add_u32 m0, s73, 133120\n"
"s_add_u32 s63, s64, 4259840\n"
"buffer_load_dwordx4 v8, s[52:55], s63 offen lds\n"
"s_add_u32 m0, s73, 134144\n"
"s_add_u32 s63, s64, 4292608\n"
"buffer_load_dwordx4 v9, s[52:55], s63 offen lds\n"
"s_cmp_eq_u32 s56, 0\n"
"s_cselect_b32 s88, 1, 0\n"
"s_lshl_b32 s87, s56, 10\n"
"s_add_u32 s87, s87, s57\n"
"s_add_u32 s62, s57, 156672\n"
"s_cmp_eq_u32 s56, 0\n"
"s_cselect_b32 s89, s57, s62\n"
"s_lshl_b32 s62, s56, 6\n"
"s_add_u32 s62, s62, 0\n"
"v_add_u32 v212, s62, v1\n"
"v_mul_u32_u24 v213, 911, v212\n"
"v_lshrrev_b32 v213, 16, v213\n"
"v_mul_u32_u24 v214, 72, v213\n"
"v_sub_u32 v214, v212, v214\n"
"v_bfe_u32 v215, v214, 2, 2\n"
"v_and_b32 v216, 1, v213\n"
"v_lshlrev_b32 v216, 1, v216\n"
"v_and_b32 v217, 3, v214\n"
"v_xor_b32 v216, v216, v217\n"
"v_lshlrev_b32 v216, 4, v216\n"
"v_lshrrev_b32 v217, 4, v214\n"
"v_lshl_add_u32 v216, v217, 6, v216\n"
"v_cmp_gt_u32 vcc, 64, v214\n"
"v_bfe_u32 v217, v214, 1, 2\n"
"s_nop 0\n"
"v_cndmask_b32 v215, v217, v215, vcc\n"
"v_and_b32 v217, 1, v214\n"
"v_lshlrev_b32 v217, 4, v217\n"
"v_add_u32 v217, 256, v217\n"
"v_cndmask_b32 v216, v217, v216, vcc\n"
"v_lshl_add_u32 v252, v213, 2, v215\n"
"v_mov_b32 v255, v216\n"
"s_lshl_b32 s62, s56, 6\n"
"s_add_u32 s62, s62, 256\n"
"v_add_u32 v212, s62, v1\n"
"v_mul_u32_u24 v213, 911, v212\n"
"v_lshrrev_b32 v213, 16, v213\n"
"v_mul_u32_u24 v214, 72, v213\n"
"v_sub_u32 v214, v212, v214\n"
"v_bfe_u32 v215, v214, 2, 2\n"
"v_and_b32 v216, 1, v213\n"
"v_lshlrev_b32 v216, 1, v216\n"
"v_and_b32 v217, 3, v214\n"
"v_xor_b32 v216, v216, v217\n"
"v_lshlrev_b32 v216, 4, v216\n"
"v_lshrrev_b32 v217, 4, v214\n"
"v_lshl_add_u32 v216, v217, 6, v216\n"
"v_cmp_gt_u32 vcc, 64, v214\n"
"v_bfe_u32 v217, v214, 1, 2\n"
"s_nop 0\n"
"v_cndmask_b32 v215, v217, v215, vcc\n"
"v_and_b32 v217, 1, v214\n"
"v_lshlrev_b32 v217, 4, v217\n"
"v_add_u32 v217, 256, v217\n"
"v_cndmask_b32 v216, v217, v216, vcc\n"
"v_lshl_add_u32 v253, v213, 2, v215\n"
"v_lshl_add_u32 v255, v216, 10, v255\n"
"v_add_u32 v212, 512, v1\n"
"v_mul_u32_u24 v213, 911, v212\n"
"v_lshrrev_b32 v213, 16, v213\n"
"v_mul_u32_u24 v214, 72, v213\n"
"v_sub_u32 v214, v212, v214\n"
"v_bfe_u32 v215, v214, 2, 2\n"
"v_and_b32 v216, 1, v213\n"
"v_lshlrev_b32 v216, 1, v216\n"
"v_and_b32 v217, 3, v214\n"
"v_xor_b32 v216, v216, v217\n"
"v_lshlrev_b32 v216, 4, v216\n"
"v_lshrrev_b32 v217, 4, v214\n"
"v_lshl_add_u32 v216, v217, 6, v216\n"
"v_cmp_gt_u32 vcc, 64, v214\n"
"v_bfe_u32 v217, v214, 1, 2\n"
"s_nop 0\n"
"v_cndmask_b32 v215, v217, v215, vcc\n"
"v_and_b32 v217, 1, v214\n"
"v_lshlrev_b32 v217, 4, v217\n"
"v_add_u32 v217, 256, v217\n"
"v_cndmask_b32 v216, v217, v216, vcc\n"
"v_lshl_add_u32 v254, v213, 2, v215\n"
"v_lshl_add_u32 v255, v216, 20, v255\n"
"s_lshl_b32 s65, s59, 2\n"
"s_add_u32 s65, s65, s60\n"
"s_mul_i32 s62, s58, 294912\n"
"s_add_u32 s36, %[wslo], 262144\n"
"s_addc_u32 s37, %[wshi], 0\n"
"s_add_u32 s36, s36, s62\n"
"s_addc_u32 s37, s37, 0\n"
"s_and_b32 s37, s37, 0xffff\n"
"s_mov_b32 s38, 294912\n"
"s_mov_b32 s39, 0x20000\n"
"s_lshl_b32 s62, s65, 19\n"
"s_add_u32 s40, %[klo], 0\n"
"s_addc_u32 s41, %[khi], 0\n"
"s_add_u32 s40, s40, s62\n"
"s_addc_u32 s41, s41, 0\n"
"s_and_b32 s41, s41, 0xffff\n"
"s_mov_b32 s42, 524288\n"
"s_mov_b32 s43, 0x20000\n"
"s_add_u32 s44, %[vlo], 0\n"
"s_addc_u32 s45, %[vhi], 0\n"
"s_add_u32 s44, s44, s62\n"
"s_addc_u32 s45, s45, 0\n"
"s_and_b32 s45, s45, 0xffff\n"
"s_mov_b32 s46, 524288\n"
"s_mov_b32 s47, 0x20000\n"
"s_mul_i32 s62, s58, 2359296\n"
"s_lshl_b32 s63, s65, 14\n"
"s_add_u32 s62, s62, s63\n"
"s_add_u32 s48, %[wslo], 7340032\n"
"s_addc_u32 s49, %[wshi], 0\n"
"s_add_u32 s48, s48, s62\n"
"s_addc_u32 s49, s49, 0\n"
"s_and_b32 s49, s49, 0xffff\n"
"s_mov_b32 s50, 1589248\n"
"s_mov_b32 s51, 0x20000\n"
"s_waitcnt vmcnt(12)\n"
"s_add_u32 s66, %[wslo], 77824\n"
"s_addc_u32 s67, %[wshi], 0\n"
"s_mov_b64 s[68:69], s[66:67]\n"
"s_lshl_b32 s62, s58, 8\n"
"s_add_u32 s62, s62, 65536\n"
"s_add_u32 s66, %[wslo], s62\n"
"s_addc_u32 s67, %[wshi], 0\n"
"s_cmp_eq_u32 s56, 0\n"
"s_cbranch_scc0 L_wd6_%=\n"
"v_and_b32 v236, 31, v1\n"
"v_lshlrev_b32 v236, 3, v236\n"
"v_min_u32 v237, 53, v1\n"
"v_lshlrev_b32 v237, 4, v237\n"
"s_mov_b32 s63, 131072\n"
"L_wp6_%=:\n"
"global_load_dwordx4 v[232:235], v237, s[68:69] sc1\n"
"global_load_dwordx4 v[240:243], v237, s[68:69] offset:864 sc1\n"
"global_load_dwordx2 v[238:239], v236, s[66:67] sc1\n"
"s_waitcnt vmcnt(0)\n"
"v_cmp_eq_u64 vcc, s[98:99], v[238:239]\n"
"v_cmp_eq_u64 s[64:65], s[98:99], v[232:233]\n"
"v_cmp_eq_u64 s[70:71], s[98:99], v[234:235]\n"
"s_nop 1\n"
"s_and_b64 vcc, vcc, s[64:65]\n"
"s_and_b64 vcc, vcc, s[70:71]\n"
"v_cmp_eq_u64 s[64:65], s[98:99], v[240:241]\n"
"v_cmp_eq_u64 s[70:71], s[98:99], v[242:243]\n"
"s_nop 1\n"
"s_and_b64 vcc, vcc, s[64:65]\n"
"s_and_b64 vcc, vcc, s[70:71]\n"
"s_cmp_eq_u64 vcc, -1\n"
"s_cbranch_scc1 L_wd6_%=\n"
"s_sleep 2\n"
"s_sub_u32 s63, s63, 1\n"
"s_cmp_lg_u32 s63, 0\n"
"s_cbranch_scc1 L_wp6_%=\n"
"L_wd6_%=:\n"
"s_barrier\n"
"s_mul_i32 s83, s56, 73728\n"
"s_add_u32 s83, s83, 0\n"
"s_add_u32 s84, s83, 4096\n"
"buffer_load_dwordx4 a[128:131], v7, s[36:39], s83 offen sc1\n"
"buffer_load_dwordx4 a[132:135], v7, s[36:39], s83 offen offset:1024 sc1\n"
"buffer_load_dwordx4 a[136:139], v7, s[36:39], s83 offen offset:2048 sc1\n"
"buffer_load_dwordx4 a[140:143], v7, s[36:39], s83 offen offset:3072 sc1\n"
"buffer_load_dwordx4 a[144:147], v7, s[36:39], s84 offen sc1\n"
"buffer_load_dwordx4 a[148:151], v7, s[36:39], s84 offen offset:1024 sc1\n"
"buffer_load_dwordx4 a[152:155], v7, s[36:39], s84 offen offset:2048 sc1\n"
"buffer_load_dwordx4 a[156:159], v7, s[36:39], s84 offen offset:3072 sc1\n"
"ds_read_b64_tr_b16 a[64:65], v10\n"
"dT 66,10,4096\n"
"dT 68,10,8192\n"
"dT 70,10,12288\n"
"ds_read_b64_tr_b16 a[72:73], v11\n"
"dT 74,11,4096\n"
"dT 76,11,8192\n"
"dT 78,11,12288\n"
"dT 80,10,256\n"
"dT 82,10,4352\n"
"dT 84,10,8448\n"
"dT 86,10,12544\n"
"dT 88,11,256\n"
"dT 90,11,4352\n"
"dT 92,11,8448\n"
"dT 94,11,12544\n"
"dT 96,10,512\n"
"dT 98,10,4608\n"
"dT 100,10,8704\n"
"dT 102,10,12800\n"
"dT 104,11,512\n"
"dT 106,11,4608\n"
"dT 108,11,8704\n"
"dT 110,11,12800\n"
"dT 112,10,768\n"
"dT 114,10,4864\n"
"dT 116,10,8960\n"
"dT 118,10,13056\n"
"dT 120,11,768\n"
"dT 122,11,4864\n"
"dT 124,11,8960\n"
"dT 126,11,13056\n"
"s_waitcnt vmcnt(7) lgkmcnt(15)\n"
"mZ 16,64,128\n"
"ds_read_b64_tr_b16 a[0:1], v3\n"
"s_waitcnt vmcnt(6)\n"
"mA 16,68,132,16\n"
"s_waitcnt vmcnt(5)\n"
"mZ 20,64,136\n"
"dT 2,3,4096\n"
"s_waitcnt vmcnt(4)\n"
"mA 20,68,140,20\n"
"s_waitcnt vmcnt(3)\n"
"mZ 24,64,144\n"
"dT 4,3,8192\n"
"s_waitcnt vmcnt(2)\n"
"mA 24,68,148,24\n"
"s_mul_i32 s83, s56, 73728\n"
"s_add_u32 s83, s83, 8192\n"
"s_add_u32 s84, s83, 4096\n"
"buffer_load_dwordx4 a[160:163], v7, s[36:39], s83 offen sc1\n"
"s_waitcnt vmcnt(2)\n"
"mZ 28,64,152\n"
"dT 6,3,12288\n"
"s_waitcnt vmcnt(1)\n"
"mA 28,68,156,28\n"
"mZ 32,72,128\n"
"ds_read_b64_tr_b16 a[8:9], v4\n"
"mA 32,76,132,32\n"
"mZ 36,72,136\n"
"dT 10,4,4096\n"
"mA 36,76,140,36\n"
"buffer_load_dwordx4 a[164:167], v7, s[36:39], s83 offen offset:1024 sc1\n"
"mZ 40,72,144\n"
"dT 12,4,8192\n"
"mA 40,76,148,40\n"
"mZ 44,72,152\n"
"dT 14,4,12288\n"
"mA 44,76,156,44\n"
"mZ 48,80,128\n"
"dT 16,3,256\n"
"mA 48,84,132,48\n"
"buffer_load_dwordx4 a[168:171], v7, s[36:39], s83 offen offset:2048 sc1\n"
"mZ 52,80,136\n"
"dT 18,3,4352\n"
"mA 52,84,140,52\n"
"mZ 56,80,144\n"
"dT 20,3,8448\n"
"mA 56,84,148,56\n"
"mZ 60,80,152\n"
"dT 22,3,12544\n"
"mA 60,84,156,60\n"
"buffer_load_dwordx4 a[172:175], v7, s[36:39], s83 offen offset:3072 sc1\n"
"mZ 64,88,128\n"
"dT 24,4,256\n"
"mA 64,92,132,64\n"
"mZ 68,88,136\n"
"dT 26,4,4352\n"
"mA 68,92,140,68\n"
"mZ 72,88,144\n"
"dT 28,4,8448\n"
"mA 72,92,148,72\n"
"buffer_load_dwordx4 a[176:179], v7, s[36:39], s84 offen sc1\n"
"mZ 76,88,152\n"
"dT 30,4,12544\n"
"mA 76,92,156,76\n"
"s_waitcnt lgkmcnt(12)\n"
"mZ 80,96,128\n"
"dT 32,3,512\n"
"mA 80,100,132,80\n"
"mZ 84,96,136\n"
"dT 34,3,4608\n"
"mA 84,100,140,84\n"
"buffer_load_dwordx4 a[180:183], v7, s[36:39], s84 offen offset:1024 sc1\n"
"mZ 88,96,144\n"
"dT 36,3,8704\n"
"mA 88,100,148,88\n"
"mZ 92,96,152\n"
"dT 38,3,12800\n"
"mA 92,100,156,92\n"
"mZ 96,104,128\n"
"dT 40,4,512\n"
"mA 96,108,132,96\n"
"buffer_load_dwordx4 a[184:187], v7, s[36:39], s84 offen offset:2048 sc1\n"
"mZ 100,104,136\n"
"dT 42,4,4608\n"
"mA 100,108,140,100\n"
"mZ 104,104,144\n"
"dT 44,4,8704\n"
"mA 104,108,148,104\n"
"mZ 108,104,152\n"
"dT 46,4,12800\n"
"mA 108,108,156,108\n"
"buffer_load_dwordx4 a[188:191], v7, s[36:39], s84 offen offset:3072 sc1\n"
"mZ 112,112,128\n"
"dT 48,3,768\n"
"mA 112,116,132,112\n"
"mZ 116,112,136\n"
"dT 50,3,4864\n"
"mA 116,116,140,116\n"
"mZ 120,112,144\n"
"dT 52,3,8960\n"
"mA 120,116,148,120\n"
"s_mul_i32 s83, s56, 73728\n"
"s_add_u32 s83, s83, 16384\n"
"s_add_u32 s84, s83, 4096\n"
"buffer_load_dwordx4 a[192:195], v7, s[36:39], s83 offen sc1\n"
"mZ 124,112,152\n"
"dT 54,3,13056\n"
"mA 124,116,156,124\n"
"mZ 128,120,128\n"
"dT 56,4,768\n"
"mA 128,124,132,128\n"
"mZ 132,120,136\n"
"dT 58,4,4864\n"
"mA 132,124,140,132\n"
"buffer_load_dwordx4 a[196:199], v7, s[36:39], s83 offen offset:1024 sc1\n"
"mZ 136,120,144\n"
"dT 60,4,8960\n"
"mA 136,124,148,136\n"
"mZ 140,120,152\n"
"dT 62,4,13056\n"
"mA 140,124,156,140\n"
"s_waitcnt vmcnt(9)\n"
"mA 16,0,160,16\n"
"dT 64,10,16384\n"
"s_waitcnt vmcnt(8)\n"
"mA 16,4,164,16\n"
"buffer_load_dwordx4 a[200:203], v7, s[36:39], s83 offen offset:2048 sc1\n"
"s_waitcnt vmcnt(8)\n"
"mA 20,0,168,20\n"
"dT 66,10,20480\n"
"s_waitcnt vmcnt(7)\n"
"mA 20,4,172,20\n"
"s_waitcnt vmcnt(6)\n"
"mA 24,0,176,24\n"
"dT 68,10,24576\n"
"s_waitcnt vmcnt(5)\n"
"mA 24,4,180,24\n"
"s_waitcnt vmcnt(4)\n"
"mA 28,0,184,28\n"
"dT 70,10,28672\n"
"s_waitcnt vmcnt(3)\n"
"mA 28,4,188,28\n"
"buffer_load_dwordx4 a[204:207], v7, s[36:39], s83 offen offset:3072 sc1\n"
"s_waitcnt lgkmcnt(9)\n"
"mA 32,8,160,32\n"
"dT 72,11,16384\n"
"mA 32,12,164,32\n"
"mA 36,8,168,36\n"
"dT 74,11,20480\n"
"mA 36,12,172,36\n"
"mA 40,8,176,40\n"
"dT 76,11,24576\n"
"mA 40,12,180,40\n"
"buffer_load_dwordx4 a[208:211], v7, s[36:39], s84 offen sc1\n"
"mA 44,8,184,44\n"
"dT 78,11,28672\n"
"mA 44,12,188,44\n"
"mA 48,16,160,48\n"
"dT 80,10,16640\n"
"mA 48,20,164,48\n"
"mA 52,16,168,52\n"
"dT 82,10,20736\n"
"mA 52,20,172,52\n"
"buffer_load_dwordx4 a[212:215], v7, s[36:39], s84 offen offset:1024 sc1\n"
"mA 56,16,176,56\n"
"dT 84,10,24832\n"
"mA 56,20,180,56\n"
"mA 60,16,184,60\n"
"dT 86,10,28928\n"
"mA 60,20,188,60\n"
"mA 64,24,160,64\n"
"dT 88,11,16640\n"
"mA 64,28,164,64\n"
"buffer_load_dwordx4 a[216:219], v7, s[36:39], s84 offen offset:2048 sc1\n"
"mA 68,24,168,68\n"
"dT 90,11,20736\n"
"mA 68,28,172,68\n"
"mA 72,24,176,72\n"
"dT 92,11,24832\n"
"mA 72,28,180,72\n"
"mA 76,24,184,76\n"
"dT 94,11,28928\n"
"mA 76,28,188,76\n"
"buffer_load_dwordx4 a[220:223], v7, s[36:39], s84 offen offset:3072 sc1\n"
"mA 80,32,160,80\n"
"dT 96,10,16896\n"
"mA 80,36,164,80\n"
"mA 84,32,168,84\n"
"dT 98,10,20992\n"
"mA 84,36,172,84\n"
"mA 88,32,176,88\n"
"dT 100,10,25088\n"
"mA 88,36,180,88\n"
"s_mul_i32 s83, s56, 73728\n"
"s_add_u32 s83, s83, 24576\n"
"s_add_u32 s84, s83, 4096\n"
"buffer_load_dwordx4 a[224:227], v7, s[36:39], s83 offen sc1\n"
"mA 92,32,184,92\n"
"dT 102,10,29184\n"
"mA 92,36,188,92\n"
"mA 96,40,160,96\n"
"dT 104,11,16896\n"
"mA 96,44,164,96\n"
"mA 100,40,168,100\n"
"dT 106,11,20992\n"
"mA 100,44,172,100\n"
"buffer_load_dwordx4 a[228:231], v7, s[36:39], s83 offen offset:1024 sc1\n"
"mA 104,40,176,104\n"
"dT 108,11,25088\n"
"mA 104,44,180,104\n"
"mA 108,40,184,108\n"
"dT 110,11,29184\n"
"mA 108,44,188,108\n"
"mA 112,48,160,112\n"
"dT 112,10,17152\n"
"s_waitcnt lgkmcnt(12)\n"
"mA 112,52,164,112\n"
"buffer_load_dwordx4 a[232:235], v7, s[36:39], s83 offen offset:2048 sc1\n"
"mA 116,48,168,116\n"
"dT 114,10,21248\n"
"mA 116,52,172,116\n"
"mA 120,48,176,120\n"
"dT 116,10,25344\n"
"mA 120,52,180,120\n"
"mA 124,48,184,124\n"
"dT 118,10,29440\n"
"mA 124,52,188,124\n"
"buffer_load_dwordx4 a[236:239], v7, s[36:39], s83 offen offset:3072 sc1\n"
"mA 128,56,160,128\n"
"dT 120,11,17152\n"
"mA 128,60,164,128\n"
"mA 132,56,168,132\n"
"dT 122,11,21248\n"
"mA 132,60,172,132\n"
"mA 136,56,176,136\n"
"dT 124,11,25344\n"
"mA 136,60,180,136\n"
"buffer_load_dwordx4 a[240:243], v7, s[36:39], s84 offen sc1\n"
"mA 140,56,184,140\n"
"dT 126,11,29440\n"
"mA 140,60,188,140\n"
"s_waitcnt lgkmcnt(0)\n"
"s_barrier\n"
"s_waitcnt vmcnt(12)\n"
"mA 16,64,192,16\n"
"dT 0,3,16384\n"
"s_waitcnt vmcnt(11)\n"
"mA 16,68,196,16\n"
"s_waitcnt vmcnt(10)\n"
"mA 20,64,200,20\n"
"dT 2,3,20480\n"
"s_waitcnt vmcnt(9)\n"
"mA 20,68,204,20\n"
"buffer_load_dwordx4 a[244:247], v7, s[36:39], s84 offen offset:1024 sc1\n"
"s_waitcnt vmcnt(9)\n"
"mA 24,64,208,24\n"
"dT 4,3,24576\n"
"s_waitcnt vmcnt(8)\n"
"mA 24,68,212,24\n"
"s_waitcnt vmcnt(7)\n"
"mA 28,64,216,28\n"
"dT 6,3,28672\n"
"s_waitcnt vmcnt(6)\n"
"mA 28,68,220,28\n"
"mA 32,72,192,32\n"
"dT 8,4,16384\n"
"mA 32,76,196,32\n"
"buffer_load_dwordx4 a[248:251], v7, s[36:39], s84 offen offset:2048 sc1\n"
"mA 36,72,200,36\n"
"dT 10,4,20480\n"
"mA 36,76,204,36\n"
"mA 40,72,208,40\n"
"dT 12,4,24576\n"
"mA 40,76,212,40\n"
"mA 44,72,216,44\n"
"dT 14,4,28672\n"
"mA 44,76,220,44\n"
"buffer_load_dwordx4 a[252:255], v7, s[36:39], s84 offen offset:3072 sc1\n"
"mA 48,80,192,48\n"
"dT 16,3,16640\n"
"mA 48,84,196,48\n"
"mA 52,80,200,52\n"
"dT 18,3,20736\n"
"mA 52,84,204,52\n"
"mA 56,80,208,56\n"
"dT 20,3,24832\n"
"mA 56,84,212,56\n"
"s_mul_i32 s83, s56, 73728\n"
"s_add_u32 s83, s83, 32768\n"
"s_add_u32 s84, s83, 4096\n"
"buffer_load_dwordx4 a[128:131], v7, s[36:39], s83 offen sc1\n"
"mA 60,80,216,60\n"
"dT 22,3,28928\n"
"mA 60,84,220,60\n"
"mA 64,88,192,64\n"
"dT 24,4,16640\n"
"mA 64,92,196,64\n"
"mA 68,88,200,68\n"
"dT 26,4,20736\n"
"mA 68,92,204,68\n"
"buffer_load_dwordx4 a[132:135], v7, s[36:39], s83 offen offset:1024 sc1\n"
"mA 72,88,208,72\n"
"dT 28,4,24832\n"
"mA 72,92,212,72\n"
"mA 76,88,216,76\n"
"dT 30,4,28928\n"
"mA 76,92,220,76\n"
"mA 80,96,192,80\n"
"dT 32,3,16896\n"
"mA 80,100,196,80\n"
"buffer_load_dwordx4 a[136:139], v7, s[36:39], s83 offen offset:2048 sc1\n"
"mA 84,96,200,84\n"
"dT 34,3,20992\n"
"mA 84,100,204,84\n"
"mA 88,96,208,88\n"
"dT 36,3,25088\n"
"mA 88,100,212,88\n"
"mA 92,96,216,92\n"
"dT 38,3,29184\n"
"mA 92,100,220,92\n"
"buffer_load_dwordx4 a[140:143], v7, s[36:39], s83 offen offset:3072 sc1\n"
"mA 96,104,192,96\n"
"dT 40,4,16896\n"
"mA 96,108,196,96\n"
"mA 100,104,200,100\n"
"dT 42,4,20992\n"
"mA 100,108,204,100\n"
"mA 104,104,208,104\n"
"dT 44,4,25088\n"
"mA 104,108,212,104\n"
"buffer_load_dwordx4 a[144:147], v7, s[36:39], s84 offen sc1\n"
"mA 108,104,216,108\n"
"dT 46,4,29184\n"
"mA 108,108,220,108\n"
"mA 112,112,192,112\n"
"dT 48,3,17152\n"
"mA 112,116,196,112\n"
"mA 116,112,200,116\n"
"dT 50,3,21248\n"
"mA 116,116,204,116\n"
"buffer_load_dwordx4 a[148:151], v7, s[36:39], s84 offen offset:1024 sc1\n"
"mA 120,112,208,120\n"
"dT 52,3,25344\n"
"mA 120,116,212,120\n"
"mA 124,112,216,124\n"
"dT 54,3,29440\n"
"mA 124,116,220,124\n"
"mA 128,120,192,128\n"
"dT 56,4,17152\n"
"mA 128,124,196,128\n"
"buffer_load_dwordx4 a[152:155], v7, s[36:39], s84 offen offset:2048 sc1\n"
"mA 132,120,200,132\n"
"dT 58,4,21248\n"
"mA 132,124,204,132\n"
"mA 136,120,208,136\n"
"dT 60,4,25344\n"
"mA 136,124,212,136\n"
"mA 140,120,216,140\n"
"dT 62,4,29440\n"
"mA 140,124,220,140\n"
"buffer_load_dwordx4 a[156:159], v7, s[36:39], s84 offen offset:3072 sc1\n"
"s_waitcnt lgkmcnt(0)\n"
"s_barrier\n"
"s_waitcnt vmcnt(15)\n"
"mA 16,0,224,16\n"
"dT 64,10,32768\n"
"s_waitcnt vmcnt(14)\n"
"mA 16,4,228,16\n"
"s_waitcnt vmcnt(13)\n"
"mA 20,0,232,20\n"
"dT 66,10,36864\n"
"s_waitcnt vmcnt(12)\n"
"mA 20,4,236,20\n"
"s_waitcnt vmcnt(11)\n"
"mA 24,0,240,24\n"
"dT 68,10,40960\n"
"s_waitcnt vmcnt(10)\n"
"mA 24,4,244,24\n"
"s_mul_i32 s83, s56, 73728\n"
"s_add_u32 s83, s83, 40960\n"
"s_add_u32 s84, s83, 4096\n"
"buffer_load_dwordx4 a[160:163], v7, s[36:39], s83 offen sc1\n"
"s_waitcnt vmcnt(10)\n"
"mA 28,0,248,28\n"
"dT 70,10,45056\n"
"s_waitcnt vmcnt(9)\n"
"mA 28,4,252,28\n"
"mA 32,8,224,32\n"
"dT 72,11,32768\n"
"mA 32,12,228,32\n"
"mA 36,8,232,36\n"
"dT 74,11,36864\n"
"mA 36,12,236,36\n"
"buffer_load_dwordx4 a[164:167], v7, s[36:39], s83 offen offset:1024 sc1\n"
"mA 40,8,240,40\n"
"dT 76,11,40960\n"
"mA 40,12,244,40\n"
"mA 44,8,248,44\n"
"dT 78,11,45056\n"
"mA 44,12,252,44\n"
"mA 48,16,224,48\n"
"dT 80,10,33024\n"
"mA 48,20,228,48\n"
"buffer_load_dwordx4 a[168:171], v7, s[36:39], s83 offen offset:2048 sc1\n"
"mA 52,16,232,52\n"
"dT 82,10,37120\n"
"mA 52,20,236,52\n"
"mA 56,16,240,56\n"
"dT 84,10,41216\n"
"mA 56,20,244,56\n"
"mA 60,16,248,60\n"
"dT 86,10,45312\n"
"mA 60,20,252,60\n"
"buffer_load_dwordx4 a[172:175], v7, s[36:39], s83 offen offset:3072 sc1\n"
"mA 64,24,224,64\n"
"dT 88,11,33024\n"
"mA 64,28,228,64\n"
"mA 68,24,232,68\n"
"dT 90,11,37120\n"
"mA 68,28,236,68\n"
"mA 72,24,240,72\n"
"dT 92,11,41216\n"
"mA 72,28,244,72\n"
"buffer_load_dwordx4 a[176:179], v7, s[36:39], s84 offen sc1\n"
"mA 76,24,248,76\n"
"dT 94,11,45312\n"
"mA 76,28,252,76\n"
"mA 80,32,224,80\n"
"dT 96,10,33280\n"
"mA 80,36,228,80\n"
"mA 84,32,232,84\n"
"dT 98,10,37376\n"
"mA 84,36,236,84\n"
"buffer_load_dwordx4 a[180:183], v7, s[36:39], s84 offen offset:1024 sc1\n"
"mA 88,32,240,88\n"
"dT 100,10,41472\n"
"mA 88,36,244,88\n"
"mA 92,32,248,92\n"
"dT 102,10,45568\n"
"mA 92,36,252,92\n"
"mA 96,40,224,96\n"
"dT 104,11,33280\n"
"mA 96,44,228,96\n"
"buffer_load_dwordx4 a[184:187], v7, s[36:39], s84 offen offset:2048 sc1\n"
"mA 100,40,232,100\n"
"dT 106,11,37376\n"
"mA 100,44,236,100\n"
"mA 104,40,240,104\n"
"dT 108,11,41472\n"
"mA 104,44,244,104\n"
"mA 108,40,248,108\n"
"dT 110,11,45568\n"
"mA 108,44,252,108\n"
"buffer_load_dwordx4 a[188:191], v7, s[36:39], s84 offen offset:3072 sc1\n"
"mA 112,48,224,112\n"
"dT 112,10,33536\n"
"mA 112,52,228,112\n"
"mA 116,48,232,116\n"
"dT 114,10,37632\n"
"mA 116,52,236,116\n"
"mA 120,48,240,120\n"
"dT 116,10,41728\n"
"mA 120,52,244,120\n"
"s_mul_i32 s83, s56, 73728\n"
"s_add_u32 s83, s83, 49152\n"
"s_add_u32 s84, s83, 4096\n"
"buffer_load_dwordx4 a[192:195], v7, s[36:39], s83 offen sc1\n"
"mA 124,48,248,124\n"
"dT 118,10,45824\n"
"mA 124,52,252,124\n"
"mA 128,56,224,128\n"
"dT 120,11,33536\n"
"mA 128,60,228,128\n"
"mA 132,56,232,132\n"
"dT 122,11,37632\n"
"mA 132,60,236,132\n"
"buffer_load_dwordx4 a[196:199], v7, s[36:39], s83 offen offset:1024 sc1\n"
"mA 136,56,240,136\n"
"dT 124,11,41728\n"
"mA 136,60,244,136\n"
"mA 140,56,248,140\n"
"dT 126,11,45824\n"
"mA 140,60,252,140\n"
"s_waitcnt lgkmcnt(0)\n"
"s_barrier\n"
"s_waitcnt vmcnt(17)\n"
"mA 16,64,128,16\n"
"dT 0,3,32768\n"
"s_waitcnt vmcnt(16)\n"
"mA 16,68,132,16\n"
"buffer_load_dwordx4 a[200:203], v7, s[36:39], s83 offen offset:2048 sc1\n"
"s_waitcnt vmcnt(16)\n"
"mA 20,64,136,20\n"
"dT 2,3,36864\n"
"s_waitcnt vmcnt(15)\n"
"mA 20,68,140,20\n"
"s_waitcnt vmcnt(14)\n"
"mA 24,64,144,24\n"
"dT 4,3,40960\n"
"s_waitcnt vmcnt(13)\n"
"mA 24,68,148,24\n"
"s_waitcnt vmcnt(12)\n"
"mA 28,64,152,28\n"
"dT 6,3,45056\n"
"s_waitcnt vmcnt(11)\n"
"mA 28,68,156,28\n"
"buffer_load_dwordx4 a[204:207], v7, s[36:39], s83 offen offset:3072 sc1\n"
"mA 32,72,128,32\n"
"dT 8,4,32768\n"
"mA 32,76,132,32\n"
"mA 36,72,136,36\n"
"dT 10,4,36864\n"
"mA 36,76,140,36\n"
"mA 40,72,144,40\n"
"dT 12,4,40960\n"
"mA 40,76,148,40\n"
"buffer_load_dwordx4 a[208:211], v7, s[36:39], s84 offen sc1\n"
"mA 44,72,152,44\n"
"dT 14,4,45056\n"
"mA 44,76,156,44\n"
"mA 48,80,128,48\n"
"dT 16,3,33024\n"
"mA 48,84,132,48\n"
"mA 52,80,136,52\n"
"dT 18,3,37120\n"
"mA 52,84,140,52\n"
"buffer_load_dwordx4 a[212:215], v7, s[36:39], s84 offen offset:1024 sc1\n"
"mA 56,80,144,56\n"
"dT 20,3,41216\n"
"mA 56,84,148,56\n"
"mA 60,80,152,60\n"
"dT 22,3,45312\n"
"mA 60,84,156,60\n"
"mA 64,88,128,64\n"
"dT 24,4,33024\n"
"mA 64,92,132,64\n"
"buffer_load_dwordx4 a[216:219], v7, s[36:39], s84 offen offset:2048 sc1\n"
"mA 68,88,136,68\n"
"dT 26,4,37120\n"
"mA 68,92,140,68\n"
"mA 72,88,144,72\n"
"dT 28,4,41216\n"
"mA 72,92,148,72\n"
"mA 76,88,152,76\n"
"dT 30,4,45312\n"
"mA 76,92,156,76\n"
"buffer_load_dwordx4 a[220:223], v7, s[36:39], s84 offen offset:3072 sc1\n"
"mA 80,96,128,80\n"
"dT 32,3,33280\n"
"mA 80,100,132,80\n"
"mA 84,96,136,84\n"
"dT 34,3,37376\n"
"mA 84,100,140,84\n"
"mA 88,96,144,88\n"
"dT 36,3,41472\n"
"mA 88,100,148,88\n"
"s_mov_b32 m0, s73\n"
"s_nop 0\n"
"buffer_load_dwordx4 v5, s[40:43], s81 offen lds\n"
"mA 92,96,152,92\n"
"dT 38,3,45568\n"
"mA 92,100,156,92\n"
"mA 96,104,128,96\n"
"dT 40,4,33280\n"
"mA 96,108,132,96\n"
"mA 100,104,136,100\n"
"dT 42,4,37376\n"
"mA 100,108,140,100\n"
"buffer_load_dwordx4 v5, s[40:43], s81 offen offset:1024 lds\n"
"mA 104,104,144,104\n"
"dT 44,4,41472\n"
"mA 104,108,148,104\n"
"mA 108,104,152,108\n"
"dT 46,4,45568\n"
"mA 108,108,156,108\n"
"mA 112,112,128,112\n"
"dT 48,3,33536\n"
"mA 112,116,132,112\n"
"buffer_load_dwordx4 v6, s[40:43], s81 offen offset:2048 lds\n"
"mA 116,112,136,116\n"
"dT 50,3,37632\n"
"mA 116,116,140,116\n"
"mA 120,112,144,120\n"
"dT 52,3,41728\n"
"mA 120,116,148,120\n"
"mA 124,112,152,124\n"
"dT 54,3,45824\n"
"mA 124,116,156,124\n"
"buffer_load_dwordx4 v6, s[40:43], s81 offen offset:3072 lds\n"
"mA 128,120,128,128\n"
"dT 56,4,33536\n"
"mA 128,124,132,128\n"
"mA 132,120,136,132\n"
"dT 58,4,37632\n"
"mA 132,124,140,132\n"
"mA 136,120,144,136\n"
"dT 60,4,41728\n"
"mA 136,124,148,136\n"
"s_mov_b32 m0, s77\n"
"s_nop 0\n"
"buffer_load_dwordx4 v5, s[44:47], s81 offen lds\n"
"mA 140,120,152,140\n"
"dT 62,4,45824\n"
"mA 140,124,156,140\n"
"s_waitcnt lgkmcnt(0)\n"
"s_barrier\n"
"s_waitcnt vmcnt(20)\n"
"mA 16,0,160,16\n"
"dT 64,10,49152\n"
"s_waitcnt vmcnt(19)\n"
"mA 16,4,164,16\n"
"s_waitcnt vmcnt(18)\n"
"mA 20,0,168,20\n"
"dT 66,10,53248\n"
"s_waitcnt vmcnt(17)\n"
"mA 20,4,172,20\n"
"buffer_load_dwordx4 v6, s[44:47], s81 offen offset:1024 lds\n"
"s_waitcnt vmcnt(17)\n"
"mA 24,0,176,24\n"
"dT 68,10,57344\n"
"s_waitcnt vmcnt(16)\n"
"mA 24,4,180,24\n"
"s_waitcnt vmcnt(15)\n"
"mA 28,0,184,28\n"
"dT 70,10,61440\n"
"s_waitcnt vmcnt(14)\n"
"mA 28,4,188,28\n"
"mA 32,8,160,32\n"
"dT 72,11,49152\n"
"mA 32,12,164,32\n"
"buffer_load_dwordx4 v5, s[44:47], s81 offen offset:2048 lds\n"
"mA 36,8,168,36\n"
"dT 74,11,53248\n"
"mA 36,12,172,36\n"
"mA 40,8,176,40\n"
"dT 76,11,57344\n"
"mA 40,12,180,40\n"
"mA 44,8,184,44\n"
"dT 78,11,61440\n"
"mA 44,12,188,44\n"
"buffer_load_dwordx4 v6, s[44:47], s81 offen offset:3072 lds\n"
"s_add_u32 s81, s81, 16384\n"
"mA 48,16,160,48\n"
"dT 80,10,49408\n"
"mA 48,20,164,48\n"
"mA 52,16,168,52\n"
"dT 82,10,53504\n"
"mA 52,20,172,52\n"
"mA 56,16,176,56\n"
"dT 84,10,57600\n"
"mA 56,20,180,56\n"
"s_mul_i32 s83, s56, 73728\n"
"s_add_u32 s83, s83, 57344\n"
"s_add_u32 s84, s83, 4096\n"
"buffer_load_dwordx4 a[224:227], v7, s[36:39], s83 offen sc1\n"
"mA 60,16,184,60\n"
"dT 86,10,61696\n"
"mA 60,20,188,60\n"
"mA 64,24,160,64\n"
"dT 88,11,49408\n"
"mA 64,28,164,64\n"
"mA 68,24,168,68\n"
"dT 90,11,53504\n"
"mA 68,28,172,68\n"
"buffer_load_dwordx4 a[228:231], v7, s[36:39], s83 offen offset:1024 sc1\n"
"mA 72,24,176,72\n"
"dT 92,11,57600\n"
"mA 72,28,180,72\n"
"mA 76,24,184,76\n"
"dT 94,11,61696\n"
"mA 76,28,188,76\n"
"mA 80,32,160,80\n"
"dT 96,10,49664\n"
"mA 80,36,164,80\n"
"buffer_load_dwordx4 a[232:235], v7, s[36:39], s83 offen offset:2048 sc1\n"
"mA 84,32,168,84\n"
"dT 98,10,53760\n"
"mA 84,36,172,84\n"
"mA 88,32,176,88\n"
"dT 100,10,57856\n"
"mA 88,36,180,88\n"
"mA 92,32,184,92\n"
"dT 102,10,61952\n"
"mA 92,36,188,92\n"
"buffer_load_dwordx4 a[236:239], v7, s[36:39], s83 offen offset:3072 sc1\n"
"mA 96,40,160,96\n"
"dT 104,11,49664\n"
"mA 96,44,164,96\n"
"mA 100,40,168,100\n"
"dT 106,11,53760\n"
"mA 100,44,172,100\n"
"mA 104,40,176,104\n"
"dT 108,11,57856\n"
"mA 104,44,180,104\n"
"buffer_load_dwordx4 a[240:243], v7, s[36:39], s84 offen sc1\n"
"mA 108,40,184,108\n"
"dT 110,11,61952\n"
"mA 108,44,188,108\n"
"mA 112,48,160,112\n"
"dT 112,10,49920\n"
"mA 112,52,164,112\n"
"mA 116,48,168,116\n"
"dT 114,10,54016\n"
"mA 116,52,172,116\n"
"buffer_load_dwordx4 a[244:247], v7, s[36:39], s84 offen offset:1024 sc1\n"
"mA 120,48,176,120\n"
"dT 116,10,58112\n"
"mA 120,52,180,120\n"
"mA 124,48,184,124\n"
"dT 118,10,62208\n"
"mA 124,52,188,124\n"
"mA 128,56,160,128\n"
"dT 120,11,49920\n"
"mA 128,60,164,128\n"
"buffer_load_dwordx4 a[248:251], v7, s[36:39], s84 offen offset:2048 sc1\n"
"mA 132,56,168,132\n"
"dT 122,11,54016\n"
"mA 132,60,172,132\n"
"mA 136,56,176,136\n"
"dT 124,11,58112\n"
"mA 136,60,180,136\n"
"mA 140,56,184,140\n"
"dT 126,11,62208\n"
"mA 140,60,188,140\n"
"buffer_load_dwordx4 a[252:255], v7, s[36:39], s84 offen offset:3072 sc1\n"
"s_waitcnt lgkmcnt(0)\n"
"s_barrier\n"
"s_waitcnt vmcnt(23)\n"
"mA 16,64,192,16\n"
"dT 0,3,49152\n"
"s_waitcnt vmcnt(22)\n"
"mA 16,68,196,16\n"
"s_waitcnt vmcnt(21)\n"
"mA 20,64,200,20\n"
"dT 2,3,53248\n"
"s_waitcnt vmcnt(20)\n"
"mA 20,68,204,20\n"
"s_waitcnt vmcnt(19)\n"
"mA 24,64,208,24\n"
"dT 4,3,57344\n"
"s_waitcnt vmcnt(18)\n"
"mA 24,68,212,24\n"
"s_mul_i32 s83, s56, 73728\n"
"s_add_u32 s83, s83, 65536\n"
"s_add_u32 s84, s83, 4096\n"
"buffer_load_dwordx4 a[128:131], v7, s[36:39], s83 offen sc1\n"
"s_waitcnt vmcnt(18)\n"
"mA 28,64,216,28\n"
"dT 6,3,61440\n"
"s_waitcnt vmcnt(17)\n"
"mA 28,68,220,28\n"
"mA 32,72,192,32\n"
"dT 8,4,49152\n"
"mA 32,76,196,32\n"
"mA 36,72,200,36\n"
"dT 10,4,53248\n"
"mA 36,76,204,36\n"
"buffer_load_dwordx4 a[132:135], v7, s[36:39], s83 offen offset:1024 sc1\n"
"mA 40,72,208,40\n"
"dT 12,4,57344\n"
"mA 40,76,212,40\n"
"mA 44,72,216,44\n"
"dT 14,4,61440\n"
"mA 44,76,220,44\n"
"mA 48,80,192,48\n"
"dT 16,3,49408\n"
"mA 48,84,196,48\n"
"buffer_load_dwordx4 a[136:139], v7, s[36:39], s83 offen offset:2048 sc1\n"
"mA 52,80,200,52\n"
"dT 18,3,53504\n"
"mA 52,84,204,52\n"
"mA 56,80,208,56\n"
"dT 20,3,57600\n"
"mA 56,84,212,56\n"
"mA 60,80,216,60\n"
"dT 22,3,61696\n"
"mA 60,84,220,60\n"
"buffer_load_dwordx4 a[140:143], v7, s[36:39], s83 offen offset:3072 sc1\n"
"mA 64,88,192,64\n"
"dT 24,4,49408\n"
"mA 64,92,196,64\n"
"mA 68,88,200,68\n"
"dT 26,4,53504\n"
"mA 68,92,204,68\n"
"mA 72,88,208,72\n"
"dT 28,4,57600\n"
"mA 72,92,212,72\n"
"buffer_load_dwordx4 a[144:147], v7, s[36:39], s84 offen sc1\n"
"mA 76,88,216,76\n"
"dT 30,4,61696\n"
"mA 76,92,220,76\n"
"mA 80,96,192,80\n"
"dT 32,3,49664\n"
"mA 80,100,196,80\n"
"mA 84,96,200,84\n"
"dT 34,3,53760\n"
"mA 84,100,204,84\n"
"buffer_load_dwordx4 a[148:151], v7, s[36:39], s84 offen offset:1024 sc1\n"
"mA 88,96,208,88\n"
"dT 36,3,57856\n"
"mA 88,100,212,88\n"
"mA 92,96,216,92\n"
"dT 38,3,61952\n"
"mA 92,100,220,92\n"
"mA 96,104,192,96\n"
"dT 40,4,49664\n"
"mA 96,108,196,96\n"
"buffer_load_dwordx4 a[152:155], v7, s[36:39], s84 offen offset:2048 sc1\n"
"mA 100,104,200,100\n"
"dT 42,4,53760\n"
"mA 100,108,204,100\n"
"mA 104,104,208,104\n"
"dT 44,4,57856\n"
"mA 104,108,212,104\n"
"mA 108,104,216,108\n"
"dT 46,4,61952\n"
"mA 108,108,220,108\n"
"buffer_load_dwordx4 a[156:159], v7, s[36:39], s84 offen offset:3072 sc1\n"
"mA 112,112,192,112\n"
"dT 48,3,49920\n"
"mA 112,116,196,112\n"
"mA 116,112,200,116\n"
"dT 50,3,54016\n"
"mA 116,116,204,116\n"
"mA 120,112,208,120\n"
"dT 52,3,58112\n"
"mA 120,116,212,120\n"
"s_mov_b32 m0, s74\n"
"s_nop 0\n"
"buffer_load_dwordx4 v5, s[40:43], s81 offen lds\n"
"mA 124,112,216,124\n"
"dT 54,3,62208\n"
"mA 124,116,220,124\n"
"mA 128,120,192,128\n"
"dT 56,4,49920\n"
"mA 128,124,196,128\n"
"mA 132,120,200,132\n"
"dT 58,4,54016\n"
"mA 132,124,204,132\n"
"buffer_load_dwordx4 v5, s[40:43], s81 offen offset:1024 lds\n"
"mA 136,120,208,136\n"
"dT 60,4,58112\n"
"mA 136,124,212,136\n"
"mA 140,120,216,140\n"
"dT 62,4,62208\n"
"mA 140,124,220,140\n"
"s_waitcnt lgkmcnt(0)\n"
"s_barrier\n"
"s_waitcnt vmcnt(17)\n"
"mA 16,0,224,16\n"
"ds_read_b64_tr_b16 a[64:65], v12\n"
"s_waitcnt vmcnt(16)\n"
"mA 16,4,228,16\n"
"buffer_load_dwordx4 v6, s[40:43], s81 offen offset:2048 lds\n"
"s_waitcnt vmcnt(16)\n"
"mA 20,0,232,20\n"
"dT 66,12,4096\n"
"s_waitcnt vmcnt(15)\n"
"mA 20,4,236,20\n"
"s_waitcnt vmcnt(14)\n"
"mA 24,0,240,24\n"
"dT 68,12,8192\n"
"s_waitcnt vmcnt(13)\n"
"mA 24,4,244,24\n"
"s_waitcnt vmcnt(12)\n"
"mA 28,0,248,28\n"
"dT 70,12,12288\n"
"s_waitcnt vmcnt(11)\n"
"mA 28,4,252,28\n"
"buffer_load_dwordx4 v6, s[40:43], s81 offen offset:3072 lds\n"
"mA 32,8,224,32\n"
"ds_read_b64_tr_b16 a[72:73], v13\n"
"mA 32,12,228,32\n"
"mA 36,8,232,36\n"
"dT 74,13,4096\n"
"mA 36,12,236,36\n"
"mA 40,8,240,40\n"
"dT 76,13,8192\n"
"mA 40,12,244,40\n"
"s_mov_b32 m0, s78\n"
"s_nop 0\n"
"buffer_load_dwordx4 v5, s[44:47], s81 offen lds\n"
"mA 44,8,248,44\n"
"dT 78,13,12288\n"
"mA 44,12,252,44\n"
"mA 48,16,224,48\n"
"dT 80,12,256\n"
"mA 48,20,228,48\n"
"mA 52,16,232,52\n"
"dT 82,12,4352\n"
"mA 52,20,236,52\n"
"buffer_load_dwordx4 v6, s[44:47], s81 offen offset:1024 lds\n"
"mA 56,16,240,56\n"
"dT 84,12,8448\n"
"mA 56,20,244,56\n"
"mA 60,16,248,60\n"
"dT 86,12,12544\n"
"mA 60,20,252,60\n"
"mA 64,24,224,64\n"
"dT 88,13,256\n"
"mA 64,28,228,64\n"
"buffer_load_dwordx4 v5, s[44:47], s81 offen offset:2048 lds\n"
"mA 68,24,232,68\n"
"dT 90,13,4352\n"
"mA 68,28,236,68\n"
"mA 72,24,240,72\n"
"dT 92,13,8448\n"
"mA 72,28,244,72\n"
"mA 76,24,248,76\n"
"dT 94,13,12544\n"
"mA 76,28,252,76\n"
"buffer_load_dwordx4 v6, s[44:47], s81 offen offset:3072 lds\n"
"s_add_u32 s81, s81, 16384\n"
"mA 80,32,224,80\n"
"dT 96,12,512\n"
"mA 80,36,228,80\n"
"mA 84,32,232,84\n"
"dT 98,12,4608\n"
"mA 84,36,236,84\n"
"mA 88,32,240,88\n"
"dT 100,12,8704\n"
"mA 88,36,244,88\n"
"s_mov_b32 m0, s75\n"
"s_nop 0\n"
"buffer_load_dwordx4 v5, s[40:43], s81 offen lds\n"
"mA 92,32,248,92\n"
"dT 102,12,12800\n"
"mA 92,36,252,92\n"
"mA 96,40,224,96\n"
"dT 104,13,512\n"
"mA 96,44,228,96\n"
"mA 100,40,232,100\n"
"dT 106,13,4608\n"
"mA 100,44,236,100\n"
"buffer_load_dwordx4 v5, s[40:43], s81 offen offset:1024 lds\n"
"mA 104,40,240,104\n"
"dT 108,13,8704\n"
"mA 104,44,244,104\n"
"mA 108,40,248,108\n"
"dT 110,13,12800\n"
"mA 108,44,252,108\n"
"mA 112,48,224,112\n"
"dT 112,12,768\n"
"mA 112,52,228,112\n"
"buffer_load_dwordx4 v6, s[40:43], s81 offen offset:2048 lds\n"
"mA 116,48,232,116\n"
"dT 114,12,4864\n"
"mA 116,52,236,116\n"
"mA 120,48,240,120\n"
"dT 116,12,8960\n"
"mA 120,52,244,120\n"
"mA 124,48,248,124\n"
"dT 118,12,13056\n"
"mA 124,52,252,124\n"
"buffer_load_dwordx4 v6, s[40:43], s81 offen offset:3072 lds\n"
"mA 128,56,224,128\n"
"dT 120,13,768\n"
"mA 128,60,228,128\n"
"mA 132,56,232,132\n"
"dT 122,13,4864\n"
"mA 132,60,236,132\n"
"mA 136,56,240,136\n"
"dT 124,13,8960\n"
"mA 136,60,244,136\n"
"s_mov_b32 m0, s79\n"
"s_nop 0\n"
"buffer_load_dwordx4 v5, s[44:47], s81 offen lds\n"
"mA 140,56,248,140\n"
"dT 126,13,13056\n"
"mA 140,60,252,140\n"
"buffer_load_dwordx4 v6, s[44:47], s81 offen offset:1024 lds\n"
"buffer_load_dwordx4 v5, s[44:47], s81 offen offset:2048 lds\n"
"buffer_load_dwordx4 v6, s[44:47], s81 offen offset:3072 lds\n"
"s_add_u32 s81, s81, 16384\n"
"v_lshlrev_b32 v15, 6, v1\n"
"s_mul_i32 s62, s59, 589824\n"
"s_add_u32 s36, %[wolo], s62\n"
"s_addc_u32 s37, %[wohi], 0\n"
"s_and_b32 s37, s37, 0xffff\n"
"s_mov_b32 s38, 589824\n"
"s_mul_i32 s62, s59, 663552\n"
"s_add_u32 s66, %[w1lo], s62\n"
"s_addc_u32 s67, %[w1hi], 0\n"
"s_and_b32 s67, s67, 0xffff\n"
"s_mov_b32 s39, 663552\n"
"s_mul_i32 s62, s59, 663552\n"
"s_add_u32 s68, %[w2lo], s62\n"
"s_addc_u32 s69, %[w2hi], 0\n"
"s_and_b32 s69, s69, 0xffff\n"
"s_mov_b32 s70, 663552\n"
"s_lshr_b32 s93, %[bid], 3\n"
"s_lshl_b32 s93, s93, 2\n"
"s_add_u32 s93, s93, s56\n"
"s_lshl_b32 s93, s93, 12\n"
"s_add_u32 s71, s93, 524288\n"
"s_lshl_b32 s83, %[bid], 2\n"
"s_add_u32 s83, s83, s56\n"
"s_lshl_b32 s83, s83, 10\n"
"s_waitcnt lgkmcnt(0)\n"
"s_barrier\n"
"s_waitcnt vmcnt(23)\n"
"mA 16,64,128,16\n"
"ds_read_b128 a[192:195], v2\n"
"s_waitcnt vmcnt(22)\n"
"mA 16,68,132,16\n"
"mA 32,72,128,32\n"
"dK 196,2,256\n"
"mA 32,76,132,32\n"
"mA 48,80,128,48\n"
"dK 200,2,512\n"
"mA 48,84,132,48\n"
"mA 64,88,128,64\n"
"dK 204,2,768\n"
"mA 64,92,132,64\n"
"mA 80,96,128,80\n"
"dK 208,2,4096\n"
"mA 80,100,132,80\n"
"mA 96,104,128,96\n"
"dK 212,2,4352\n"
"mA 96,108,132,96\n"
"mA 112,112,128,112\n"
"dK 216,2,4608\n"
"mA 112,116,132,112\n"
"mA 128,120,128,128\n"
"dK 220,2,4864\n"
"mA 128,124,132,128\n"
"s_waitcnt vmcnt(21)\n"
"mA 20,64,136,20\n"
"dK 224,2,8192\n"
"s_waitcnt vmcnt(20)\n"
"mA 20,68,140,20\n"
"v_mul_f32 v16, s85, v16\n"
"v_mul_f32 v17, s85, v17\n"
"v_mul_f32 v18, s85, v18\n"
"mA 36,72,136,36\n"
"dK 228,2,8448\n"
"v_mul_f32 v19, s85, v19\n"
"v_mul_f32 v32, s85, v32\n"
"v_mul_f32 v33, s85, v33\n"
"mA 36,76,140,36\n"
"v_mul_f32 v34, s85, v34\n"
"v_mul_f32 v35, s85, v35\n"
"cP 212,16,17\n"
"mA 52,80,136,52\n"
"dK 232,2,8704\n"
"cP 213,18,19\n"
"cP 214,32,33\n"
"cP 215,34,35\n"
"mA 52,84,140,52\n"
"v_mul_f32 v48, s85, v48\n"
"v_mul_f32 v49, s85, v49\n"
"v_mul_f32 v50, s85, v50\n"
"mA 68,88,136,68\n"
"dK 236,2,8960\n"
"v_mul_f32 v51, s85, v51\n"
"v_mul_f32 v64, s85, v64\n"
"v_mul_f32 v65, s85, v65\n"
"mA 68,92,140,68\n"
"v_mul_f32 v66, s85, v66\n"
"v_mul_f32 v67, s85, v67\n"
"cP 216,48,49\n"
"mA 84,96,136,84\n"
"dK 240,2,12288\n"
"cP 217,50,51\n"
"cP 218,64,65\n"
"cP 219,66,67\n"
"mA 84,100,140,84\n"
"v_mul_f32 v80, s85, v80\n"
"v_mul_f32 v81, s85, v81\n"
"v_mul_f32 v82, s85, v82\n"
"mA 100,104,136,100\n"
"dK 244,2,12544\n"
"v_mul_f32 v83, s85, v83\n"
"v_mul_f32 v96, s85, v96\n"
"v_mul_f32 v97, s85, v97\n"
"mA 100,108,140,100\n"
"v_mul_f32 v98, s85, v98\n"
"v_mul_f32 v99, s85, v99\n"
"cP 220,80,81\n"
"mA 116,112,136,116\n"
"dK 248,2,12800\n"
"cP 221,82,83\n"
"cP 222,96,97\n"
"cP 223,98,99\n"
"mA 116,116,140,116\n"
"v_mul_f32 v112, s85, v112\n"
"v_mul_f32 v113, s85, v113\n"
"v_mul_f32 v114, s85, v114\n"
"mA 132,120,136,132\n"
"dK 252,2,13056\n"
"v_mul_f32 v115, s85, v115\n"
"v_mul_f32 v128, s85, v128\n"
"v_mul_f32 v129, s85, v129\n"
"mA 132,124,140,132\n"
"v_mul_f32 v130, s85, v130\n"
"v_mul_f32 v131, s85, v131\n"
"cP 224,112,113\n"
"s_waitcnt vmcnt(19)\n"
"mA 24,64,144,24\n"
"cP 225,114,115\n"
"cP 226,128,129\n"
"cP 227,130,131\n"
"s_waitcnt vmcnt(18)\n"
"mA 24,68,148,24\n"
"v_permlane32_swap_b32 v212, v214\n"
"v_permlane32_swap_b32 v213, v215\n"
"v_permlane32_swap_b32 v216, v218\n"
"v_permlane32_swap_b32 v217, v219\n"
"v_permlane32_swap_b32 v220, v222\n"
"v_permlane32_swap_b32 v221, v223\n"
"v_permlane32_swap_b32 v224, v226\n"
"v_permlane32_swap_b32 v225, v227\n"
"v_permlane16_swap_b32 v212, v214\n"
"v_permlane16_swap_b32 v213, v215\n"
"v_permlane16_swap_b32 v216, v218\n"
"v_permlane16_swap_b32 v217, v219\n"
"v_permlane16_swap_b32 v220, v222\n"
"v_permlane16_swap_b32 v221, v223\n"
"v_permlane16_swap_b32 v224, v226\n"
"v_permlane16_swap_b32 v225, v227\n"
"v_accvgpr_write_b32 a0, v212\n"
"v_accvgpr_write_b32 a1, v213\n"
"v_accvgpr_write_b32 a2, v214\n"
"v_accvgpr_write_b32 a3, v215\n"
"v_accvgpr_write_b32 a4, v216\n"
"v_accvgpr_write_b32 a5, v217\n"
"v_accvgpr_write_b32 a6, v218\n"
"v_accvgpr_write_b32 a7, v219\n"
"v_accvgpr_write_b32 a8, v220\n"
"v_accvgpr_write_b32 a9, v221\n"
"v_accvgpr_write_b32 a10, v222\n"
"v_accvgpr_write_b32 a11, v223\n"
"v_accvgpr_write_b32 a12, v224\n"
"v_accvgpr_write_b32 a13, v225\n"
"v_accvgpr_write_b32 a14, v226\n"
"v_accvgpr_write_b32 a15, v227\n"
"v_mul_f32 v20, s85, v20\n"
"v_mul_f32 v21, s85, v21\n"
"v_mul_f32 v22, s85, v22\n"
"mA 40,72,144,40\n"
"v_mul_f32 v23, s85, v23\n"
"v_mul_f32 v36, s85, v36\n"
"v_mul_f32 v37, s85, v37\n"
"mA 40,76,148,40\n"
"v_mul_f32 v38, s85, v38\n"
"v_mul_f32 v39, s85, v39\n"
"cP 212,20,21\n"
"mA 56,80,144,56\n"
"cP 213,22,23\n"
"cP 214,36,37\n"
"cP 215,38,39\n"
"mA 56,84,148,56\n"
"v_mul_f32 v52, s85, v52\n"
"v_mul_f32 v53, s85, v53\n"
"v_mul_f32 v54, s85, v54\n"
"mA 72,88,144,72\n"
"v_mul_f32 v55, s85, v55\n"
"v_mul_f32 v68, s85, v68\n"
"v_mul_f32 v69, s85, v69\n"
"mA 72,92,148,72\n"
"v_mul_f32 v70, s85, v70\n"
"v_mul_f32 v71, s85, v71\n"
"cP 216,52,53\n"
"mA 88,96,144,88\n"
"cP 217,54,55\n"
"cP 218,68,69\n"
"cP 219,70,71\n"
"mA 88,100,148,88\n"
"v_mul_f32 v84, s85, v84\n"
"v_mul_f32 v85, s85, v85\n"
"v_mul_f32 v86, s85, v86\n"
"mA 104,104,144,104\n"
"v_mul_f32 v87, s85, v87\n"
"v_mul_f32 v100, s85, v100\n"
"v_mul_f32 v101, s85, v101\n"
"mA 104,108,148,104\n"
"v_mul_f32 v102, s85, v102\n"
"v_mul_f32 v103, s85, v103\n"
"cP 220,84,85\n"
"mA 120,112,144,120\n"
"cP 221,86,87\n"
"cP 222,100,101\n"
"cP 223,102,103\n"
"mA 120,116,148,120\n"
"v_mul_f32 v116, s85, v116\n"
"v_mul_f32 v117, s85, v117\n"
"v_mul_f32 v118, s85, v118\n"
"mA 136,120,144,136\n"
"v_mul_f32 v119, s85, v119\n"
"v_mul_f32 v132, s85, v132\n"
"v_mul_f32 v133, s85, v133\n"
"mA 136,124,148,136\n"
"v_mul_f32 v134, s85, v134\n"
"v_mul_f32 v135, s85, v135\n"
"cP 224,116,117\n"
"s_waitcnt vmcnt(17)\n"
"mA 28,64,152,28\n"
"cP 225,118,119\n"
"cP 226,132,133\n"
"cP 227,134,135\n"
"s_waitcnt vmcnt(16)\n"
"mA 28,68,156,28\n"
"v_permlane32_swap_b32 v212, v214\n"
"v_permlane32_swap_b32 v213, v215\n"
"v_permlane32_swap_b32 v216, v218\n"
"v_permlane32_swap_b32 v217, v219\n"
"v_permlane32_swap_b32 v220, v222\n"
"v_permlane32_swap_b32 v221, v223\n"
"v_permlane32_swap_b32 v224, v226\n"
"v_permlane32_swap_b32 v225, v227\n"
"v_permlane16_swap_b32 v212, v214\n"
"v_permlane16_swap_b32 v213, v215\n"
"v_permlane16_swap_b32 v216, v218\n"
"v_permlane16_swap_b32 v217, v219\n"
"v_permlane16_swap_b32 v220, v222\n"
"v_permlane16_swap_b32 v221, v223\n"
"v_permlane16_swap_b32 v224, v226\n"
"v_permlane16_swap_b32 v225, v227\n"
"v_accvgpr_write_b32 a16, v212\n"
"v_accvgpr_write_b32 a17, v213\n"
"v_accvgpr_write_b32 a18, v214\n"
"v_accvgpr_write_b32 a19, v215\n"
"v_accvgpr_write_b32 a20, v216\n"
"v_accvgpr_write_b32 a21, v217\n"
"v_accvgpr_write_b32 a22, v218\n"
"v_accvgpr_write_b32 a23, v219\n"
"v_accvgpr_write_b32 a24, v220\n"
"v_accvgpr_write_b32 a25, v221\n"
"v_accvgpr_write_b32 a26, v222\n"
"v_accvgpr_write_b32 a27, v223\n"
"v_accvgpr_write_b32 a28, v224\n"
"v_accvgpr_write_b32 a29, v225\n"
"v_accvgpr_write_b32 a30, v226\n"
"v_accvgpr_write_b32 a31, v227\n"
"v_mul_f32 v24, s85, v24\n"
"v_mul_f32 v25, s85, v25\n"
"v_mul_f32 v26, s85, v26\n"
"mA 44,72,152,44\n"
"v_mul_f32 v27, s85, v27\n"
"v_mul_f32 v40, s85, v40\n"
"v_mul_f32 v41, s85, v41\n"
"mA 44,76,156,44\n"
"v_mul_f32 v42, s85, v42\n"
"v_mul_f32 v43, s85, v43\n"
"cP 212,24,25\n"
"mA 60,80,152,60\n"
"cP 213,26,27\n"
"cP 214,40,41\n"
"cP 215,42,43\n"
"mA 60,84,156,60\n"
"v_mul_f32 v56, s85, v56\n"
"v_mul_f32 v57, s85, v57\n"
"v_mul_f32 v58, s85, v58\n"
"mA 76,88,152,76\n"
"v_mul_f32 v59, s85, v59\n"
"v_mul_f32 v72, s85, v72\n"
"v_mul_f32 v73, s85, v73\n"
"mA 76,92,156,76\n"
"v_mul_f32 v74, s85, v74\n"
"v_mul_f32 v75, s85, v75\n"
"cP 216,56,57\n"
"mA 92,96,152,92\n"
"cP 217,58,59\n"
"cP 218,72,73\n"
"cP 219,74,75\n"
"mA 92,100,156,92\n"
"v_mul_f32 v88, s85, v88\n"
"v_mul_f32 v89, s85, v89\n"
"v_mul_f32 v90, s85, v90\n"
"mA 108,104,152,108\n"
"v_mul_f32 v91, s85, v91\n"
"v_mul_f32 v104, s85, v104\n"
"v_mul_f32 v105, s85, v105\n"
"mA 108,108,156,108\n"
"v_mul_f32 v106, s85, v106\n"
"v_mul_f32 v107, s85, v107\n"
"cP 220,88,89\n"
"mA 124,112,152,124\n"
"cP 221,90,91\n"
"cP 222,104,105\n"
"cP 223,106,107\n"
"mA 124,116,156,124\n"
"v_mul_f32 v120, s85, v120\n"
"v_mul_f32 v121, s85, v121\n"
"v_mul_f32 v122, s85, v122\n"
"mA 140,120,152,140\n"
"v_mul_f32 v123, s85, v123\n"
"v_mul_f32 v136, s85, v136\n"
"v_mul_f32 v137, s85, v137\n"
"mA 140,124,156,140\n"
"v_mul_f32 v138, s85, v138\n"
"v_mul_f32 v139, s85, v139\n"
"cP 224,120,121\n"
"ds_read_b64_tr_b16 a[128:129], v3\n"
"dT 130,3,4096\n"
"ds_read_b64_tr_b16 a[132:133], v4\n"
"dT 134,4,4096\n"
"dT 136,3,256\n"
"dT 138,3,4352\n"
"dT 140,4,256\n"
"dT 142,4,4352\n"
"dT 144,3,512\n"
"dT 146,3,4608\n"
"dT 148,4,512\n"
"dT 150,4,4608\n"
"dT 152,3,768\n"
"dT 154,3,4864\n"
"dT 156,4,768\n"
"dT 158,4,4864\n"
"cP 225,122,123\n"
"cP 226,136,137\n"
"cP 227,138,139\n"
"v_permlane32_swap_b32 v212, v214\n"
"v_permlane32_swap_b32 v213, v215\n"
"v_permlane32_swap_b32 v216, v218\n"
"v_permlane32_swap_b32 v217, v219\n"
"v_permlane32_swap_b32 v220, v222\n"
"v_permlane32_swap_b32 v221, v223\n"
"v_permlane32_swap_b32 v224, v226\n"
"v_permlane32_swap_b32 v225, v227\n"
"v_permlane16_swap_b32 v212, v214\n"
"v_permlane16_swap_b32 v213, v215\n"
"v_permlane16_swap_b32 v216, v218\n"
"v_permlane16_swap_b32 v217, v219\n"
"v_permlane16_swap_b32 v220, v222\n"
"v_permlane16_swap_b32 v221, v223\n"
"v_permlane16_swap_b32 v224, v226\n"
"v_permlane16_swap_b32 v225, v227\n"
"v_accvgpr_write_b32 a32, v212\n"
"v_accvgpr_write_b32 a33, v213\n"
"v_accvgpr_write_b32 a34, v214\n"
"v_accvgpr_write_b32 a35, v215\n"
"v_accvgpr_write_b32 a36, v216\n"
"v_accvgpr_write_b32 a37, v217\n"
"v_accvgpr_write_b32 a38, v218\n"
"v_accvgpr_write_b32 a39, v219\n"
"v_accvgpr_write_b32 a40, v220\n"
"v_accvgpr_write_b32 a41, v221\n"
"v_accvgpr_write_b32 a42, v222\n"
"v_accvgpr_write_b32 a43, v223\n"
"v_accvgpr_write_b32 a44, v224\n"
"v_accvgpr_write_b32 a45, v225\n"
"v_accvgpr_write_b32 a46, v226\n"
"v_accvgpr_write_b32 a47, v227\n"
"v_mov_b32 v238, 0x3f803f80\n"
"s_waitcnt lgkmcnt(4)\n"
"mZ 144,192,0\n"
"v_mul_f32 v28, s85, v28\n"
"v_mul_f32 v29, s85, v29\n"
"v_mul_f32 v30, s85, v30\n"
"v_mul_f32 v31, s85, v31\n"
"mA 144,196,4,144\n"
"v_mul_f32 v44, s85, v44\n"
"v_mul_f32 v45, s85, v45\n"
"v_mul_f32 v46, s85, v46\n"
"v_mul_f32 v47, s85, v47\n"
"mA 144,200,8,144\n"
"cP 212,28,29\n"
"cP 213,30,31\n"
"cP 214,44,45\n"
"cP 215,46,47\n"
"mA 144,204,12,144\n"
"v_mul_f32 v60, s85, v60\n"
"v_mul_f32 v61, s85, v61\n"
"v_mul_f32 v62, s85, v62\n"
"v_mul_f32 v63, s85, v63\n"
"mZ 148,208,0\n"
"v_mul_f32 v76, s85, v76\n"
"v_mul_f32 v77, s85, v77\n"
"v_mul_f32 v78, s85, v78\n"
"v_mul_f32 v79, s85, v79\n"
"mA 148,212,4,148\n"
"cP 216,60,61\n"
"cP 217,62,63\n"
"cP 218,76,77\n"
"cP 219,78,79\n"
"mA 148,216,8,148\n"
"v_mul_f32 v92, s85, v92\n"
"v_mul_f32 v93, s85, v93\n"
"v_mul_f32 v94, s85, v94\n"
"v_mul_f32 v95, s85, v95\n"
"mA 148,220,12,148\n"
"v_mul_f32 v108, s85, v108\n"
"v_mul_f32 v109, s85, v109\n"
"v_mul_f32 v110, s85, v110\n"
"v_mul_f32 v111, s85, v111\n"
"mZ 152,192,16\n"
"cP 220,92,93\n"
"cP 221,94,95\n"
"cP 222,108,109\n"
"cP 223,110,111\n"
"mA 152,196,20,152\n"
"v_mul_f32 v124, s85, v124\n"
"v_mul_f32 v125, s85, v125\n"
"v_mul_f32 v126, s85, v126\n"
"v_mul_f32 v127, s85, v127\n"
"mA 152,200,24,152\n"
"v_mul_f32 v140, s85, v140\n"
"v_mul_f32 v141, s85, v141\n"
"v_mul_f32 v142, s85, v142\n"
"v_mul_f32 v143, s85, v143\n"
"mA 152,204,28,152\n"
"cP 224,124,125\n"
"cP 225,126,127\n"
"cP 226,140,141\n"
"cP 227,142,143\n"
"mZ 156,208,16\n"
"v_permlane32_swap_b32 v212, v214\n"
"v_permlane32_swap_b32 v213, v215\n"
"v_permlane32_swap_b32 v216, v218\n"
"v_permlane32_swap_b32 v217, v219\n"
"mA 156,212,20,156\n"
"v_permlane32_swap_b32 v220, v222\n"
"v_permlane32_swap_b32 v221, v223\n"
"v_permlane32_swap_b32 v224, v226\n"
"v_permlane32_swap_b32 v225, v227\n"
"mA 156,216,24,156\n"
"v_permlane16_swap_b32 v212, v214\n"
"v_permlane16_swap_b32 v213, v215\n"
"v_permlane16_swap_b32 v216, v218\n"
"v_permlane16_swap_b32 v217, v219\n"
"mA 156,220,28,156\n"
"v_permlane16_swap_b32 v220, v222\n"
"v_permlane16_swap_b32 v221, v223\n"
"v_permlane16_swap_b32 v224, v226\n"
"v_permlane16_swap_b32 v225, v227\n"
"mZ 160,192,32\n"
"v_accvgpr_write_b32 a48, v212\n"
"v_accvgpr_write_b32 a49, v213\n"
"v_accvgpr_write_b32 a50, v214\n"
"v_accvgpr_write_b32 a51, v215\n"
"mA 160,196,36,160\n"
"v_accvgpr_write_b32 a52, v216\n"
"v_accvgpr_write_b32 a53, v217\n"
"v_accvgpr_write_b32 a54, v218\n"
"v_accvgpr_write_b32 a55, v219\n"
"mA 160,200,40,160\n"
"v_accvgpr_write_b32 a56, v220\n"
"v_accvgpr_write_b32 a57, v221\n"
"v_accvgpr_write_b32 a58, v222\n"
"v_accvgpr_write_b32 a59, v223\n"
"mA 160,204,44,160\n"
"v_accvgpr_write_b32 a60, v224\n"
"v_accvgpr_write_b32 a61, v225\n"
"v_accvgpr_write_b32 a62, v226\n"
"v_accvgpr_write_b32 a63, v227\n"
"mZ 164,208,32\n"
"mA 164,212,36,164\n"
"mA 164,216,40,164\n"
"mA 164,220,44,164\n"
"mZ 168,192,48\n"
"v_max3_f32 v228, v144, v145, v146\n"
"v_max3_f32 v229, v147, v148, v149\n"
"v_max3_f32 v228, v228, v150, v151\n"
"mA 168,196,52,168\n"
"v_max_f32 v228, v228, v229\n"
"v_mov_b32 v229, v228\n"
"v_accvgpr_write_b32 a64, v238\n"
"mA 168,200,56,168\n"
"v_permlane32_swap_b32 v228, v229\n"
"v_max_f32 v228, v228, v229\n"
"v_mov_b32 v229, v228\n"
"mA 168,204,60,168\n"
"s_nop 0\n"
"v_permlane16_swap_b32 v228, v229\n"
"v_max_f32 v228, v228, v229\n"
"v_sub_f32 v144, v144, v228\n"
"mZ 172,208,48\n"
"v_sub_f32 v145, v145, v228\n"
"v_sub_f32 v146, v146, v228\n"
"v_sub_f32 v147, v147, v228\n"
"mA 172,212,52,172\n"
"v_sub_f32 v148, v148, v228\n"
"v_sub_f32 v149, v149, v228\n"
"v_sub_f32 v150, v150, v228\n"
"mA 172,216,56,172\n"
"v_sub_f32 v151, v151, v228\n"
"v_sub_f32 v192, 0, v228\n"
"v_sub_f32 v193, 0, v228\n"
"mA 172,220,60,172\n"
"v_sub_f32 v194, 0, v228\n"
"v_sub_f32 v195, 0, v228\n"
"v_max3_f32 v230, v152, v153, v154\n"
"v_max3_f32 v231, v155, v156, v157\n"
"v_max3_f32 v230, v230, v158, v159\n"
"v_max_f32 v230, v230, v231\n"
"v_mov_b32 v231, v230\n"
"v_accvgpr_write_b32 a65, v238\n"
"s_nop 0\n"
"v_permlane32_swap_b32 v230, v231\n"
"v_max_f32 v230, v230, v231\n"
"v_mov_b32 v231, v230\n"
"s_nop 1\n"
"v_permlane16_swap_b32 v230, v231\n"
"v_max_f32 v230, v230, v231\n"
"v_sub_f32 v152, v152, v230\n"
"v_sub_f32 v153, v153, v230\n"
"v_sub_f32 v154, v154, v230\n"
"v_sub_f32 v155, v155, v230\n"
"v_sub_f32 v156, v156, v230\n"
"v_sub_f32 v157, v157, v230\n"
"v_sub_f32 v158, v158, v230\n"
"v_sub_f32 v159, v159, v230\n"
"v_sub_f32 v196, 0, v230\n"
"v_sub_f32 v197, 0, v230\n"
"v_sub_f32 v198, 0, v230\n"
"v_sub_f32 v199, 0, v230\n"
"v_max3_f32 v232, v160, v161, v162\n"
"v_max3_f32 v233, v163, v164, v165\n"
"v_max3_f32 v232, v232, v166, v167\n"
"v_max_f32 v232, v232, v233\n"
"v_mov_b32 v233, v232\n"
"v_accvgpr_write_b32 a66, v238\n"
"s_nop 0\n"
"v_permlane32_swap_b32 v232, v233\n"
"v_max_f32 v232, v232, v233\n"
"v_mov_b32 v233, v232\n"
"s_nop 1\n"
"v_permlane16_swap_b32 v232, v233\n"
"v_max_f32 v232, v232, v233\n"
"v_sub_f32 v160, v160, v232\n"
"v_sub_f32 v161, v161, v232\n"
"v_sub_f32 v162, v162, v232\n"
"v_sub_f32 v163, v163, v232\n"
"v_sub_f32 v164, v164, v232\n"
"v_sub_f32 v165, v165, v232\n"
"v_sub_f32 v166, v166, v232\n"
"v_sub_f32 v167, v167, v232\n"
"v_sub_f32 v200, 0, v232\n"
"v_sub_f32 v201, 0, v232\n"
"v_sub_f32 v202, 0, v232\n"
"v_sub_f32 v203, 0, v232\n"
"v_max3_f32 v234, v168, v169, v170\n"
"v_max3_f32 v235, v171, v172, v173\n"
"v_max3_f32 v234, v234, v174, v175\n"
"v_max_f32 v234, v234, v235\n"
"v_mov_b32 v235, v234\n"
"v_accvgpr_write_b32 a67, v238\n"
"s_nop 0\n"
"v_permlane32_swap_b32 v234, v235\n"
"v_max_f32 v234, v234, v235\n"
"v_mov_b32 v235, v234\n"
"s_nop 1\n"
"v_permlane16_swap_b32 v234, v235\n"
"v_max_f32 v234, v234, v235\n"
"v_sub_f32 v168, v168, v234\n"
"v_sub_f32 v169, v169, v234\n"
"v_sub_f32 v170, v170, v234\n"
"v_sub_f32 v171, v171, v234\n"
"v_sub_f32 v172, v172, v234\n"
"v_sub_f32 v173, v173, v234\n"
"v_sub_f32 v174, v174, v234\n"
"v_sub_f32 v175, v175, v234\n"
"v_sub_f32 v204, 0, v234\n"
"v_sub_f32 v205, 0, v234\n"
"v_sub_f32 v206, 0, v234\n"
"v_sub_f32 v207, 0, v234\n"
"eX 144,144\n"
"eX 145,145\n"
"s_nop 0\n"
"cP 176,144,145\n"
"eX 146,146\n"
"eX 147,147\n"
"s_nop 0\n"
"cP 177,146,147\n"
"eX 148,148\n"
"eX 149,149\n"
"s_nop 0\n"
"cP 178,148,149\n"
"eX 150,150\n"
"eX 151,151\n"
"s_nop 0\n"
"cP 179,150,151\n"
"eX 152,152\n"
"eX 153,153\n"
"dT 160,3,8192\n"
"cP 180,152,153\n"
"eX 154,154\n"
"dT 162,3,12288\n"
"eX 155,155\n"
"s_nop 0\n"
"cP 181,154,155\n"
"v_mfma_f32_16x16x32_bf16 v[16:19], a[128:131], v[176:179], 0\n"
"eX 156,156\n"
"v_mfma_f32_16x16x32_bf16 v[32:35], a[132:135], v[176:179], 0\n"
"v_mfma_f32_16x16x32_bf16 v[48:51], a[136:139], v[176:179], 0\n"
"eX 157,157\n"
"v_mfma_f32_16x16x32_bf16 v[64:67], a[140:143], v[176:179], 0\n"
"dT 164,4,8192\n"
"cP 182,156,157\n"
"v_mfma_f32_16x16x32_bf16 v[80:83], a[144:147], v[176:179], 0\n"
"eX 158,158\n"
"v_mfma_f32_16x16x32_bf16 v[96:99], a[148:151], v[176:179], 0\n"
"dT 166,4,12288\n"
"s_waitcnt lgkmcnt(4)\n"
"v_mfma_f32_16x16x32_bf16 v[112:115], a[152:155], v[176:179], 0\n"
"eX 159,159\n"
"v_mfma_f32_16x16x32_bf16 v[128:131], a[156:159], v[176:179], 0\n"
"cP 183,158,159\n"
"v_mfma_f32_16x16x32_bf16 v[208:211], a[64:67], v[176:179], 0\n"
"eX 160,160\n"
"eX 161,161\n"
"dT 168,3,8448\n"
"cP 184,160,161\n"
"eX 162,162\n"
"dT 170,3,12544\n"
"eX 163,163\n"
"s_nop 0\n"
"cP 185,162,163\n"
"v_mfma_f32_16x16x32_bf16 v[20:23], a[128:131], v[180:183], 0\n"
"s_waitcnt vmcnt(8)\n"
"s_barrier\n"
"eX 164,164\n"
"v_mfma_f32_16x16x32_bf16 v[36:39], a[132:135], v[180:183], 0\n"
"dK 192,2,16384\n"
"v_mfma_f32_16x16x32_bf16 v[52:55], a[136:139], v[180:183], 0\n"
"eX 165,165\n"
"v_mfma_f32_16x16x32_bf16 v[68:71], a[140:143], v[180:183], 0\n"
"dT 172,4,8448\n"
"cP 186,164,165\n"
"v_mfma_f32_16x16x32_bf16 v[84:87], a[144:147], v[180:183], 0\n"
"eX 166,166\n"
"v_mfma_f32_16x16x32_bf16 v[100:103], a[148:151], v[180:183], 0\n"
"dT 174,4,12544\n"
"v_mfma_f32_16x16x32_bf16 v[116:119], a[152:155], v[180:183], 0\n"
"eX 167,167\n"
"v_mfma_f32_16x16x32_bf16 v[132:135], a[156:159], v[180:183], 0\n"
"s_mov_b32 m0, s76\n"
"s_nop 0\n"
"buffer_load_dwordx4 v5, s[40:43], s81 offen lds\n"
"cP 187,166,167\n"
"v_mfma_f32_16x16x32_bf16 v[212:215], a[64:67], v[180:183], 0\n"
"mA 144,224,0,192\n"
"eX 168,168\n"
"mA 144,228,4,144\n"
"dK 196,2,16640\n"
"mA 144,232,8,144\n"
"eX 169,169\n"
"mA 144,236,12,144\n"
"dT 176,3,8704\n"
"cP 188,168,169\n"
"mA 148,240,0,192\n"
"eX 170,170\n"
"mA 148,244,4,148\n"
"dT 178,3,12800\n"
"mA 148,248,8,148\n"
"eX 171,171\n"
"mA 148,252,12,148\n"
"cP 189,170,171\n"
"v_mfma_f32_16x16x32_bf16 v[24:27], a[128:131], v[184:187], 0\n"
"eX 172,172\n"
"v_mfma_f32_16x16x32_bf16 v[40:43], a[132:135], v[184:187], 0\n"
"dK 200,2,16896\n"
"v_mfma_f32_16x16x32_bf16 v[56:59], a[136:139], v[184:187], 0\n"
"eX 173,173\n"
"v_mfma_f32_16x16x32_bf16 v[72:75], a[140:143], v[184:187], 0\n"
"dT 180,4,8704\n"
"cP 190,172,173\n"
"v_mfma_f32_16x16x32_bf16 v[88:91], a[144:147], v[184:187], 0\n"
"eX 174,174\n"
"v_mfma_f32_16x16x32_bf16 v[104:107], a[148:151], v[184:187], 0\n"
"dT 182,4,12800\n"
"v_mfma_f32_16x16x32_bf16 v[120:123], a[152:155], v[184:187], 0\n"
"eX 175,175\n"
"v_mfma_f32_16x16x32_bf16 v[136:139], a[156:159], v[184:187], 0\n"
"buffer_load_dwordx4 v5, s[40:43], s81 offen offset:1024 lds\n"
"cP 191,174,175\n"
"v_mfma_f32_16x16x32_bf16 v[216:219], a[64:67], v[184:187], 0\n"
"mA 152,224,16,196\n"
"eX 144,144\n"
"mA 152,228,20,152\n"
"dK 204,2,17152\n"
"mA 152,232,24,152\n"
"eX 145,145\n"
"mA 152,236,28,152\n"
"dT 184,3,8960\n"
"cP 176,144,145\n"
"mA 156,240,16,196\n"
"eX 146,146\n"
"mA 156,244,20,156\n"
"dT 186,3,13056\n"
"mA 156,248,24,156\n"
"eX 147,147\n"
"mA 156,252,28,156\n"
"cP 177,146,147\n"
"v_mfma_f32_16x16x32_bf16 v[28:31], a[128:131], v[188:191], 0\n"
"eX 148,148\n"
"v_mfma_f32_16x16x32_bf16 v[44:47], a[132:135], v[188:191], 0\n"
"dK 208,2,20480\n"
"v_mfma_f32_16x16x32_bf16 v[60:63], a[136:139], v[188:191], 0\n"
"eX 149,149\n"
"v_mfma_f32_16x16x32_bf16 v[76:79], a[140:143], v[188:191], 0\n"
"dT 188,4,8960\n"
"cP 178,148,149\n"
"v_mfma_f32_16x16x32_bf16 v[92:95], a[144:147], v[188:191], 0\n"
"eX 150,150\n"
"v_mfma_f32_16x16x32_bf16 v[108:111], a[148:151], v[188:191], 0\n"
"dT 190,4,13056\n"
"v_mfma_f32_16x16x32_bf16 v[124:127], a[152:155], v[188:191], 0\n"
"eX 151,151\n"
"v_mfma_f32_16x16x32_bf16 v[140:143], a[156:159], v[188:191], 0\n"
"buffer_load_dwordx4 v6, s[40:43], s81 offen offset:2048 lds\n"
"cP 179,150,151\n"
"v_mfma_f32_16x16x32_bf16 v[220:223], a[64:67], v[188:191], 0\n"
"mA 160,224,32,200\n"
"eX 152,152\n"
"mA 160,228,36,160\n"
"dK 212,2,20736\n"
"mA 160,232,40,160\n"
"eX 153,153\n"
"mA 160,236,44,160\n"
"dT 128,3,16384\n"
"cP 180,152,153\n"
"mA 164,240,32,200\n"
"eX 154,154\n"
"mA 164,244,36,164\n"
"dT 130,3,20480\n"
"mA 164,248,40,164\n"
"eX 155,155\n"
"mA 164,252,44,164\n"
"cP 181,154,155\n"
"s_waitcnt lgkmcnt(6)\n"
"mB 16,160,176,16\n"
"eX 156,156\n"
"mB 32,164,176,32\n"
"dK 216,2,20992\n"
"mB 48,168,176,48\n"
"eX 157,157\n"
"mB 64,172,176,64\n"
"dT 132,4,16384\n"
"cP 182,156,157\n"
"mB 80,176,176,80\n"
"eX 158,158\n"
"mB 96,180,176,96\n"
"dT 134,4,20480\n"
"mB 112,184,176,112\n"
"eX 159,159\n"
"s_waitcnt lgkmcnt(6)\n"
"mB 128,188,176,128\n"
"buffer_load_dwordx4 v6, s[40:43], s81 offen offset:3072 lds\n"
"cP 183,158,159\n"
"mB 208,64,176,208\n"
"s_mov_b32 s82, 7\n"
"L_loop_%=:\n"
"mA 168,224,48,204\n"
"eX 160,160\n"
"mA 168,228,52,168\n"
"dK 220,2,21248\n"
"mA 168,232,56,168\n"
"eX 161,161\n"
"mA 168,236,60,168\n"
"dT 136,3,16640\n"
"cP 184,160,161\n"
"mA 172,240,48,204\n"
"eX 162,162\n"
"mA 172,244,52,172\n"
"dT 138,3,20736\n"
"mA 172,248,56,172\n"
"eX 163,163\n"
"mA 172,252,60,172\n"
"cP 185,162,163\n"
"mB 20,160,180,20\n"
"eX 164,164\n"
"mB 36,164,180,36\n"
"dK 224,2,24576\n"
"mB 52,168,180,52\n"
"eX 165,165\n"
"mB 68,172,180,68\n"
"dT 140,4,16640\n"
"cP 186,164,165\n"
"mB 84,176,180,84\n"
"eX 166,166\n"
"mB 100,180,180,100\n"
"dT 142,4,20736\n"
"mB 116,184,180,116\n"
"eX 167,167\n"
"mB 132,188,180,132\n"
"s_mov_b32 m0, s80\n"
"s_nop 0\n"
"buffer_load_dwordx4 v5, s[44:47], s81 offen lds\n"
"cP 187,166,167\n"
"mB 212,64,180,212\n"
"mA 144,192,0,192\n"
"eX 168,168\n"
"mA 144,196,4,144\n"
"dK 228,2,24832\n"
"mA 144,200,8,144\n"
"eX 169,169\n"
"mA 144,204,12,144\n"
"dT 144,3,16896\n"
"cP 188,168,169\n"
"mA 148,208,0,192\n"
"eX 170,170\n"
"s_waitcnt lgkmcnt(6)\n"
"mA 148,212,4,148\n"
"dT 146,3,20992\n"
"mA 148,216,8,148\n"
"eX 171,171\n"
"mA 148,220,12,148\n"
"cP 189,170,171\n"
"mB 24,160,184,24\n"
"eX 172,172\n"
"mB 40,164,184,40\n"
"dK 232,2,25088\n"
"mB 56,168,184,56\n"
"eX 173,173\n"
"mB 72,172,184,72\n"
"dT 148,4,16896\n"
"cP 190,172,173\n"
"mB 88,176,184,88\n"
"eX 174,174\n"
"mB 104,180,184,104\n"
"dT 150,4,20992\n"
"mB 120,184,184,120\n"
"eX 175,175\n"
"mB 136,188,184,136\n"
"buffer_load_dwordx4 v6, s[44:47], s81 offen offset:1024 lds\n"
"cP 191,174,175\n"
"mB 216,64,184,216\n"
"mA 152,192,16,196\n"
"eX 144,144\n"
"mA 152,196,20,152\n"
"dK 236,2,25344\n"
"mA 152,200,24,152\n"
"eX 145,145\n"
"mA 152,204,28,152\n"
"dT 152,3,17152\n"
"cP 176,144,145\n"
"mA 156,208,16,196\n"
"eX 146,146\n"
"mA 156,212,20,156\n"
"dT 154,3,21248\n"
"mA 156,216,24,156\n"
"eX 147,147\n"
"mA 156,220,28,156\n"
"cP 177,146,147\n"
"mB 28,160,188,28\n"
"eX 148,148\n"
"mB 44,164,188,44\n"
"dK 240,2,28672\n"
"mB 60,168,188,60\n"
"eX 149,149\n"
"mB 76,172,188,76\n"
"dT 156,4,17152\n"
"cP 178,148,149\n"
"mB 92,176,188,92\n"
"eX 150,150\n"
"mB 108,180,188,108\n"
"dT 158,4,21248\n"
"mB 124,184,188,124\n"
"eX 151,151\n"
"mB 140,188,188,140\n"
"buffer_load_dwordx4 v5, s[44:47], s81 offen offset:2048 lds\n"
"cP 179,150,151\n"
"mB 220,64,188,220\n"
"mA 160,192,32,200\n"
"eX 152,152\n"
"mA 160,196,36,160\n"
"dK 244,2,28928\n"
"mA 160,200,40,160\n"
"eX 153,153\n"
"mA 160,204,44,160\n"
"dT 160,3,24576\n"
"cP 180,152,153\n"
"mA 164,208,32,200\n"
"eX 154,154\n"
"mA 164,212,36,164\n"
"dT 162,3,28672\n"
"mA 164,216,40,164\n"
"eX 155,155\n"
"mA 164,220,44,164\n"
"cP 181,154,155\n"
"mB 16,128,176,16\n"
"eX 156,156\n"
"mB 32,132,176,32\n"
"dK 248,2,29184\n"
"s_waitcnt lgkmcnt(7)\n"
"mB 48,136,176,48\n"
"eX 157,157\n"
"mB 64,140,176,64\n"
"dT 164,4,24576\n"
"cP 182,156,157\n"
"mB 80,144,176,80\n"
"eX 158,158\n"
"mB 96,148,176,96\n"
"dT 166,4,28672\n"
"mB 112,152,176,112\n"
"eX 159,159\n"
"s_waitcnt lgkmcnt(6)\n"
"mB 128,156,176,128\n"
"buffer_load_dwordx4 v6, s[44:47], s81 offen offset:3072 lds\n"
"s_add_u32 s81, s81, 16384\n"
"cP 183,158,159\n"
"mB 208,64,176,208\n"
"mA 168,192,48,204\n"
"eX 160,160\n"
"mA 168,196,52,168\n"
"dK 252,2,29440\n"
"mA 168,200,56,168\n"
"eX 161,161\n"
"mA 168,204,60,168\n"
"dT 168,3,24832\n"
"cP 184,160,161\n"
"mA 172,208,48,204\n"
"eX 162,162\n"
"mA 172,212,52,172\n"
"dT 170,3,28928\n"
"mA 172,216,56,172\n"
"eX 163,163\n"
"mA 172,220,60,172\n"
"s_mov_b32 s52, s36\n"
"s_mov_b32 s53, s37\n"
"s_mov_b32 s54, s38\n"
"s_mov_b32 s36, s66\n"
"s_mov_b32 s37, s67\n"
"s_mov_b32 s38, s39\n"
"s_mov_b32 s66, s68\n"
"s_mov_b32 s67, s69\n"
"s_mov_b32 s39, s70\n"
"s_mov_b32 s70, 0\n"
"s_add_u32 m0, s57, 156672\n"
"s_nop 0\n"
"buffer_load_dwordx4 v15, s[52:55], s93 offen lds\n"
"cP 185,162,163\n"
"mB 20,128,180,20\n"
"s_waitcnt vmcnt(9)\n"
"s_barrier\n"
"eX 164,164\n"
"mB 36,132,180,36\n"
"dK 192,2,32768\n"
"mB 52,136,180,52\n"
"eX 165,165\n"
"mB 68,140,180,68\n"
"dT 172,4,24832\n"
"cP 186,164,165\n"
"mB 84,144,180,84\n"
"eX 166,166\n"
"mB 100,148,180,100\n"
"dT 174,4,28928\n"
"mB 116,152,180,116\n"
"eX 167,167\n"
"mB 132,156,180,132\n"
"s_mov_b32 m0, s73\n"
"s_nop 0\n"
"buffer_load_dwordx4 v5, s[40:43], s81 offen lds\n"
"cP 187,166,167\n"
"mB 212,64,180,212\n"
"mA 144,224,0,192\n"
"eX 168,168\n"
"mA 144,228,4,144\n"
"dK 196,2,33024\n"
"mA 144,232,8,144\n"
"eX 169,169\n"
"mA 144,236,12,144\n"
"dT 176,3,25088\n"
"cP 188,168,169\n"
"mA 148,240,0,192\n"
"eX 170,170\n"
"s_waitcnt lgkmcnt(5)\n"
"mA 148,244,4,148\n"
"dT 178,3,29184\n"
"mA 148,248,8,148\n"
"eX 171,171\n"
"mA 148,252,12,148\n"
"cP 189,170,171\n"
"mB 24,128,184,24\n"
"eX 172,172\n"
"mB 40,132,184,40\n"
"dK 200,2,33280\n"
"mB 56,136,184,56\n"
"eX 173,173\n"
"mB 72,140,184,72\n"
"dT 180,4,25088\n"
"cP 190,172,173\n"
"mB 88,144,184,88\n"
"eX 174,174\n"
"mB 104,148,184,104\n"
"dT 182,4,29184\n"
"mB 120,152,184,120\n"
"eX 175,175\n"
"mB 136,156,184,136\n"
"buffer_load_dwordx4 v5, s[40:43], s81 offen offset:1024 lds\n"
"cP 191,174,175\n"
"mB 216,64,184,216\n"
"mA 152,224,16,196\n"
"eX 144,144\n"
"mA 152,228,20,152\n"
"dK 204,2,33536\n"
"mA 152,232,24,152\n"
"eX 145,145\n"
"mA 152,236,28,152\n"
"dT 184,3,25344\n"
"cP 176,144,145\n"
"mA 156,240,16,196\n"
"eX 146,146\n"
"mA 156,244,20,156\n"
"dT 186,3,29440\n"
"mA 156,248,24,156\n"
"eX 147,147\n"
"mA 156,252,28,156\n"
"cP 177,146,147\n"
"mB 28,128,188,28\n"
"eX 148,148\n"
"mB 44,132,188,44\n"
"dK 208,2,36864\n"
"mB 60,136,188,60\n"
"eX 149,149\n"
"mB 76,140,188,76\n"
"dT 188,4,25344\n"
"cP 178,148,149\n"
"mB 92,144,188,92\n"
"eX 150,150\n"
"mB 108,148,188,108\n"
"dT 190,4,29440\n"
"mB 124,152,188,124\n"
"eX 151,151\n"
"mB 140,156,188,140\n"
"buffer_load_dwordx4 v6, s[40:43], s81 offen offset:2048 lds\n"
"cP 179,150,151\n"
"mB 220,64,188,220\n"
"mA 160,224,32,200\n"
"eX 152,152\n"
"mA 160,228,36,160\n"
"dK 212,2,37120\n"
"mA 160,232,40,160\n"
"eX 153,153\n"
"mA 160,236,44,160\n"
"dT 128,3,32768\n"
"cP 180,152,153\n"
"mA 164,240,32,200\n"
"eX 154,154\n"
"mA 164,244,36,164\n"
"dT 130,3,36864\n"
"mA 164,248,40,164\n"
"eX 155,155\n"
"mA 164,252,44,164\n"
"cP 181,154,155\n"
"mB 16,160,176,16\n"
"eX 156,156\n"
"mB 32,164,176,32\n"
"dK 216,2,37376\n"
"mB 48,168,176,48\n"
"eX 157,157\n"
"s_waitcnt lgkmcnt(7)\n"
"mB 64,172,176,64\n"
"dT 132,4,32768\n"
"cP 182,156,157\n"
"mB 80,176,176,80\n"
"eX 158,158\n"
"mB 96,180,176,96\n"
"dT 134,4,36864\n"
"mB 112,184,176,112\n"
"eX 159,159\n"
"s_waitcnt lgkmcnt(6)\n"
"mB 128,188,176,128\n"
"buffer_load_dwordx4 v6, s[40:43], s81 offen offset:3072 lds\n"
"cP 183,158,159\n"
"mB 208,64,176,208\n"
"mA 168,224,48,204\n"
"eX 160,160\n"
"mA 168,228,52,168\n"
"dK 220,2,37632\n"
"mA 168,232,56,168\n"
"eX 161,161\n"
"mA 168,236,60,168\n"
"dT 136,3,33024\n"
"cP 184,160,161\n"
"mA 172,240,48,204\n"
"eX 162,162\n"
"mA 172,244,52,172\n"
"dT 138,3,37120\n"
"mA 172,248,56,172\n"
"eX 163,163\n"
"mA 172,252,60,172\n"
"s_add_u32 m0, s57, 156672\n"
"s_nop 0\n"
"buffer_load_dwordx4 v15, s[52:55], s71 offen lds\n"
"cP 185,162,163\n"
"mB 20,160,180,20\n"
"eX 164,164\n"
"mB 36,164,180,36\n"
"dK 224,2,40960\n"
"mB 52,168,180,52\n"
"eX 165,165\n"
"mB 68,172,180,68\n"
"dT 140,4,33024\n"
"cP 186,164,165\n"
"mB 84,176,180,84\n"
"eX 166,166\n"
"mB 100,180,180,100\n"
"dT 142,4,37120\n"
"mB 116,184,180,116\n"
"eX 167,167\n"
"mB 132,188,180,132\n"
"s_mov_b32 m0, s77\n"
"s_nop 0\n"
"buffer_load_dwordx4 v5, s[44:47], s81 offen lds\n"
"cP 187,166,167\n"
"mB 212,64,180,212\n"
"mA 144,192,0,192\n"
"eX 168,168\n"
"mA 144,196,4,144\n"
"dK 228,2,41216\n"
"mA 144,200,8,144\n"
"eX 169,169\n"
"mA 144,204,12,144\n"
"dT 144,3,33280\n"
"cP 188,168,169\n"
"mA 148,208,0,192\n"
"eX 170,170\n"
"s_waitcnt lgkmcnt(6)\n"
"mA 148,212,4,148\n"
"dT 146,3,37376\n"
"mA 148,216,8,148\n"
"eX 171,171\n"
"mA 148,220,12,148\n"
"cP 189,170,171\n"
"mB 24,160,184,24\n"
"eX 172,172\n"
"mB 40,164,184,40\n"
"dK 232,2,41472\n"
"mB 56,168,184,56\n"
"eX 173,173\n"
"mB 72,172,184,72\n"
"dT 148,4,33280\n"
"cP 190,172,173\n"
"mB 88,176,184,88\n"
"eX 174,174\n"
"mB 104,180,184,104\n"
"dT 150,4,37376\n"
"mB 120,184,184,120\n"
"eX 175,175\n"
"mB 136,188,184,136\n"
"buffer_load_dwordx4 v6, s[44:47], s81 offen offset:1024 lds\n"
"cP 191,174,175\n"
"mB 216,64,184,216\n"
"mA 152,192,16,196\n"
"eX 144,144\n"
"mA 152,196,20,152\n"
"dK 236,2,41728\n"
"mA 152,200,24,152\n"
"eX 145,145\n"
"mA 152,204,28,152\n"
"dT 152,3,33536\n"
"cP 176,144,145\n"
"mA 156,208,16,196\n"
"eX 146,146\n"
"mA 156,212,20,156\n"
"dT 154,3,37632\n"
"mA 156,216,24,156\n"
"eX 147,147\n"
"mA 156,220,28,156\n"
"cP 177,146,147\n"
"mB 28,160,188,28\n"
"eX 148,148\n"
"mB 44,164,188,44\n"
"dK 240,2,45056\n"
"mB 60,168,188,60\n"
"eX 149,149\n"
"mB 76,172,188,76\n"
"dT 156,4,33536\n"
"cP 178,148,149\n"
"mB 92,176,188,92\n"
"eX 150,150\n"
"mB 108,180,188,108\n"
"dT 158,4,37632\n"
"mB 124,184,188,124\n"
"eX 151,151\n"
"mB 140,188,188,140\n"
"buffer_load_dwordx4 v5, s[44:47], s81 offen offset:2048 lds\n"
"cP 179,150,151\n"
"mB 220,64,188,220\n"
"mA 160,192,32,200\n"
"eX 152,152\n"
"mA 160,196,36,160\n"
"dK 244,2,45312\n"
"mA 160,200,40,160\n"
"eX 153,153\n"
"mA 160,204,44,160\n"
"dT 160,3,40960\n"
"cP 180,152,153\n"
"mA 164,208,32,200\n"
"eX 154,154\n"
"mA 164,212,36,164\n"
"dT 162,3,45056\n"
"mA 164,216,40,164\n"
"eX 155,155\n"
"mA 164,220,44,164\n"
"cP 181,154,155\n"
"mB 16,128,176,16\n"
"eX 156,156\n"
"mB 32,132,176,32\n"
"dK 248,2,45568\n"
"s_waitcnt lgkmcnt(7)\n"
"mB 48,136,176,48\n"
"eX 157,157\n"
"mB 64,140,176,64\n"
"dT 164,4,40960\n"
"cP 182,156,157\n"
"mB 80,144,176,80\n"
"eX 158,158\n"
"mB 96,148,176,96\n"
"dT 166,4,45056\n"
"mB 112,152,176,112\n"
"eX 159,159\n"
"s_waitcnt lgkmcnt(6)\n"
"mB 128,156,176,128\n"
"buffer_load_dwordx4 v6, s[44:47], s81 offen offset:3072 lds\n"
"s_add_u32 s81, s81, 16384\n"
"cP 183,158,159\n"
"mB 208,64,176,208\n"
"mA 168,192,48,204\n"
"eX 160,160\n"
"mA 168,196,52,168\n"
"dK 252,2,45824\n"
"mA 168,200,56,168\n"
"eX 161,161\n"
"mA 168,204,60,168\n"
"dT 168,3,41216\n"
"cP 184,160,161\n"
"mA 172,208,48,204\n"
"eX 162,162\n"
"mA 172,212,52,172\n"
"dT 170,3,45312\n"
"mA 172,216,56,172\n"
"eX 163,163\n"
"mA 172,220,60,172\n"
"s_add_u32 s52, %[wslo], 26214400\n"
"s_addc_u32 s53, %[wshi], 0\n"
"s_and_b32 s53, s53, 0xffff\n"
"s_mov_b32 s54, 18874368\n"
"s_mov_b32 s55, 0x20000\n"
"s_add_u32 m0, s57, 156672\n"
"s_nop 0\n"
"buffer_load_dwordx4 v7, s[52:55], s83 offen lds\n"
"s_add_u32 s83, s83, 1048576\n"
"cP 185,162,163\n"
"mB 20,128,180,20\n"
"s_waitcnt vmcnt(11)\n"
"s_barrier\n"
"eX 164,164\n"
"mB 36,132,180,36\n"
"dK 192,2,49152\n"
"mB 52,136,180,52\n"
"eX 165,165\n"
"mB 68,140,180,68\n"
"dT 172,4,41216\n"
"cP 186,164,165\n"
"mB 84,144,180,84\n"
"eX 166,166\n"
"mB 100,148,180,100\n"
"dT 174,4,45312\n"
"mB 116,152,180,116\n"
"eX 167,167\n"
"mB 132,156,180,132\n"
"s_mov_b32 m0, s74\n"
"s_nop 0\n"
"buffer_load_dwordx4 v5, s[40:43], s81 offen lds\n"
"cP 187,166,167\n"
"mB 212,64,180,212\n"
"mA 144,224,0,192\n"
"eX 168,168\n"
"mA 144,228,4,144\n"
"dK 196,2,49408\n"
"mA 144,232,8,144\n"
"eX 169,169\n"
"mA 144,236,12,144\n"
"dT 176,3,41472\n"
"cP 188,168,169\n"
"mA 148,240,0,192\n"
"eX 170,170\n"
"s_waitcnt lgkmcnt(5)\n"
"mA 148,244,4,148\n"
"dT 178,3,45568\n"
"mA 148,248,8,148\n"
"eX 171,171\n"
"mA 148,252,12,148\n"
"cP 189,170,171\n"
"mB 24,128,184,24\n"
"eX 172,172\n"
"mB 40,132,184,40\n"
"dK 200,2,49664\n"
"mB 56,136,184,56\n"
"eX 173,173\n"
"mB 72,140,184,72\n"
"dT 180,4,41472\n"
"cP 190,172,173\n"
"mB 88,144,184,88\n"
"eX 174,174\n"
"mB 104,148,184,104\n"
"dT 182,4,45568\n"
"mB 120,152,184,120\n"
"eX 175,175\n"
"mB 136,156,184,136\n"
"buffer_load_dwordx4 v5, s[40:43], s81 offen offset:1024 lds\n"
"cP 191,174,175\n"
"mB 216,64,184,216\n"
"mA 152,224,16,196\n"
"eX 144,144\n"
"mA 152,228,20,152\n"
"dK 204,2,49920\n"
"mA 152,232,24,152\n"
"eX 145,145\n"
"mA 152,236,28,152\n"
"dT 184,3,41728\n"
"cP 176,144,145\n"
"mA 156,240,16,196\n"
"eX 146,146\n"
"mA 156,244,20,156\n"
"dT 186,3,45824\n"
"mA 156,248,24,156\n"
"eX 147,147\n"
"mA 156,252,28,156\n"
"cP 177,146,147\n"
"mB 28,128,188,28\n"
"eX 148,148\n"
"mB 44,132,188,44\n"
"dK 208,2,53248\n"
"mB 60,136,188,60\n"
"eX 149,149\n"
"mB 76,140,188,76\n"
"dT 188,4,41728\n"
"cP 178,148,149\n"
"mB 92,144,188,92\n"
"eX 150,150\n"
"mB 108,148,188,108\n"
"dT 190,4,45824\n"
"mB 124,152,188,124\n"
"eX 151,151\n"
"mB 140,156,188,140\n"
"buffer_load_dwordx4 v6, s[40:43], s81 offen offset:2048 lds\n"
"cP 179,150,151\n"
"mB 220,64,188,220\n"
"mA 160,224,32,200\n"
"eX 152,152\n"
"mA 160,228,36,160\n"
"dK 212,2,53504\n"
"mA 160,232,40,160\n"
"eX 153,153\n"
"mA 160,236,44,160\n"
"dT 128,3,49152\n"
"cP 180,152,153\n"
"mA 164,240,32,200\n"
"eX 154,154\n"
"mA 164,244,36,164\n"
"dT 130,3,53248\n"
"mA 164,248,40,164\n"
"eX 155,155\n"
"mA 164,252,44,164\n"
"cP 181,154,155\n"
"mB 16,160,176,16\n"
"eX 156,156\n"
"mB 32,164,176,32\n"
"dK 216,2,53760\n"
"mB 48,168,176,48\n"
"eX 157,157\n"
"s_waitcnt lgkmcnt(7)\n"
"mB 64,172,176,64\n"
"dT 132,4,49152\n"
"cP 182,156,157\n"
"mB 80,176,176,80\n"
"eX 158,158\n"
"mB 96,180,176,96\n"
"dT 134,4,53248\n"
"mB 112,184,176,112\n"
"eX 159,159\n"
"s_waitcnt lgkmcnt(6)\n"
"mB 128,188,176,128\n"
"buffer_load_dwordx4 v6, s[40:43], s81 offen offset:3072 lds\n"
"cP 183,158,159\n"
"mB 208,64,176,208\n"
"mA 168,224,48,204\n"
"eX 160,160\n"
"mA 168,228,52,168\n"
"dK 220,2,54016\n"
"mA 168,232,56,168\n"
"eX 161,161\n"
"mA 168,236,60,168\n"
"dT 136,3,49408\n"
"cP 184,160,161\n"
"mA 172,240,48,204\n"
"eX 162,162\n"
"mA 172,244,52,172\n"
"dT 138,3,53504\n"
"mA 172,248,56,172\n"
"eX 163,163\n"
"mA 172,252,60,172\n"
"s_add_u32 m0, s57, 156672\n"
"s_nop 0\n"
"buffer_load_dwordx4 v7, s[52:55], s83 offen lds\n"
"s_add_u32 s83, s83, 1048576\n"
"cP 185,162,163\n"
"mB 20,160,180,20\n"
"eX 164,164\n"
"mB 36,164,180,36\n"
"dK 224,2,57344\n"
"mB 52,168,180,52\n"
"eX 165,165\n"
"mB 68,172,180,68\n"
"dT 140,4,49408\n"
"cP 186,164,165\n"
"mB 84,176,180,84\n"
"eX 166,166\n"
"mB 100,180,180,100\n"
"dT 142,4,53504\n"
"mB 116,184,180,116\n"
"eX 167,167\n"
"mB 132,188,180,132\n"
"s_mov_b32 m0, s78\n"
"s_nop 0\n"
"buffer_load_dwordx4 v5, s[44:47], s81 offen lds\n"
"cP 187,166,167\n"
"mB 212,64,180,212\n"
"mA 144,192,0,192\n"
"eX 168,168\n"
"mA 144,196,4,144\n"
"dK 228,2,57600\n"
"mA 144,200,8,144\n"
"eX 169,169\n"
"mA 144,204,12,144\n"
"dT 144,3,49664\n"
"cP 188,168,169\n"
"mA 148,208,0,192\n"
"eX 170,170\n"
"s_waitcnt lgkmcnt(5)\n"
"mA 148,212,4,148\n"
"dT 146,3,53760\n"
"mA 148,216,8,148\n"
"eX 171,171\n"
"mA 148,220,12,148\n"
"cP 189,170,171\n"
"mB 24,160,184,24\n"
"eX 172,172\n"
"mB 40,164,184,40\n"
"dK 232,2,57856\n"
"mB 56,168,184,56\n"
"eX 173,173\n"
"mB 72,172,184,72\n"
"dT 148,4,49664\n"
"cP 190,172,173\n"
"mB 88,176,184,88\n"
"eX 174,174\n"
"mB 104,180,184,104\n"
"dT 150,4,53760\n"
"mB 120,184,184,120\n"
"eX 175,175\n"
"mB 136,188,184,136\n"
"buffer_load_dwordx4 v6, s[44:47], s81 offen offset:1024 lds\n"
"cP 191,174,175\n"
"mB 216,64,184,216\n"
"mA 152,192,16,196\n"
"eX 144,144\n"
"mA 152,196,20,152\n"
"dK 236,2,58112\n"
"mA 152,200,24,152\n"
"eX 145,145\n"
"mA 152,204,28,152\n"
"dT 152,3,49920\n"
"cP 176,144,145\n"
"mA 156,208,16,196\n"
"eX 146,146\n"
"mA 156,212,20,156\n"
"dT 154,3,54016\n"
"mA 156,216,24,156\n"
"eX 147,147\n"
"mA 156,220,28,156\n"
"cP 177,146,147\n"
"mB 28,160,188,28\n"
"eX 148,148\n"
"mB 44,164,188,44\n"
"dK 240,2,61440\n"
"mB 60,168,188,60\n"
"eX 149,149\n"
"mB 76,172,188,76\n"
"dT 156,4,49920\n"
"cP 178,148,149\n"
"mB 92,176,188,92\n"
"eX 150,150\n"
"mB 108,180,188,108\n"
"dT 158,4,54016\n"
"mB 124,184,188,124\n"
"eX 151,151\n"
"mB 140,188,188,140\n"
"buffer_load_dwordx4 v5, s[44:47], s81 offen offset:2048 lds\n"
"cP 179,150,151\n"
"mB 220,64,188,220\n"
"mA 160,192,32,200\n"
"eX 152,152\n"
"mA 160,196,36,160\n"
"dK 244,2,61696\n"
"mA 160,200,40,160\n"
"eX 153,153\n"
"mA 160,204,44,160\n"
"dT 160,3,57344\n"
"cP 180,152,153\n"
"mA 164,208,32,200\n"
"eX 154,154\n"
"mA 164,212,36,164\n"
"dT 162,3,61440\n"
"mA 164,216,40,164\n"
"eX 155,155\n"
"mA 164,220,44,164\n"
"cP 181,154,155\n"
"mB 16,128,176,16\n"
"eX 156,156\n"
"mB 32,132,176,32\n"
"dK 248,2,61952\n"
"mB 48,136,176,48\n"
"eX 157,157\n"
"s_waitcnt lgkmcnt(7)\n"
"mB 64,140,176,64\n"
"dT 164,4,57344\n"
"cP 182,156,157\n"
"mB 80,144,176,80\n"
"eX 158,158\n"
"mB 96,148,176,96\n"
"dT 166,4,61440\n"
"mB 112,152,176,112\n"
"eX 159,159\n"
"s_waitcnt lgkmcnt(6)\n"
"mB 128,156,176,128\n"
"buffer_load_dwordx4 v6, s[44:47], s81 offen offset:3072 lds\n"
"s_add_u32 s81, s81, 16384\n"
"cP 183,158,159\n"
"mB 208,64,176,208\n"
"mA 168,192,48,204\n"
"eX 160,160\n"
"mA 168,196,52,168\n"
"dK 252,2,62208\n"
"mA 168,200,56,168\n"
"eX 161,161\n"
"mA 168,204,60,168\n"
"dT 168,3,57600\n"
"cP 184,160,161\n"
"mA 172,208,48,204\n"
"eX 162,162\n"
"mA 172,212,52,172\n"
"dT 170,3,61696\n"
"mA 172,216,56,172\n"
"eX 163,163\n"
"mA 172,220,60,172\n"
"s_add_u32 m0, s57, 156672\n"
"s_nop 0\n"
"buffer_load_dwordx4 v7, s[52:55], s83 offen lds\n"
"s_add_u32 s83, s83, 1048576\n"
"cP 185,162,163\n"
"mB 20,128,180,20\n"
"s_waitcnt vmcnt(11)\n"
"s_barrier\n"
"eX 164,164\n"
"mB 36,132,180,36\n"
"ds_read_b128 a[192:195], v2\n"
"mB 52,136,180,52\n"
"eX 165,165\n"
"mB 68,140,180,68\n"
"dT 172,4,57600\n"
"cP 186,164,165\n"
"mB 84,144,180,84\n"
"eX 166,166\n"
"mB 100,148,180,100\n"
"dT 174,4,61696\n"
"mB 116,152,180,116\n"
"eX 167,167\n"
"mB 132,156,180,132\n"
"s_mov_b32 m0, s75\n"
"s_nop 0\n"
"buffer_load_dwordx4 v5, s[40:43], s81 offen lds\n"
"cP 187,166,167\n"
"mB 212,64,180,212\n"
"mA 144,224,0,192\n"
"eX 168,168\n"
"mA 144,228,4,144\n"
"dK 196,2,256\n"
"mA 144,232,8,144\n"
"eX 169,169\n"
"mA 144,236,12,144\n"
"dT 176,3,57856\n"
"cP 188,168,169\n"
"mA 148,240,0,192\n"
"eX 170,170\n"
"s_waitcnt lgkmcnt(5)\n"
"mA 148,244,4,148\n"
"dT 178,3,61952\n"
"mA 148,248,8,148\n"
"eX 171,171\n"
"mA 148,252,12,148\n"
"cP 189,170,171\n"
"mB 24,128,184,24\n"
"eX 172,172\n"
"mB 40,132,184,40\n"
"dK 200,2,512\n"
"mB 56,136,184,56\n"
"eX 173,173\n"
"mB 72,140,184,72\n"
"dT 180,4,57856\n"
"cP 190,172,173\n"
"mB 88,144,184,88\n"
"eX 174,174\n"
"mB 104,148,184,104\n"
"dT 182,4,61952\n"
"mB 120,152,184,120\n"
"eX 175,175\n"
"mB 136,156,184,136\n"
"buffer_load_dwordx4 v5, s[40:43], s81 offen offset:1024 lds\n"
"cP 191,174,175\n"
"mB 216,64,184,216\n"
"mA 152,224,16,196\n"
"eX 144,144\n"
"mA 152,228,20,152\n"
"dK 204,2,768\n"
"mA 152,232,24,152\n"
"eX 145,145\n"
"mA 152,236,28,152\n"
"dT 184,3,58112\n"
"cP 176,144,145\n"
"mA 156,240,16,196\n"
"eX 146,146\n"
"mA 156,244,20,156\n"
"dT 186,3,62208\n"
"mA 156,248,24,156\n"
"eX 147,147\n"
"mA 156,252,28,156\n"
"cP 177,146,147\n"
"mB 28,128,188,28\n"
"eX 148,148\n"
"mB 44,132,188,44\n"
"dK 208,2,4096\n"
"mB 60,136,188,60\n"
"eX 149,149\n"
"mB 76,140,188,76\n"
"dT 188,4,58112\n"
"cP 178,148,149\n"
"mB 92,144,188,92\n"
"eX 150,150\n"
"mB 108,148,188,108\n"
"dT 190,4,62208\n"
"mB 124,152,188,124\n"
"eX 151,151\n"
"mB 140,156,188,140\n"
"buffer_load_dwordx4 v6, s[40:43], s81 offen offset:2048 lds\n"
"cP 179,150,151\n"
"mB 220,64,188,220\n"
"mA 160,224,32,200\n"
"eX 152,152\n"
"mA 160,228,36,160\n"
"dK 212,2,4352\n"
"mA 160,232,40,160\n"
"eX 153,153\n"
"mA 160,236,44,160\n"
"ds_read_b64_tr_b16 a[128:129], v3\n"
"cP 180,152,153\n"
"mA 164,240,32,200\n"
"eX 154,154\n"
"mA 164,244,36,164\n"
"dT 130,3,4096\n"
"mA 164,248,40,164\n"
"eX 155,155\n"
"mA 164,252,44,164\n"
"cP 181,154,155\n"
"mB 16,160,176,16\n"
"eX 156,156\n"
"mB 32,164,176,32\n"
"dK 216,2,4608\n"
"mB 48,168,176,48\n"
"eX 157,157\n"
"s_waitcnt lgkmcnt(7)\n"
"mB 64,172,176,64\n"
"ds_read_b64_tr_b16 a[132:133], v4\n"
"cP 182,156,157\n"
"mB 80,176,176,80\n"
"eX 158,158\n"
"mB 96,180,176,96\n"
"dT 134,4,4096\n"
"mB 112,184,176,112\n"
"eX 159,159\n"
"s_waitcnt lgkmcnt(6)\n"
"mB 128,188,176,128\n"
"buffer_load_dwordx4 v6, s[40:43], s81 offen offset:3072 lds\n"
"cP 183,158,159\n"
"mB 208,64,176,208\n"
"mA 168,224,48,204\n"
"eX 160,160\n"
"mA 168,228,52,168\n"
"dK 220,2,4864\n"
"mA 168,232,56,168\n"
"eX 161,161\n"
"mA 168,236,60,168\n"
"dT 136,3,256\n"
"cP 184,160,161\n"
"mA 172,240,48,204\n"
"eX 162,162\n"
"mA 172,244,52,172\n"
"dT 138,3,4352\n"
"mA 172,248,56,172\n"
"eX 163,163\n"
"mA 172,252,60,172\n"
"cP 185,162,163\n"
"mB 20,160,180,20\n"
"eX 164,164\n"
"mB 36,164,180,36\n"
"dK 224,2,8192\n"
"mB 52,168,180,52\n"
"eX 165,165\n"
"mB 68,172,180,68\n"
"dT 140,4,256\n"
"cP 186,164,165\n"
"mB 84,176,180,84\n"
"eX 166,166\n"
"mB 100,180,180,100\n"
"dT 142,4,4352\n"
"mB 116,184,180,116\n"
"eX 167,167\n"
"mB 132,188,180,132\n"
"s_mov_b32 m0, s79\n"
"s_nop 0\n"
"buffer_load_dwordx4 v5, s[44:47], s81 offen lds\n"
"cP 187,166,167\n"
"mB 212,64,180,212\n"
"mA 144,192,0,192\n"
"eX 168,168\n"
"mA 144,196,4,144\n"
"dK 228,2,8448\n"
"mA 144,200,8,144\n"
"eX 169,169\n"
"mA 144,204,12,144\n"
"dT 144,3,512\n"
"cP 188,168,169\n"
"mA 148,208,0,192\n"
"eX 170,170\n"
"s_waitcnt lgkmcnt(6)\n"
"mA 148,212,4,148\n"
"dT 146,3,4608\n"
"mA 148,216,8,148\n"
"eX 171,171\n"
"mA 148,220,12,148\n"
"cP 189,170,171\n"
"mB 24,160,184,24\n"
"eX 172,172\n"
"mB 40,164,184,40\n"
"dK 232,2,8704\n"
"mB 56,168,184,56\n"
"eX 173,173\n"
"mB 72,172,184,72\n"
"dT 148,4,512\n"
"cP 190,172,173\n"
"mB 88,176,184,88\n"
"eX 174,174\n"
"mB 104,180,184,104\n"
"dT 150,4,4608\n"
"mB 120,184,184,120\n"
"eX 175,175\n"
"mB 136,188,184,136\n"
"buffer_load_dwordx4 v6, s[44:47], s81 offen offset:1024 lds\n"
"cP 191,174,175\n"
"mB 216,64,184,216\n"
"mA 152,192,16,196\n"
"eX 144,144\n"
"mA 152,196,20,152\n"
"dK 236,2,8960\n"
"mA 152,200,24,152\n"
"eX 145,145\n"
"mA 152,204,28,152\n"
"dT 152,3,768\n"
"cP 176,144,145\n"
"mA 156,208,16,196\n"
"eX 146,146\n"
"mA 156,212,20,156\n"
"dT 154,3,4864\n"
"mA 156,216,24,156\n"
"eX 147,147\n"
"mA 156,220,28,156\n"
"cP 177,146,147\n"
"mB 28,160,188,28\n"
"eX 148,148\n"
"mB 44,164,188,44\n"
"dK 240,2,12288\n"
"mB 60,168,188,60\n"
"eX 149,149\n"
"mB 76,172,188,76\n"
"dT 156,4,768\n"
"cP 178,148,149\n"
"mB 92,176,188,92\n"
"eX 150,150\n"
"mB 108,180,188,108\n"
"dT 158,4,4864\n"
"mB 124,184,188,124\n"
"eX 151,151\n"
"mB 140,188,188,140\n"
"buffer_load_dwordx4 v5, s[44:47], s81 offen offset:2048 lds\n"
"cP 179,150,151\n"
"mB 220,64,188,220\n"
"mA 160,192,32,200\n"
"eX 152,152\n"
"mA 160,196,36,160\n"
"dK 244,2,12544\n"
"mA 160,200,40,160\n"
"eX 153,153\n"
"mA 160,204,44,160\n"
"dT 160,3,8192\n"
"cP 180,152,153\n"
"mA 164,208,32,200\n"
"eX 154,154\n"
"mA 164,212,36,164\n"
"dT 162,3,12288\n"
"mA 164,216,40,164\n"
"eX 155,155\n"
"mA 164,220,44,164\n"
"cP 181,154,155\n"
"mB 16,128,176,16\n"
"eX 156,156\n"
"mB 32,132,176,32\n"
"dK 248,2,12800\n"
"s_waitcnt lgkmcnt(7)\n"
"mB 48,136,176,48\n"
"eX 157,157\n"
"mB 64,140,176,64\n"
"dT 164,4,8192\n"
"cP 182,156,157\n"
"mB 80,144,176,80\n"
"eX 158,158\n"
"mB 96,148,176,96\n"
"dT 166,4,12288\n"
"mB 112,152,176,112\n"
"eX 159,159\n"
"s_waitcnt lgkmcnt(6)\n"
"mB 128,156,176,128\n"
"buffer_load_dwordx4 v6, s[44:47], s81 offen offset:3072 lds\n"
"s_add_u32 s81, s81, 16384\n"
"cP 183,158,159\n"
"mB 208,64,176,208\n"
"mA 168,192,48,204\n"
"eX 160,160\n"
"mA 168,196,52,168\n"
"dK 252,2,13056\n"
"mA 168,200,56,168\n"
"eX 161,161\n"
"mA 168,204,60,168\n"
"dT 168,3,8448\n"
"cP 184,160,161\n"
"mA 172,208,48,204\n"
"eX 162,162\n"
"mA 172,212,52,172\n"
"dT 170,3,12544\n"
"mA 172,216,56,172\n"
"eX 163,163\n"
"mA 172,220,60,172\n"
"cP 185,162,163\n"
"mB 20,128,180,20\n"
"s_waitcnt vmcnt(9)\n"
"s_barrier\n"
"eX 164,164\n"
"mB 36,132,180,36\n"
"dK 192,2,16384\n"
"mB 52,136,180,52\n"
"eX 165,165\n"
"mB 68,140,180,68\n"
"dT 172,4,8448\n"
"cP 186,164,165\n"
"mB 84,144,180,84\n"
"eX 166,166\n"
"mB 100,148,180,100\n"
"dT 174,4,12544\n"
"mB 116,152,180,116\n"
"eX 167,167\n"
"mB 132,156,180,132\n"
"s_mov_b32 m0, s76\n"
"s_nop 0\n"
"buffer_load_dwordx4 v5, s[40:43], s81 offen lds\n"
"cP 187,166,167\n"
"mB 212,64,180,212\n"
"mA 144,224,0,192\n"
"eX 168,168\n"
"mA 144,228,4,144\n"
"dK 196,2,16640\n"
"mA 144,232,8,144\n"
"eX 169,169\n"
"mA 144,236,12,144\n"
"dT 176,3,8704\n"
"cP 188,168,169\n"
"mA 148,240,0,192\n"
"eX 170,170\n"
"s_waitcnt lgkmcnt(6)\n"
"mA 148,244,4,148\n"
"dT 178,3,12800\n"
"mA 148,248,8,148\n"
"eX 171,171\n"
"mA 148,252,12,148\n"
"cP 189,170,171\n"
"mB 24,128,184,24\n"
"eX 172,172\n"
"mB 40,132,184,40\n"
"dK 200,2,16896\n"
"mB 56,136,184,56\n"
"eX 173,173\n"
"mB 72,140,184,72\n"
"dT 180,4,8704\n"
"cP 190,172,173\n"
"mB 88,144,184,88\n"
"eX 174,174\n"
"mB 104,148,184,104\n"
"dT 182,4,12800\n"
"mB 120,152,184,120\n"
"eX 175,175\n"
"mB 136,156,184,136\n"
"buffer_load_dwordx4 v5, s[40:43], s81 offen offset:1024 lds\n"
"cP 191,174,175\n"
"mB 216,64,184,216\n"
"mA 152,224,16,196\n"
"eX 144,144\n"
"mA 152,228,20,152\n"
"dK 204,2,17152\n"
"mA 152,232,24,152\n"
"eX 145,145\n"
"mA 152,236,28,152\n"
"dT 184,3,8960\n"
"cP 176,144,145\n"
"mA 156,240,16,196\n"
"eX 146,146\n"
"mA 156,244,20,156\n"
"dT 186,3,13056\n"
"mA 156,248,24,156\n"
"eX 147,147\n"
"mA 156,252,28,156\n"
"cP 177,146,147\n"
"mB 28,128,188,28\n"
"eX 148,148\n"
"mB 44,132,188,44\n"
"dK 208,2,20480\n"
"mB 60,136,188,60\n"
"eX 149,149\n"
"mB 76,140,188,76\n"
"dT 188,4,8960\n"
"cP 178,148,149\n"
"mB 92,144,188,92\n"
"eX 150,150\n"
"mB 108,148,188,108\n"
"dT 190,4,13056\n"
"mB 124,152,188,124\n"
"eX 151,151\n"
"mB 140,156,188,140\n"
"buffer_load_dwordx4 v6, s[40:43], s81 offen offset:2048 lds\n"
"cP 179,150,151\n"
"mB 220,64,188,220\n"
"mA 160,224,32,200\n"
"eX 152,152\n"
"mA 160,228,36,160\n"
"dK 212,2,20736\n"
"mA 160,232,40,160\n"
"eX 153,153\n"
"mA 160,236,44,160\n"
"dT 128,3,16384\n"
"cP 180,152,153\n"
"mA 164,240,32,200\n"
"eX 154,154\n"
"mA 164,244,36,164\n"
"dT 130,3,20480\n"
"mA 164,248,40,164\n"
"eX 155,155\n"
"mA 164,252,44,164\n"
"cP 181,154,155\n"
"mB 16,160,176,16\n"
"eX 156,156\n"
"mB 32,164,176,32\n"
"dK 216,2,20992\n"
"s_waitcnt lgkmcnt(7)\n"
"mB 48,168,176,48\n"
"eX 157,157\n"
"mB 64,172,176,64\n"
"dT 132,4,16384\n"
"cP 182,156,157\n"
"mB 80,176,176,80\n"
"eX 158,158\n"
"mB 96,180,176,96\n"
"dT 134,4,20480\n"
"mB 112,184,176,112\n"
"eX 159,159\n"
"s_waitcnt lgkmcnt(6)\n"
"mB 128,188,176,128\n"
"buffer_load_dwordx4 v6, s[40:43], s81 offen offset:3072 lds\n"
"cP 183,158,159\n"
"mB 208,64,176,208\n"
"s_sub_u32 s82, s82, 1\n"
"s_cmp_lg_u32 s82, 0\n"
"s_cbranch_scc1 L_loop_%=\n"
"mA 168,224,48,204\n"
"eX 160,160\n"
"mA 168,228,52,168\n"
"dK 220,2,21248\n"
"mA 168,232,56,168\n"
"eX 161,161\n"
"mA 168,236,60,168\n"
"dT 136,3,16640\n"
"cP 184,160,161\n"
"mA 172,240,48,204\n"
"eX 162,162\n"
"mA 172,244,52,172\n"
"dT 138,3,20736\n"
"mA 172,248,56,172\n"
"eX 163,163\n"
"mA 172,252,60,172\n"
"s_getpc_b64 s[52:53]\n"
"L_pc7_%=:\n"
"s_add_u32 s52, s52, L_post_%=-L_pc7_%=\n"
"s_addc_u32 s53, s53, 0\n"
"s_and_b32 s53, s53, 0xffff\n"
"s_mov_b32 s54, L_fin_%=-L_post_%=\n"
"s_mov_b32 s55, 0x20000\n"
"s_lshr_b32 s62, %[bid], 3\n"
"s_lshl_b32 s62, s62, 2\n"
"s_add_u32 s62, s62, s56\n"
"s_lshl_b32 s62, s62, 10\n"
"s_add_u32 m0, s57, 156672\n"
"s_nop 0\n"
"buffer_load_dwordx4 v7, s[52:55], s62 offen lds\n"
"cP 185,162,163\n"
"mB 20,160,180,20\n"
"eX 164,164\n"
"mB 36,164,180,36\n"
"dK 224,2,24576\n"
"mB 52,168,180,52\n"
"eX 165,165\n"
"mB 68,172,180,68\n"
"dT 140,4,16640\n"
"cP 186,164,165\n"
"mB 84,176,180,84\n"
"eX 166,166\n"
"mB 100,180,180,100\n"
"dT 142,4,20736\n"
"mB 116,184,180,116\n"
"eX 167,167\n"
"mB 132,188,180,132\n"
"s_mov_b32 m0, s80\n"
"s_nop 0\n"
"buffer_load_dwordx4 v5, s[44:47], s81 offen lds\n"
"cP 187,166,167\n"
"mB 212,64,180,212\n"
"mA 144,192,0,192\n"
"eX 168,168\n"
"mA 144,196,4,144\n"
"dK 228,2,24832\n"
"mA 144,200,8,144\n"
"eX 169,169\n"
"mA 144,204,12,144\n"
"dT 144,3,16896\n"
"cP 188,168,169\n"
"mA 148,208,0,192\n"
"eX 170,170\n"
"s_waitcnt lgkmcnt(5)\n"
"mA 148,212,4,148\n"
"dT 146,3,20992\n"
"mA 148,216,8,148\n"
"eX 171,171\n"
"mA 148,220,12,148\n"
"cP 189,170,171\n"
"mB 24,160,184,24\n"
"eX 172,172\n"
"mB 40,164,184,40\n"
"dK 232,2,25088\n"
"mB 56,168,184,56\n"
"eX 173,173\n"
"mB 72,172,184,72\n"
"dT 148,4,16896\n"
"cP 190,172,173\n"
"mB 88,176,184,88\n"
"eX 174,174\n"
"mB 104,180,184,104\n"
"dT 150,4,20992\n"
"mB 120,184,184,120\n"
"eX 175,175\n"
"mB 136,188,184,136\n"
"buffer_load_dwordx4 v6, s[44:47], s81 offen offset:1024 lds\n"
"cP 191,174,175\n"
"mB 216,64,184,216\n"
"mA 152,192,16,196\n"
"eX 144,144\n"
"mA 152,196,20,152\n"
"dK 236,2,25344\n"
"mA 152,200,24,152\n"
"eX 145,145\n"
"mA 152,204,28,152\n"
"dT 152,3,17152\n"
"cP 176,144,145\n"
"mA 156,208,16,196\n"
"eX 146,146\n"
"mA 156,212,20,156\n"
"dT 154,3,21248\n"
"mA 156,216,24,156\n"
"eX 147,147\n"
"mA 156,220,28,156\n"
"s_mov_b32 s64, 1152\n"
"v_mul_lo_u32 v248, v252, s64\n"
"v_bfe_u32 v232, v255, 0, 10\n"
"v_add_u32 v248, v248, v232\n"
"v_mul_lo_u32 v249, v253, s64\n"
"v_bfe_u32 v232, v255, 10, 10\n"
"v_add_u32 v249, v249, v232\n"
"v_mul_lo_u32 v250, v254, s64\n"
"v_bfe_u32 v232, v255, 20, 10\n"
"v_add_u32 v250, v250, v232\n"
"s_cmp_eq_u32 s56, 0\n"
"s_cselect_b32 s62, 0, 0x7f000000\n"
"v_or_b32 v250, s62, v250\n"
"s_mul_i32 s62, s59, 589824\n"
"s_mul_i32 s63, s60, 288\n"
"s_add_u32 s62, s62, s63\n"
"s_add_u32 s52, %[wolo], 0\n"
"s_addc_u32 s53, %[wohi], 0\n"
"s_add_u32 s52, s52, s62\n"
"s_addc_u32 s53, s53, 0\n"
"s_and_b32 s53, s53, 0xffff\n"
"s_mov_b32 s54, 589824\n"
"s_mov_b32 s55, 0x20000\n"
"s_sub_u32 s54, s54, s63\n"
"cP 177,146,147\n"
"mB 28,160,188,28\n"
"eX 148,148\n"
"mB 44,164,188,44\n"
"dK 240,2,28672\n"
"mB 60,168,188,60\n"
"eX 149,149\n"
"mB 76,172,188,76\n"
"dT 156,4,17152\n"
"cP 178,148,149\n"
"mB 92,176,188,92\n"
"eX 150,150\n"
"mB 108,180,188,108\n"
"dT 158,4,21248\n"
"mB 124,184,188,124\n"
"eX 151,151\n"
"mB 140,188,188,140\n"
"buffer_load_dwordx4 v5, s[44:47], s81 offen offset:2048 lds\n"
"cP 179,150,151\n"
"mB 220,64,188,220\n"
"mA 160,192,32,200\n"
"eX 152,152\n"
"mA 160,196,36,160\n"
"dK 244,2,28928\n"
"mA 160,200,40,160\n"
"eX 153,153\n"
"mA 160,204,44,160\n"
"dT 160,3,24576\n"
"cP 180,152,153\n"
"mA 164,208,32,200\n"
"eX 154,154\n"
"mA 164,212,36,164\n"
"dT 162,3,28672\n"
"mA 164,216,40,164\n"
"eX 155,155\n"
"mA 164,220,44,164\n"
"cP 181,154,155\n"
"mB 16,128,176,16\n"
"eX 156,156\n"
"mB 32,132,176,32\n"
"dK 248,2,29184\n"
"mB 48,136,176,48\n"
"eX 157,157\n"
"s_waitcnt lgkmcnt(7)\n"
"mB 64,140,176,64\n"
"dT 164,4,24576\n"
"cP 182,156,157\n"
"mB 80,144,176,80\n"
"eX 158,158\n"
"mB 96,148,176,96\n"
"dT 166,4,28672\n"
"mB 112,152,176,112\n"
"eX 159,159\n"
"s_waitcnt lgkmcnt(6)\n"
"mB 128,156,176,128\n"
"buffer_load_dwordx4 v6, s[44:47], s81 offen offset:3072 lds\n"
"s_add_u32 s81, s81, 16384\n"
"cP 183,158,159\n"
"mB 208,64,176,208\n"
"mA 168,192,48,204\n"
"eX 160,160\n"
"mA 168,196,52,168\n"
"dK 252,2,29440\n"
"mA 168,200,56,168\n"
"eX 161,161\n"
"mA 168,204,60,168\n"
"dT 168,3,24832\n"
"cP 184,160,161\n"
"mA 172,208,48,204\n"
"eX 162,162\n"
"mA 172,212,52,172\n"
"dT 170,3,28928\n"
"mA 172,216,56,172\n"
"eX 163,163\n"
"mA 172,220,60,172\n"
"s_add_u32 m0, s87, 138240\n"
"s_cmp_le_u32 s60, 2\n"
"s_cselect_b32 s65, 3, 2\n"
"s_mul_i32 s65, s65, 147456\n"
"s_add_u32 s65, s65, 110592\n"
"buffer_load_dwordx4 v248, s[52:55], s65 offen lds\n"
"cP 185,162,163\n"
"mB 20,128,180,20\n"
"s_waitcnt vmcnt(10)\n"
"s_barrier\n"
"eX 164,164\n"
"mB 36,132,180,36\n"
"dK 192,2,32768\n"
"mB 52,136,180,52\n"
"eX 165,165\n"
"mB 68,140,180,68\n"
"dT 172,4,24832\n"
"cP 186,164,165\n"
"mB 84,144,180,84\n"
"eX 166,166\n"
"mB 100,148,180,100\n"
"dT 174,4,28928\n"
"mB 116,152,180,116\n"
"eX 167,167\n"
"mB 132,156,180,132\n"
"s_add_u32 m0, s87, 142336\n"
"s_cmp_le_u32 s60, 2\n"
"s_cselect_b32 s65, 3, 2\n"
"s_mul_i32 s65, s65, 147456\n"
"s_add_u32 s65, s65, 110592\n"
"buffer_load_dwordx4 v249, s[52:55], s65 offen lds\n"
"cP 187,166,167\n"
"mB 212,64,180,212\n"
"mA 144,224,0,192\n"
"eX 168,168\n"
"mA 144,228,4,144\n"
"dK 196,2,33024\n"
"mA 144,232,8,144\n"
"eX 169,169\n"
"mA 144,236,12,144\n"
"dT 176,3,25088\n"
"cP 188,168,169\n"
"mA 148,240,0,192\n"
"eX 170,170\n"
"s_waitcnt lgkmcnt(5)\n"
"mA 148,244,4,148\n"
"dT 178,3,29184\n"
"mA 148,248,8,148\n"
"eX 171,171\n"
"mA 148,252,12,148\n"
"s_mul_i32 s65, s88, 146432\n"
"s_add_u32 m0, s65, s89\n"
"s_cmp_le_u32 s60, 2\n"
"s_cselect_b32 s65, 3, 2\n"
"s_mul_i32 s65, s65, 147456\n"
"s_add_u32 s65, s65, 110592\n"
"buffer_load_dwordx4 v250, s[52:55], s65 offen lds\n"
"cP 189,170,171\n"
"mB 24,128,184,24\n"
"eX 172,172\n"
"mB 40,132,184,40\n"
"dK 200,2,33280\n"
"mB 56,136,184,56\n"
"eX 173,173\n"
"mB 72,140,184,72\n"
"dT 180,4,25088\n"
"cP 190,172,173\n"
"mB 88,144,184,88\n"
"eX 174,174\n"
"mB 104,148,184,104\n"
"dT 182,4,29184\n"
"mB 120,152,184,120\n"
"eX 175,175\n"
"mB 136,156,184,136\n"
"cP 191,174,175\n"
"mB 216,64,184,216\n"
"mA 152,224,16,196\n"
"eX 144,144\n"
"mA 152,228,20,152\n"
"dK 204,2,33536\n"
"mA 152,232,24,152\n"
"eX 145,145\n"
"mA 152,236,28,152\n"
"dT 184,3,25344\n"
"cP 176,144,145\n"
"mA 156,240,16,196\n"
"eX 146,146\n"
"mA 156,244,20,156\n"
"dT 186,3,29440\n"
"mA 156,248,24,156\n"
"eX 147,147\n"
"mA 156,252,28,156\n"
"cP 177,146,147\n"
"mB 28,128,188,28\n"
"eX 148,148\n"
"mB 44,132,188,44\n"
"dK 208,2,36864\n"
"mB 60,136,188,60\n"
"eX 149,149\n"
"mB 76,140,188,76\n"
"dT 188,4,25344\n"
"cP 178,148,149\n"
"mB 92,144,188,92\n"
"eX 150,150\n"
"mB 108,148,188,108\n"
"dT 190,4,29440\n"
"mB 124,152,188,124\n"
"eX 151,151\n"
"mB 140,156,188,140\n"
"cP 179,150,151\n"
"mB 220,64,188,220\n"
"mA 160,224,32,200\n"
"eX 152,152\n"
"mA 160,228,36,160\n"
"dK 212,2,37120\n"
"mA 160,232,40,160\n"
"eX 153,153\n"
"mA 160,236,44,160\n"
"dT 128,3,32768\n"
"cP 180,152,153\n"
"mA 164,240,32,200\n"
"eX 154,154\n"
"mA 164,244,36,164\n"
"dT 130,3,36864\n"
"mA 164,248,40,164\n"
"eX 155,155\n"
"mA 164,252,44,164\n"
"cP 181,154,155\n"
"mB 16,160,176,16\n"
"eX 156,156\n"
"mB 32,164,176,32\n"
"dK 216,2,37376\n"
"mB 48,168,176,48\n"
"eX 157,157\n"
"s_waitcnt lgkmcnt(7)\n"
"mB 64,172,176,64\n"
"dT 132,4,32768\n"
"cP 182,156,157\n"
"mB 80,176,176,80\n"
"eX 158,158\n"
"mB 96,180,176,96\n"
"dT 134,4,36864\n"
"mB 112,184,176,112\n"
"eX 159,159\n"
"s_waitcnt lgkmcnt(6)\n"
"mB 128,188,176,128\n"
"cP 183,158,159\n"
"mB 208,64,176,208\n"
"mA 168,224,48,204\n"
"eX 160,160\n"
"mA 168,228,52,168\n"
"dK 220,2,37632\n"
"mA 168,232,56,168\n"
"eX 161,161\n"
"mA 168,236,60,168\n"
"dT 136,3,33024\n"
"cP 184,160,161\n"
"mA 172,240,48,204\n"
"eX 162,162\n"
"mA 172,244,52,172\n"
"dT 138,3,37120\n"
"mA 172,248,56,172\n"
"eX 163,163\n"
"mA 172,252,60,172\n"
"cP 185,162,163\n"
"mB 20,160,180,20\n"
"eX 164,164\n"
"mB 36,164,180,36\n"
"dK 224,2,40960\n"
"mB 52,168,180,52\n"
"eX 165,165\n"
"mB 68,172,180,68\n"
"dT 140,4,33024\n"
"cP 186,164,165\n"
"mB 84,176,180,84\n"
"eX 166,166\n"
"mB 100,180,180,100\n"
"dT 142,4,37120\n"
"mB 116,184,180,116\n"
"eX 167,167\n"
"mB 132,188,180,132\n"
"cP 187,166,167\n"
"mB 212,64,180,212\n"
"mA 144,192,0,192\n"
"eX 168,168\n"
"mA 144,196,4,144\n"
"dK 228,2,41216\n"
"mA 144,200,8,144\n"
"eX 169,169\n"
"mA 144,204,12,144\n"
"dT 144,3,33280\n"
"cP 188,168,169\n"
"mA 148,208,0,192\n"
"eX 170,170\n"
"s_waitcnt lgkmcnt(7)\n"
"mA 148,212,4,148\n"
"dT 146,3,37376\n"
"mA 148,216,8,148\n"
"eX 171,171\n"
"mA 148,220,12,148\n"
"cP 189,170,171\n"
"mB 24,160,184,24\n"
"eX 172,172\n"
"mB 40,164,184,40\n"
"dK 232,2,41472\n"
"mB 56,168,184,56\n"
"eX 173,173\n"
"mB 72,172,184,72\n"
"dT 148,4,33280\n"
"cP 190,172,173\n"
"mB 88,176,184,88\n"
"eX 174,174\n"
"mB 104,180,184,104\n"
"dT 150,4,37376\n"
"mB 120,184,184,120\n"
"eX 175,175\n"
"mB 136,188,184,136\n"
"cP 191,174,175\n"
"mB 216,64,184,216\n"
"mA 152,192,16,196\n"
"eX 144,144\n"
"mA 152,196,20,152\n"
"dK 236,2,41728\n"
"mA 152,200,24,152\n"
"eX 145,145\n"
"mA 152,204,28,152\n"
"dT 152,3,33536\n"
"cP 176,144,145\n"
"mA 156,208,16,196\n"
"eX 146,146\n"
"mA 156,212,20,156\n"
"dT 154,3,37632\n"
"mA 156,216,24,156\n"
"eX 147,147\n"
"mA 156,220,28,156\n"
"cP 177,146,147\n"
"mB 28,160,188,28\n"
"eX 148,148\n"
"mB 44,164,188,44\n"
"dK 240,2,45056\n"
"mB 60,168,188,60\n"
"eX 149,149\n"
"mB 76,172,188,76\n"
"dT 156,4,33536\n"
"cP 178,148,149\n"
"mB 92,176,188,92\n"
"eX 150,150\n"
"mB 108,180,188,108\n"
"dT 158,4,37632\n"
"mB 124,184,188,124\n"
"eX 151,151\n"
"mB 140,188,188,140\n"
"cP 179,150,151\n"
"mB 220,64,188,220\n"
"mA 160,192,32,200\n"
"eX 152,152\n"
"mA 160,196,36,160\n"
"dK 244,2,45312\n"
"mA 160,200,40,160\n"
"eX 153,153\n"
"mA 160,204,44,160\n"
"dT 160,3,40960\n"
"cP 180,152,153\n"
"mA 164,208,32,200\n"
"eX 154,154\n"
"mA 164,212,36,164\n"
"dT 162,3,45056\n"
"mA 164,216,40,164\n"
"eX 155,155\n"
"mA 164,220,44,164\n"
"cP 181,154,155\n"
"mB 16,128,176,16\n"
"eX 156,156\n"
"mB 32,132,176,32\n"
"dK 248,2,45568\n"
"s_waitcnt lgkmcnt(7)\n"
"mB 48,136,176,48\n"
"eX 157,157\n"
"mB 64,140,176,64\n"
"dT 164,4,40960\n"
"cP 182,156,157\n"
"mB 80,144,176,80\n"
"eX 158,158\n"
"mB 96,148,176,96\n"
"dT 166,4,45056\n"
"mB 112,152,176,112\n"
"eX 159,159\n"
"s_waitcnt lgkmcnt(6)\n"
"mB 128,156,176,128\n"
"cP 183,158,159\n"
"mB 208,64,176,208\n"
"mA 168,192,48,204\n"
"eX 160,160\n"
"mA 168,196,52,168\n"
"dK 252,2,45824\n"
"mA 168,200,56,168\n"
"eX 161,161\n"
"mA 168,204,60,168\n"
"dT 168,3,41216\n"
"cP 184,160,161\n"
"mA 172,208,48,204\n"
"eX 162,162\n"
"mA 172,212,52,172\n"
"dT 170,3,45312\n"
"mA 172,216,56,172\n"
"eX 163,163\n"
"mA 172,220,60,172\n"
"cP 185,162,163\n"
"mB 20,128,180,20\n"
"s_waitcnt vmcnt(3)\n"
"s_barrier\n"
"eX 164,164\n"
"mB 36,132,180,36\n"
"dK 192,2,49152\n"
"mB 52,136,180,52\n"
"eX 165,165\n"
"mB 68,140,180,68\n"
"dT 172,4,41216\n"
"cP 186,164,165\n"
"mB 84,144,180,84\n"
"eX 166,166\n"
"mB 100,148,180,100\n"
"dT 174,4,45312\n"
"mB 116,152,180,116\n"
"eX 167,167\n"
"mB 132,156,180,132\n"
"cP 187,166,167\n"
"mB 212,64,180,212\n"
"mA 144,224,0,192\n"
"eX 168,168\n"
"mA 144,228,4,144\n"
"dK 196,2,49408\n"
"mA 144,232,8,144\n"
"eX 169,169\n"
"mA 144,236,12,144\n"
"dT 176,3,41472\n"
"cP 188,168,169\n"
"mA 148,240,0,192\n"
"eX 170,170\n"
"s_waitcnt lgkmcnt(6)\n"
"mA 148,244,4,148\n"
"dT 178,3,45568\n"
"mA 148,248,8,148\n"
"eX 171,171\n"
"mA 148,252,12,148\n"
"s_add_u32 m0, s87, 0\n"
"s_mov_b32 s65, s60\n"
"s_mul_i32 s65, s65, 147456\n"
"s_add_u32 s65, s65, 0\n"
"buffer_load_dwordx4 v248, s[52:55], s65 offen lds\n"
"cP 189,170,171\n"
"mB 24,128,184,24\n"
"eX 172,172\n"
"mB 40,132,184,40\n"
"dK 200,2,49664\n"
"mB 56,136,184,56\n"
"eX 173,173\n"
"mB 72,140,184,72\n"
"dT 180,4,41472\n"
"cP 190,172,173\n"
"mB 88,144,184,88\n"
"eX 174,174\n"
"mB 104,148,184,104\n"
"dT 182,4,45568\n"
"mB 120,152,184,120\n"
"eX 175,175\n"
"mB 136,156,184,136\n"
"s_add_u32 m0, s87, 4096\n"
"s_mov_b32 s65, s60\n"
"s_mul_i32 s65, s65, 147456\n"
"s_add_u32 s65, s65, 0\n"
"buffer_load_dwordx4 v249, s[52:55], s65 offen lds\n"
"cP 191,174,175\n"
"mB 216,64,184,216\n"
"mA 152,224,16,196\n"
"eX 144,144\n"
"mA 152,228,20,152\n"
"dK 204,2,49920\n"
"mA 152,232,24,152\n"
"eX 145,145\n"
"mA 152,236,28,152\n"
"dT 184,3,41728\n"
"cP 176,144,145\n"
"mA 156,240,16,196\n"
"eX 146,146\n"
"mA 156,244,20,156\n"
"dT 186,3,45824\n"
"mA 156,248,24,156\n"
"eX 147,147\n"
"mA 156,252,28,156\n"
"s_mul_i32 s65, s88, 8192\n"
"s_add_u32 m0, s65, s89\n"
"s_mov_b32 s65, s60\n"
"s_mul_i32 s65, s65, 147456\n"
"s_add_u32 s65, s65, 0\n"
"buffer_load_dwordx4 v250, s[52:55], s65 offen lds\n"
"cP 177,146,147\n"
"mB 28,128,188,28\n"
"eX 148,148\n"
"mB 44,132,188,44\n"
"dK 208,2,53248\n"
"mB 60,136,188,60\n"
"eX 149,149\n"
"mB 76,140,188,76\n"
"dT 188,4,41728\n"
"cP 178,148,149\n"
"mB 92,144,188,92\n"
"eX 150,150\n"
"mB 108,148,188,108\n"
"dT 190,4,45824\n"
"mB 124,152,188,124\n"
"eX 151,151\n"
"mB 140,156,188,140\n"
"s_add_u32 m0, s87, 9216\n"
"s_mov_b32 s65, s60\n"
"s_mul_i32 s65, s65, 147456\n"
"s_add_u32 s65, s65, 36864\n"
"buffer_load_dwordx4 v248, s[52:55], s65 offen lds\n"
"cP 179,150,151\n"
"mB 220,64,188,220\n"
"mA 160,224,32,200\n"
"eX 152,152\n"
"mA 160,228,36,160\n"
"dK 212,2,53504\n"
"mA 160,232,40,160\n"
"eX 153,153\n"
"mA 160,236,44,160\n"
"dT 128,3,49152\n"
"cP 180,152,153\n"
"mA 164,240,32,200\n"
"eX 154,154\n"
"mA 164,244,36,164\n"
"dT 130,3,53248\n"
"mA 164,248,40,164\n"
"eX 155,155\n"
"mA 164,252,44,164\n"
"s_add_u32 m0, s87, 13312\n"
"s_mov_b32 s65, s60\n"
"s_mul_i32 s65, s65, 147456\n"
"s_add_u32 s65, s65, 36864\n"
"buffer_load_dwordx4 v249, s[52:55], s65 offen lds\n"
"cP 181,154,155\n"
"mB 16,160,176,16\n"
"eX 156,156\n"
"mB 32,164,176,32\n"
"dK 216,2,53760\n"
"s_waitcnt lgkmcnt(5)\n"
"mB 48,168,176,48\n"
"eX 157,157\n"
"mB 64,172,176,64\n"
"dT 132,4,49152\n"
"cP 182,156,157\n"
"mB 80,176,176,80\n"
"eX 158,158\n"
"mB 96,180,176,96\n"
"dT 134,4,53248\n"
"mB 112,184,176,112\n"
"eX 159,159\n"
"s_waitcnt lgkmcnt(6)\n"
"mB 128,188,176,128\n"
"s_mul_i32 s65, s88, 17408\n"
"s_add_u32 m0, s65, s89\n"
"s_mov_b32 s65, s60\n"
"s_mul_i32 s65, s65, 147456\n"
"s_add_u32 s65, s65, 36864\n"
"buffer_load_dwordx4 v250, s[52:55], s65 offen lds\n"
"cP 183,158,159\n"
"mB 208,64,176,208\n"
"mA 168,224,48,204\n"
"eX 160,160\n"
"mA 168,228,52,168\n"
"dK 220,2,54016\n"
"mA 168,232,56,168\n"
"eX 161,161\n"
"mA 168,236,60,168\n"
"dT 136,3,49408\n"
"cP 184,160,161\n"
"mA 172,240,48,204\n"
"eX 162,162\n"
"mA 172,244,52,172\n"
"dT 138,3,53504\n"
"mA 172,248,56,172\n"
"eX 163,163\n"
"mA 172,252,60,172\n"
"s_add_u32 m0, s87, 18432\n"
"s_mov_b32 s65, s60\n"
"s_mul_i32 s65, s65, 147456\n"
"s_add_u32 s65, s65, 73728\n"
"buffer_load_dwordx4 v248, s[52:55], s65 offen lds\n"
"cP 185,162,163\n"
"mB 20,160,180,20\n"
"eX 164,164\n"
"mB 36,164,180,36\n"
"dK 224,2,57344\n"
"mB 52,168,180,52\n"
"eX 165,165\n"
"mB 68,172,180,68\n"
"dT 140,4,49408\n"
"cP 186,164,165\n"
"mB 84,176,180,84\n"
"eX 166,166\n"
"mB 100,180,180,100\n"
"dT 142,4,53504\n"
"mB 116,184,180,116\n"
"eX 167,167\n"
"mB 132,188,180,132\n"
"s_add_u32 m0, s87, 22528\n"
"s_mov_b32 s65, s60\n"
"s_mul_i32 s65, s65, 147456\n"
"s_add_u32 s65, s65, 73728\n"
"buffer_load_dwordx4 v249, s[52:55], s65 offen lds\n"
"cP 187,166,167\n"
"mB 212,64,180,212\n"
"mA 144,192,0,192\n"
"eX 168,168\n"
"mA 144,196,4,144\n"
"dK 228,2,57600\n"
"mA 144,200,8,144\n"
"eX 169,169\n"
"mA 144,204,12,144\n"
"dT 144,3,49664\n"
"cP 188,168,169\n"
"mA 148,208,0,192\n"
"eX 170,170\n"
"s_waitcnt lgkmcnt(5)\n"
"mA 148,212,4,148\n"
"dT 146,3,53760\n"
"mA 148,216,8,148\n"
"eX 171,171\n"
"mA 148,220,12,148\n"
"s_mul_i32 s65, s88, 26624\n"
"s_add_u32 m0, s65, s89\n"
"s_mov_b32 s65, s60\n"
"s_mul_i32 s65, s65, 147456\n"
"s_add_u32 s65, s65, 73728\n"
"buffer_load_dwordx4 v250, s[52:55], s65 offen lds\n"
"cP 189,170,171\n"
"mB 24,160,184,24\n"
"eX 172,172\n"
"mB 40,164,184,40\n"
"dK 232,2,57856\n"
"mB 56,168,184,56\n"
"eX 173,173\n"
"mB 72,172,184,72\n"
"dT 148,4,49664\n"
"cP 190,172,173\n"
"mB 88,176,184,88\n"
"eX 174,174\n"
"mB 104,180,184,104\n"
"dT 150,4,53760\n"
"mB 120,184,184,120\n"
"eX 175,175\n"
"mB 136,188,184,136\n"
"s_add_u32 m0, s87, 73728\n"
"s_cmp_le_u32 s60, 1\n"
"s_cselect_b32 s65, 2, 1\n"
"s_mul_i32 s65, s65, 147456\n"
"s_add_u32 s65, s65, 0\n"
"buffer_load_dwordx4 v248, s[52:55], s65 offen lds\n"
"cP 191,174,175\n"
"mB 216,64,184,216\n"
"mA 152,192,16,196\n"
"eX 144,144\n"
"mA 152,196,20,152\n"
"dK 236,2,58112\n"
"mA 152,200,24,152\n"
"eX 145,145\n"
"mA 152,204,28,152\n"
"dT 152,3,49920\n"
"cP 176,144,145\n"
"mA 156,208,16,196\n"
"eX 146,146\n"
"mA 156,212,20,156\n"
"dT 154,3,54016\n"
"mA 156,216,24,156\n"
"eX 147,147\n"
"mA 156,220,28,156\n"
"s_add_u32 m0, s87, 77824\n"
"s_cmp_le_u32 s60, 1\n"
"s_cselect_b32 s65, 2, 1\n"
"s_mul_i32 s65, s65, 147456\n"
"s_add_u32 s65, s65, 0\n"
"buffer_load_dwordx4 v249, s[52:55], s65 offen lds\n"
"cP 177,146,147\n"
"mB 28,160,188,28\n"
"eX 148,148\n"
"mB 44,164,188,44\n"
"dK 240,2,61440\n"
"mB 60,168,188,60\n"
"eX 149,149\n"
"mB 76,172,188,76\n"
"dT 156,4,49920\n"
"cP 178,148,149\n"
"mB 92,176,188,92\n"
"eX 150,150\n"
"mB 108,180,188,108\n"
"dT 158,4,54016\n"
"mB 124,184,188,124\n"
"eX 151,151\n"
"mB 140,188,188,140\n"
"s_mul_i32 s65, s88, 81920\n"
"s_add_u32 m0, s65, s89\n"
"s_cmp_le_u32 s60, 1\n"
"s_cselect_b32 s65, 2, 1\n"
"s_mul_i32 s65, s65, 147456\n"
"s_add_u32 s65, s65, 0\n"
"buffer_load_dwordx4 v250, s[52:55], s65 offen lds\n"
"cP 179,150,151\n"
"mB 220,64,188,220\n"
"mA 160,192,32,200\n"
"eX 152,152\n"
"mA 160,196,36,160\n"
"dK 244,2,61696\n"
"mA 160,200,40,160\n"
"eX 153,153\n"
"mA 160,204,44,160\n"
"dT 160,3,57344\n"
"cP 180,152,153\n"
"mA 164,208,32,200\n"
"eX 154,154\n"
"mA 164,212,36,164\n"
"dT 162,3,61440\n"
"mA 164,216,40,164\n"
"eX 155,155\n"
"mA 164,220,44,164\n"
"s_add_u32 m0, s87, 82944\n"
"s_cmp_le_u32 s60, 1\n"
"s_cselect_b32 s65, 2, 1\n"
"s_mul_i32 s65, s65, 147456\n"
"s_add_u32 s65, s65, 36864\n"
"buffer_load_dwordx4 v248, s[52:55], s65 offen lds\n"
"cP 181,154,155\n"
"mB 16,128,176,16\n"
"eX 156,156\n"
"mB 32,132,176,32\n"
"dK 248,2,61952\n"
"mB 48,136,176,48\n"
"eX 157,157\n"
"s_waitcnt lgkmcnt(4)\n"
"mB 64,140,176,64\n"
"dT 164,4,57344\n"
"cP 182,156,157\n"
"mB 80,144,176,80\n"
"eX 158,158\n"
"mB 96,148,176,96\n"
"dT 166,4,61440\n"
"mB 112,152,176,112\n"
"eX 159,159\n"
"mB 128,156,176,128\n"
"s_add_u32 m0, s87, 87040\n"
"s_cmp_le_u32 s60, 1\n"
"s_cselect_b32 s65, 2, 1\n"
"s_mul_i32 s65, s65, 147456\n"
"s_add_u32 s65, s65, 36864\n"
"buffer_load_dwordx4 v249, s[52:55], s65 offen lds\n"
"cP 183,158,159\n"
"mB 208,64,176,208\n"
"mA 168,192,48,204\n"
"eX 160,160\n"
"mA 168,196,52,168\n"
"dK 252,2,62208\n"
"mA 168,200,56,168\n"
"eX 161,161\n"
"mA 168,204,60,168\n"
"dT 168,3,57600\n"
"cP 184,160,161\n"
"mA 172,208,48,204\n"
"eX 162,162\n"
"mA 172,212,52,172\n"
"dT 170,3,61696\n"
"mA 172,216,56,172\n"
"eX 163,163\n"
"mA 172,220,60,172\n"
"s_mul_i32 s65, s88, 91136\n"
"s_add_u32 m0, s65, s89\n"
"s_cmp_le_u32 s60, 1\n"
"s_cselect_b32 s65, 2, 1\n"
"s_mul_i32 s65, s65, 147456\n"
"s_add_u32 s65, s65, 36864\n"
"buffer_load_dwordx4 v250, s[52:55], s65 offen lds\n"
"cP 185,162,163\n"
"mB 20,128,180,20\n"
"s_barrier\n"
"eX 164,164\n"
"mB 36,132,180,36\n"
"mB 52,136,180,52\n"
"eX 165,165\n"
"mB 68,140,180,68\n"
"dT 172,4,57600\n"
"cP 186,164,165\n"
"mB 84,144,180,84\n"
"eX 166,166\n"
"mB 100,148,180,100\n"
"dT 174,4,61696\n"
"mB 116,152,180,116\n"
"eX 167,167\n"
"mB 132,156,180,132\n"
"cP 187,166,167\n"
"mB 212,64,180,212\n"
"mA 144,224,0,192\n"
"eX 168,168\n"
"mA 144,228,4,144\n"
"mA 144,232,8,144\n"
"eX 169,169\n"
"mA 144,236,12,144\n"
"dT 176,3,57856\n"
"cP 188,168,169\n"
"mA 148,240,0,192\n"
"eX 170,170\n"
"s_waitcnt lgkmcnt(4)\n"
"mA 148,244,4,148\n"
"dT 178,3,61952\n"
"mA 148,248,8,148\n"
"eX 171,171\n"
"mA 148,252,12,148\n"
"s_add_u32 m0, s87, 27648\n"
"s_mov_b32 s65, s60\n"
"s_mul_i32 s65, s65, 147456\n"
"s_add_u32 s65, s65, 110592\n"
"buffer_load_dwordx4 v248, s[52:55], s65 offen lds\n"
"cP 189,170,171\n"
"mB 24,128,184,24\n"
"eX 172,172\n"
"mB 40,132,184,40\n"
"mB 56,136,184,56\n"
"eX 173,173\n"
"mB 72,140,184,72\n"
"dT 180,4,57856\n"
"cP 190,172,173\n"
"mB 88,144,184,88\n"
"eX 174,174\n"
"mB 104,148,184,104\n"
"dT 182,4,61952\n"
"mB 120,152,184,120\n"
"eX 175,175\n"
"mB 136,156,184,136\n"
"s_add_u32 m0, s87, 31744\n"
"s_mov_b32 s65, s60\n"
"s_mul_i32 s65, s65, 147456\n"
"s_add_u32 s65, s65, 110592\n"
"buffer_load_dwordx4 v249, s[52:55], s65 offen lds\n"
"cP 191,174,175\n"
"mB 216,64,184,216\n"
"mA 152,224,16,196\n"
"eX 144,144\n"
"mA 152,228,20,152\n"
"mA 152,232,24,152\n"
"eX 145,145\n"
"mA 152,236,28,152\n"
"dT 184,3,58112\n"
"cP 176,144,145\n"
"mA 156,240,16,196\n"
"eX 146,146\n"
"mA 156,244,20,156\n"
"dT 186,3,62208\n"
"mA 156,248,24,156\n"
"eX 147,147\n"
"mA 156,252,28,156\n"
"s_mul_i32 s65, s88, 35840\n"
"s_add_u32 m0, s65, s89\n"
"s_mov_b32 s65, s60\n"
"s_mul_i32 s65, s65, 147456\n"
"s_add_u32 s65, s65, 110592\n"
"buffer_load_dwordx4 v250, s[52:55], s65 offen lds\n"
"cP 177,146,147\n"
"mB 28,128,188,28\n"
"eX 148,148\n"
"mB 44,132,188,44\n"
"mB 60,136,188,60\n"
"eX 149,149\n"
"mB 76,140,188,76\n"
"dT 188,4,58112\n"
"cP 178,148,149\n"
"mB 92,144,188,92\n"
"eX 150,150\n"
"mB 108,148,188,108\n"
"dT 190,4,62208\n"
"mB 124,152,188,124\n"
"eX 151,151\n"
"mB 140,156,188,140\n"
"s_add_u32 m0, s87, 36864\n"
"s_cmp_le_u32 s60, 0\n"
"s_cselect_b32 s65, 1, 0\n"
"s_mul_i32 s65, s65, 147456\n"
"s_add_u32 s65, s65, 0\n"
"buffer_load_dwordx4 v248, s[52:55], s65 offen lds\n"
"cP 179,150,151\n"
"mB 220,64,188,220\n"
"mA 160,224,32,200\n"
"eX 152,152\n"
"mA 160,228,36,160\n"
"mA 160,232,40,160\n"
"eX 153,153\n"
"mA 160,236,44,160\n"
"cP 180,152,153\n"
"mA 164,240,32,200\n"
"eX 154,154\n"
"mA 164,244,36,164\n"
"mA 164,248,40,164\n"
"eX 155,155\n"
"mA 164,252,44,164\n"
"s_add_u32 m0, s87, 40960\n"
"s_cmp_le_u32 s60, 0\n"
"s_cselect_b32 s65, 1, 0\n"
"s_mul_i32 s65, s65, 147456\n"
"s_add_u32 s65, s65, 0\n"
"buffer_load_dwordx4 v249, s[52:55], s65 offen lds\n"
"cP 181,154,155\n"
"mB 16,160,176,16\n"
"eX 156,156\n"
"mB 32,164,176,32\n"
"s_waitcnt lgkmcnt(2)\n"
"mB 48,168,176,48\n"
"eX 157,157\n"
"mB 64,172,176,64\n"
"cP 182,156,157\n"
"mB 80,176,176,80\n"
"eX 158,158\n"
"mB 96,180,176,96\n"
"mB 112,184,176,112\n"
"eX 159,159\n"
"s_waitcnt lgkmcnt(0)\n"
"mB 128,188,176,128\n"
"s_mul_i32 s65, s88, 45056\n"
"s_add_u32 m0, s65, s89\n"
"s_cmp_le_u32 s60, 0\n"
"s_cselect_b32 s65, 1, 0\n"
"s_mul_i32 s65, s65, 147456\n"
"s_add_u32 s65, s65, 0\n"
"buffer_load_dwordx4 v250, s[52:55], s65 offen lds\n"
"cP 183,158,159\n"
"mB 208,64,176,208\n"
"mA 168,224,48,204\n"
"eX 160,160\n"
"mA 168,228,52,168\n"
"mA 168,232,56,168\n"
"eX 161,161\n"
"mA 168,236,60,168\n"
"cP 184,160,161\n"
"mA 172,240,48,204\n"
"eX 162,162\n"
"mA 172,244,52,172\n"
"mA 172,248,56,172\n"
"eX 163,163\n"
"mA 172,252,60,172\n"
"s_add_u32 m0, s87, 92160\n"
"s_cmp_le_u32 s60, 1\n"
"s_cselect_b32 s65, 2, 1\n"
"s_mul_i32 s65, s65, 147456\n"
"s_add_u32 s65, s65, 73728\n"
"buffer_load_dwordx4 v248, s[52:55], s65 offen lds\n"
"cP 185,162,163\n"
"mB 20,160,180,20\n"
"eX 164,164\n"
"mB 36,164,180,36\n"
"mB 52,168,180,52\n"
"eX 165,165\n"
"mB 68,172,180,68\n"
"cP 186,164,165\n"
"mB 84,176,180,84\n"
"eX 166,166\n"
"mB 100,180,180,100\n"
"mB 116,184,180,116\n"
"eX 167,167\n"
"mB 132,188,180,132\n"
"s_add_u32 m0, s87, 96256\n"
"s_cmp_le_u32 s60, 1\n"
"s_cselect_b32 s65, 2, 1\n"
"s_mul_i32 s65, s65, 147456\n"
"s_add_u32 s65, s65, 73728\n"
"buffer_load_dwordx4 v249, s[52:55], s65 offen lds\n"
"cP 187,166,167\n"
"mB 212,64,180,212\n"
"eX 168,168\n"
"eX 169,169\n"
"s_nop 0\n"
"cP 188,168,169\n"
"eX 170,170\n"
"eX 171,171\n"
"s_mul_i32 s65, s88, 100352\n"
"s_add_u32 m0, s65, s89\n"
"s_cmp_le_u32 s60, 1\n"
"s_cselect_b32 s65, 2, 1\n"
"s_mul_i32 s65, s65, 147456\n"
"s_add_u32 s65, s65, 73728\n"
"buffer_load_dwordx4 v250, s[52:55], s65 offen lds\n"
"cP 189,170,171\n"
"mB 24,160,184,24\n"
"eX 172,172\n"
"mB 40,164,184,40\n"
"mB 56,168,184,56\n"
"eX 173,173\n"
"mB 72,172,184,72\n"
"cP 190,172,173\n"
"mB 88,176,184,88\n"
"eX 174,174\n"
"mB 104,180,184,104\n"
"mB 120,184,184,120\n"
"eX 175,175\n"
"mB 136,188,184,136\n"
"s_add_u32 m0, s87, 101376\n"
"s_cmp_le_u32 s60, 1\n"
"s_cselect_b32 s65, 2, 1\n"
"s_mul_i32 s65, s65, 147456\n"
"s_add_u32 s65, s65, 110592\n"
"buffer_load_dwordx4 v248, s[52:55], s65 offen lds\n"
"cP 191,174,175\n"
"mB 216,64,184,216\n"
"s_add_u32 m0, s87, 105472\n"
"s_cmp_le_u32 s60, 1\n"
"s_cselect_b32 s65, 2, 1\n"
"s_mul_i32 s65, s65, 147456\n"
"s_add_u32 s65, s65, 110592\n"
"buffer_load_dwordx4 v249, s[52:55], s65 offen lds\n"
"mB 28,160,188,28\n"
"mB 44,164,188,44\n"
"mB 60,168,188,60\n"
"mB 76,172,188,76\n"
"mB 92,176,188,92\n"
"mB 108,180,188,108\n"
"mB 124,184,188,124\n"
"mB 140,188,188,140\n"
"s_mul_i32 s65, s88, 109568\n"
"s_add_u32 m0, s65, s89\n"
"s_cmp_le_u32 s60, 1\n"
"s_cselect_b32 s65, 2, 1\n"
"s_mul_i32 s65, s65, 147456\n"
"s_add_u32 s65, s65, 110592\n"
"buffer_load_dwordx4 v250, s[52:55], s65 offen lds\n"
"mB 220,64,188,220\n"
"L_post_%=:\n"
"v_cmp_nlt_f32 s[62:63], v208, s86\n"
"v_cmp_nlt_f32 s[64:65], v212, s86\n"
"s_nop 0\n"
"s_or_b64 s[62:63], s[62:63], s[64:65]\n"
"v_cmp_nlt_f32 s[64:65], v216, s86\n"
"s_nop 0\n"
"s_or_b64 s[62:63], s[62:63], s[64:65]\n"
"s_nop 0\n"
"v_cmp_nlt_f32 s[64:65], v220, s86\n"
"s_nop 0\n"
"s_or_b64 s[62:63], s[62:63], s[64:65]\n"
"s_cmp_eq_u64 s[62:63], 0\n"
"s_cbranch_scc1 L_noflag_%=\n"
"v_mov_b32 v15, 1\n"
"ds_write_b32 v14, v15\n"
"L_noflag_%=:\n"
"s_lshl_b32 s68, s56, 19\n"
"s_add_u32 s69, s68, 4096\n"
"s_add_u32 s70, s68, 8192\n"
"s_add_u32 s71, s68, 12288\n"
"v_rcp_f32 v228, v208\n"
"s_nop 0\n"
"mU 16,228,16\n"
"mU 17,228,17\n"
"mU 18,228,18\n"
"mU 19,228,19\n"
"mU 32,228,32\n"
"mU 33,228,33\n"
"mU 34,228,34\n"
"mU 35,228,35\n"
"cP 16,16,17\n"
"cP 17,18,19\n"
"cP 18,32,33\n"
"cP 19,34,35\n"
"buffer_store_dwordx4 v[16:19], v7, s[48:51], s68 offen sc1\n"
"mU 48,228,48\n"
"mU 49,228,49\n"
"mU 50,228,50\n"
"mU 51,228,51\n"
"mU 64,228,64\n"
"mU 65,228,65\n"
"mU 66,228,66\n"
"mU 67,228,67\n"
"cP 48,48,49\n"
"cP 49,50,51\n"
"cP 50,64,65\n"
"cP 51,66,67\n"
"buffer_store_dwordx4 v[48:51], v7, s[48:51], s68 offen offset:1024 sc1\n"
"mU 80,228,80\n"
"mU 81,228,81\n"
"mU 82,228,82\n"
"mU 83,228,83\n"
"mU 96,228,96\n"
"mU 97,228,97\n"
"mU 98,228,98\n"
"mU 99,228,99\n"
"cP 80,80,81\n"
"cP 81,82,83\n"
"cP 82,96,97\n"
"cP 83,98,99\n"
"buffer_store_dwordx4 v[80:83], v7, s[48:51], s70 offen sc1\n"
"mU 112,228,112\n"
"mU 113,228,113\n"
"mU 114,228,114\n"
"mU 115,228,115\n"
"mU 128,228,128\n"
"mU 129,228,129\n"
"mU 130,228,130\n"
"mU 131,228,131\n"
"cP 112,112,113\n"
"cP 113,114,115\n"
"cP 114,128,129\n"
"cP 115,130,131\n"
"buffer_store_dwordx4 v[112:115], v7, s[48:51], s70 offen offset:1024 sc1\n"
"v_rcp_f32 v229, v212\n"
"s_nop 0\n"
"mU 20,229,20\n"
"mU 21,229,21\n"
"mU 22,229,22\n"
"mU 23,229,23\n"
"mU 36,229,36\n"
"mU 37,229,37\n"
"mU 38,229,38\n"
"mU 39,229,39\n"
"cP 20,20,21\n"
"cP 21,22,23\n"
"cP 22,36,37\n"
"cP 23,38,39\n"
"buffer_store_dwordx4 v[20:23], v7, s[48:51], s68 offen offset:2048 sc1\n"
"mU 52,229,52\n"
"mU 53,229,53\n"
"mU 54,229,54\n"
"mU 55,229,55\n"
"mU 68,229,68\n"
"mU 69,229,69\n"
"mU 70,229,70\n"
"mU 71,229,71\n"
"cP 52,52,53\n"
"cP 53,54,55\n"
"cP 54,68,69\n"
"cP 55,70,71\n"
"buffer_store_dwordx4 v[52:55], v7, s[48:51], s68 offen offset:3072 sc1\n"
"mU 84,229,84\n"
"mU 85,229,85\n"
"mU 86,229,86\n"
"mU 87,229,87\n"
"mU 100,229,100\n"
"mU 101,229,101\n"
"mU 102,229,102\n"
"mU 103,229,103\n"
"cP 84,84,85\n"
"cP 85,86,87\n"
"cP 86,100,101\n"
"cP 87,102,103\n"
"buffer_store_dwordx4 v[84:87], v7, s[48:51], s70 offen offset:2048 sc1\n"
"mU 116,229,116\n"
"mU 117,229,117\n"
"mU 118,229,118\n"
"mU 119,229,119\n"
"mU 132,229,132\n"
"mU 133,229,133\n"
"mU 134,229,134\n"
"mU 135,229,135\n"
"cP 116,116,117\n"
"cP 117,118,119\n"
"cP 118,132,133\n"
"cP 119,134,135\n"
"buffer_store_dwordx4 v[116:119], v7, s[48:51], s70 offen offset:3072 sc1\n"
"v_rcp_f32 v230, v216\n"
"s_nop 0\n"
"mU 24,230,24\n"
"mU 25,230,25\n"
"mU 26,230,26\n"
"mU 27,230,27\n"
"mU 40,230,40\n"
"mU 41,230,41\n"
"mU 42,230,42\n"
"mU 43,230,43\n"
"cP 24,24,25\n"
"cP 25,26,27\n"
"cP 26,40,41\n"
"cP 27,42,43\n"
"buffer_store_dwordx4 v[24:27], v7, s[48:51], s69 offen sc1\n"
"mU 56,230,56\n"
"mU 57,230,57\n"
"mU 58,230,58\n"
"mU 59,230,59\n"
"mU 72,230,72\n"
"mU 73,230,73\n"
"mU 74,230,74\n"
"mU 75,230,75\n"
"cP 56,56,57\n"
"cP 57,58,59\n"
"cP 58,72,73\n"
"cP 59,74,75\n"
"buffer_store_dwordx4 v[56:59], v7, s[48:51], s69 offen offset:1024 sc1\n"
"mU 88,230,88\n"
"mU 89,230,89\n"
"mU 90,230,90\n"
"mU 91,230,91\n"
"mU 104,230,104\n"
"mU 105,230,105\n"
"mU 106,230,106\n"
"mU 107,230,107\n"
"cP 88,88,89\n"
"cP 89,90,91\n"
"cP 90,104,105\n"
"cP 91,106,107\n"
"buffer_store_dwordx4 v[88:91], v7, s[48:51], s71 offen sc1\n"
"mU 120,230,120\n"
"mU 121,230,121\n"
"mU 122,230,122\n"
"mU 123,230,123\n"
"mU 136,230,136\n"
"mU 137,230,137\n"
"mU 138,230,138\n"
"mU 139,230,139\n"
"cP 120,120,121\n"
"cP 121,122,123\n"
"cP 122,136,137\n"
"cP 123,138,139\n"
"buffer_store_dwordx4 v[120:123], v7, s[48:51], s71 offen offset:1024 sc1\n"
"v_rcp_f32 v231, v220\n"
"s_nop 0\n"
"mU 28,231,28\n"
"mU 29,231,29\n"
"mU 30,231,30\n"
"mU 31,231,31\n"
"mU 44,231,44\n"
"mU 45,231,45\n"
"mU 46,231,46\n"
"mU 47,231,47\n"
"cP 28,28,29\n"
"cP 29,30,31\n"
"cP 30,44,45\n"
"cP 31,46,47\n"
"buffer_store_dwordx4 v[28:31], v7, s[48:51], s69 offen offset:2048 sc1\n"
"mU 60,231,60\n"
"mU 61,231,61\n"
"mU 62,231,62\n"
"mU 63,231,63\n"
"mU 76,231,76\n"
"mU 77,231,77\n"
"mU 78,231,78\n"
"mU 79,231,79\n"
"cP 60,60,61\n"
"cP 61,62,63\n"
"cP 62,76,77\n"
"cP 63,78,79\n"
"buffer_store_dwordx4 v[60:63], v7, s[48:51], s69 offen offset:3072 sc1\n"
"mU 92,231,92\n"
"mU 93,231,93\n"
"mU 94,231,94\n"
"mU 95,231,95\n"
"mU 108,231,108\n"
"mU 109,231,109\n"
"mU 110,231,110\n"
"mU 111,231,111\n"
"cP 92,92,93\n"
"cP 93,94,95\n"
"cP 94,108,109\n"
"cP 95,110,111\n"
"buffer_store_dwordx4 v[92:95], v7, s[48:51], s71 offen offset:2048 sc1\n"
"mU 124,231,124\n"
"mU 125,231,125\n"
"mU 126,231,126\n"
"mU 127,231,127\n"
"mU 140,231,140\n"
"mU 141,231,141\n"
"mU 142,231,142\n"
"mU 143,231,143\n"
"cP 124,124,125\n"
"cP 125,126,127\n"
"cP 126,140,141\n"
"cP 127,142,143\n"
"buffer_store_dwordx4 v[124:127], v7, s[48:51], s71 offen offset:3072 sc1\n"
"s_waitcnt lgkmcnt(0)\n"
"s_barrier\n"
"ds_read_b32 v15, v14\n"
"s_waitcnt lgkmcnt(0)\n"
"v_readfirstlane_b32 s62, v15\n"
"s_nop 0\n"
"s_cmp_eq_u32 s62, 0\n"
"s_cbranch_scc1 L_aend_%=\n"
"s_waitcnt vmcnt(0) lgkmcnt(0)\n"
"v_mov_b32 v16, 0\n"
"v_mov_b32 v17, 0\n"
"v_mov_b32 v18, 0\n"
"v_mov_b32 v19, 0\n"
"v_mov_b32 v20, 0\n"
"v_mov_b32 v21, 0\n"
"v_mov_b32 v22, 0\n"
"v_mov_b32 v23, 0\n"
"v_mov_b32 v24, 0\n"
"v_mov_b32 v25, 0\n"
"v_mov_b32 v26, 0\n"
"v_mov_b32 v27, 0\n"
"v_mov_b32 v28, 0\n"
"v_mov_b32 v29, 0\n"
"v_mov_b32 v30, 0\n"
"v_mov_b32 v31, 0\n"
"v_mov_b32 v32, 0\n"
"v_mov_b32 v33, 0\n"
"v_mov_b32 v34, 0\n"
"v_mov_b32 v35, 0\n"
"v_mov_b32 v36, 0\n"
"v_mov_b32 v37, 0\n"
"v_mov_b32 v38, 0\n"
"v_mov_b32 v39, 0\n"
"v_mov_b32 v40, 0\n"
"v_mov_b32 v41, 0\n"
"v_mov_b32 v42, 0\n"
"v_mov_b32 v43, 0\n"
"v_mov_b32 v44, 0\n"
"v_mov_b32 v45, 0\n"
"v_mov_b32 v46, 0\n"
"v_mov_b32 v47, 0\n"
"v_mov_b32 v48, 0\n"
"v_mov_b32 v49, 0\n"
"v_mov_b32 v50, 0\n"
"v_mov_b32 v51, 0\n"
"v_mov_b32 v52, 0\n"
"v_mov_b32 v53, 0\n"
"v_mov_b32 v54, 0\n"
"v_mov_b32 v55, 0\n"
"v_mov_b32 v56, 0\n"
"v_mov_b32 v57, 0\n"
"v_mov_b32 v58, 0\n"
"v_mov_b32 v59, 0\n"
"v_mov_b32 v60, 0\n"
"v_mov_b32 v61, 0\n"
"v_mov_b32 v62, 0\n"
"v_mov_b32 v63, 0\n"
"v_mov_b32 v64, 0\n"
"v_mov_b32 v65, 0\n"
"v_mov_b32 v66, 0\n"
"v_mov_b32 v67, 0\n"
"v_mov_b32 v68, 0\n"
"v_mov_b32 v69, 0\n"
"v_mov_b32 v70, 0\n"
"v_mov_b32 v71, 0\n"
"v_mov_b32 v72, 0\n"
"v_mov_b32 v73, 0\n"
"v_mov_b32 v74, 0\n"
"v_mov_b32 v75, 0\n"
"v_mov_b32 v76, 0\n"
"v_mov_b32 v77, 0\n"
"v_mov_b32 v78, 0\n"
"v_mov_b32 v79, 0\n"
"v_mov_b32 v80, 0\n"
"v_mov_b32 v81, 0\n"
"v_mov_b32 v82, 0\n"
"v_mov_b32 v83, 0\n"
"v_mov_b32 v84, 0\n"
"v_mov_b32 v85, 0\n"
"v_mov_b32 v86, 0\n"
"v_mov_b32 v87, 0\n"
"v_mov_b32 v88, 0\n"
"v_mov_b32 v89, 0\n"
"v_mov_b32 v90, 0\n"
"v_mov_b32 v91, 0\n"
"v_mov_b32 v92, 0\n"
"v_mov_b32 v93, 0\n"
"v_mov_b32 v94, 0\n"
"v_mov_b32 v95, 0\n"
"v_mov_b32 v96, 0\n"
"v_mov_b32 v97, 0\n"
"v_mov_b32 v98, 0\n"
"v_mov_b32 v99, 0\n"
"v_mov_b32 v100, 0\n"
"v_mov_b32 v101, 0\n"
"v_mov_b32 v102, 0\n"
"v_mov_b32 v103, 0\n"
"v_mov_b32 v104, 0\n"
"v_mov_b32 v105, 0\n"
"v_mov_b32 v106, 0\n"
"v_mov_b32 v107, 0\n"
"v_mov_b32 v108, 0\n"
"v_mov_b32 v109, 0\n"
"v_mov_b32 v110, 0\n"
"v_mov_b32 v111, 0\n"
"v_mov_b32 v112, 0\n"
"v_mov_b32 v113, 0\n"
"v_mov_b32 v114, 0\n"
"v_mov_b32 v115, 0\n"
"v_mov_b32 v116, 0\n"
"v_mov_b32 v117, 0\n"
"v_mov_b32 v118, 0\n"
"v_mov_b32 v119, 0\n"
"v_mov_b32 v120, 0\n"
"v_mov_b32 v121, 0\n"
"v_mov_b32 v122, 0\n"
"v_mov_b32 v123, 0\n"
"v_mov_b32 v124, 0\n"
"v_mov_b32 v125, 0\n"
"v_mov_b32 v126, 0\n"
"v_mov_b32 v127, 0\n"
"v_mov_b32 v128, 0\n"
"v_mov_b32 v129, 0\n"
"v_mov_b32 v130, 0\n"
"v_mov_b32 v131, 0\n"
"v_mov_b32 v132, 0\n"
"v_mov_b32 v133, 0\n"
"v_mov_b32 v134, 0\n"
"v_mov_b32 v135, 0\n"
"v_mov_b32 v136, 0\n"
"v_mov_b32 v137, 0\n"
"v_mov_b32 v138, 0\n"
"v_mov_b32 v139, 0\n"
"v_mov_b32 v140, 0\n"
"v_mov_b32 v141, 0\n"
"v_mov_b32 v142, 0\n"
"v_mov_b32 v143, 0\n"
"v_mov_b32 v208, 0\n"
"v_mov_b32 v192, 0xff800000\n"
"v_mov_b32 v212, 0\n"
"v_mov_b32 v196, 0xff800000\n"
"v_mov_b32 v216, 0\n"
"v_mov_b32 v200, 0xff800000\n"
"v_mov_b32 v220, 0\n"
"v_mov_b32 v204, 0xff800000\n"
"s_mov_b32 s81, s72\n"
"s_mov_b32 s82, 32\n"
"L_slow_%=:\n"
"s_nop 11\n"
"s_barrier\n"
"s_mov_b32 m0, s76\n"
"s_nop 0\n"
"buffer_load_dwordx4 v5, s[40:43], s81 offen lds\n"
"buffer_load_dwordx4 v5, s[40:43], s81 offen offset:1024 lds\n"
"buffer_load_dwordx4 v6, s[40:43], s81 offen offset:2048 lds\n"
"buffer_load_dwordx4 v6, s[40:43], s81 offen offset:3072 lds\n"
"s_mov_b32 m0, s80\n"
"s_nop 0\n"
"buffer_load_dwordx4 v5, s[44:47], s81 offen lds\n"
"buffer_load_dwordx4 v6, s[44:47], s81 offen offset:1024 lds\n"
"buffer_load_dwordx4 v5, s[44:47], s81 offen offset:2048 lds\n"
"buffer_load_dwordx4 v6, s[44:47], s81 offen offset:3072 lds\n"
"s_add_u32 s81, s81, 16384\n"
"s_waitcnt vmcnt(0)\n"
"s_barrier\n"
"dK 192,2,49152\n"
"dK 196,2,49408\n"
"dK 200,2,49664\n"
"dK 204,2,49920\n"
"dK 208,2,53248\n"
"dK 212,2,53504\n"
"dK 216,2,53760\n"
"dK 220,2,54016\n"
"dK 224,2,57344\n"
"dK 228,2,57600\n"
"dK 232,2,57856\n"
"dK 236,2,58112\n"
"dK 240,2,61440\n"
"dK 244,2,61696\n"
"dK 248,2,61952\n"
"dK 252,2,62208\n"
"dT 128,3,49152\n"
"dT 130,3,53248\n"
"dT 132,4,49152\n"
"dT 134,4,53248\n"
"dT 136,3,49408\n"
"dT 138,3,53504\n"
"dT 140,4,49408\n"
"dT 142,4,53504\n"
"dT 144,3,49664\n"
"dT 146,3,53760\n"
"dT 148,4,49664\n"
"dT 150,4,53760\n"
"dT 152,3,49920\n"
"dT 154,3,54016\n"
"dT 156,4,49920\n"
"dT 158,4,54016\n"
"dT 160,3,57344\n"
"dT 162,3,61440\n"
"dT 164,4,57344\n"
"dT 166,4,61440\n"
"dT 168,3,57600\n"
"dT 170,3,61696\n"
"dT 172,4,57600\n"
"dT 174,4,61696\n"
"dT 176,3,57856\n"
"dT 178,3,61952\n"
"dT 180,4,57856\n"
"dT 182,4,61952\n"
"dT 184,3,58112\n"
"dT 186,3,62208\n"
"dT 188,4,58112\n"
"dT 190,4,62208\n"
"s_waitcnt lgkmcnt(0)\n"
"mZ 144,192,0\n"
"mA 144,196,4,144\n"
"mA 144,200,8,144\n"
"mA 144,204,12,144\n"
"mZ 148,208,0\n"
"mA 148,212,4,148\n"
"mA 148,216,8,148\n"
"mA 148,220,12,148\n"
"s_nop 3\n"
"v_max3_f32 v224, v144, v145, v146\n"
"s_nop 2\n"
"v_max3_f32 v225, v147, v148, v149\n"
"v_max3_f32 v224, v224, v150, v151\n"
"v_max3_f32 v224, v224, v225, v192\n"
"v_mov_b32 v225, v224\n"
"s_nop 1\n"
"v_permlane32_swap_b32 v224, v225\n"
"v_max_f32 v224, v224, v225\n"
"v_mov_b32 v225, v224\n"
"s_nop 1\n"
"v_permlane16_swap_b32 v224, v225\n"
"v_max_f32 v224, v224, v225\n"
"v_sub_f32 v226, v192, v224\n"
"eX 226,226\n"
"v_mov_b32 v192, v224\n"
"v_sub_f32 v144, v144, v224\n"
"v_sub_f32 v145, v145, v224\n"
"v_sub_f32 v146, v146, v224\n"
"v_sub_f32 v147, v147, v224\n"
"v_sub_f32 v148, v148, v224\n"
"v_sub_f32 v149, v149, v224\n"
"v_sub_f32 v150, v150, v224\n"
"v_sub_f32 v151, v151, v224\n"
"eX 144,144\n"
"eX 145,145\n"
"eX 146,146\n"
"eX 147,147\n"
"eX 148,148\n"
"eX 149,149\n"
"eX 150,150\n"
"eX 151,151\n"
"mU 208,208,226\n"
"mU 16,16,226\n"
"mU 17,17,226\n"
"mU 18,18,226\n"
"mU 19,19,226\n"
"mU 32,32,226\n"
"mU 33,33,226\n"
"mU 34,34,226\n"
"mU 35,35,226\n"
"mU 48,48,226\n"
"mU 49,49,226\n"
"mU 50,50,226\n"
"mU 51,51,226\n"
"mU 64,64,226\n"
"mU 65,65,226\n"
"mU 66,66,226\n"
"mU 67,67,226\n"
"mU 80,80,226\n"
"mU 81,81,226\n"
"mU 82,82,226\n"
"mU 83,83,226\n"
"mU 96,96,226\n"
"mU 97,97,226\n"
"mU 98,98,226\n"
"mU 99,99,226\n"
"mU 112,112,226\n"
"mU 113,113,226\n"
"mU 114,114,226\n"
"mU 115,115,226\n"
"mU 128,128,226\n"
"mU 129,129,226\n"
"mU 130,130,226\n"
"mU 131,131,226\n"
"aD 208,208,144\n"
"aD 208,208,145\n"
"aD 208,208,146\n"
"aD 208,208,147\n"
"aD 208,208,148\n"
"aD 208,208,149\n"
"aD 208,208,150\n"
"aD 208,208,151\n"
"cP 176,144,145\n"
"cP 177,146,147\n"
"cP 178,148,149\n"
"cP 179,150,151\n"
"s_nop 1\n"
"mB 16,128,176,16\n"
"mB 32,132,176,32\n"
"mB 48,136,176,48\n"
"mB 64,140,176,64\n"
"mB 80,144,176,80\n"
"mB 96,148,176,96\n"
"mB 112,152,176,112\n"
"mB 128,156,176,128\n"
"mZ 152,192,16\n"
"mA 152,196,20,152\n"
"mA 152,200,24,152\n"
"mA 152,204,28,152\n"
"mZ 156,208,16\n"
"mA 156,212,20,156\n"
"mA 156,216,24,156\n"
"mA 156,220,28,156\n"
"s_nop 3\n"
"v_max3_f32 v224, v152, v153, v154\n"
"s_nop 2\n"
"v_max3_f32 v225, v155, v156, v157\n"
"v_max3_f32 v224, v224, v158, v159\n"
"v_max3_f32 v224, v224, v225, v196\n"
"v_mov_b32 v225, v224\n"
"s_nop 1\n"
"v_permlane32_swap_b32 v224, v225\n"
"v_max_f32 v224, v224, v225\n"
"v_mov_b32 v225, v224\n"
"s_nop 1\n"
"v_permlane16_swap_b32 v224, v225\n"
"v_max_f32 v224, v224, v225\n"
"v_sub_f32 v226, v196, v224\n"
"eX 226,226\n"
"v_mov_b32 v196, v224\n"
"v_sub_f32 v152, v152, v224\n"
"v_sub_f32 v153, v153, v224\n"
"v_sub_f32 v154, v154, v224\n"
"v_sub_f32 v155, v155, v224\n"
"v_sub_f32 v156, v156, v224\n"
"v_sub_f32 v157, v157, v224\n"
"v_sub_f32 v158, v158, v224\n"
"v_sub_f32 v159, v159, v224\n"
"eX 152,152\n"
"eX 153,153\n"
"eX 154,154\n"
"eX 155,155\n"
"eX 156,156\n"
"eX 157,157\n"
"eX 158,158\n"
"eX 159,159\n"
"mU 212,212,226\n"
"mU 20,20,226\n"
"mU 21,21,226\n"
"mU 22,22,226\n"
"mU 23,23,226\n"
"mU 36,36,226\n"
"mU 37,37,226\n"
"mU 38,38,226\n"
"mU 39,39,226\n"
"mU 52,52,226\n"
"mU 53,53,226\n"
"mU 54,54,226\n"
"mU 55,55,226\n"
"mU 68,68,226\n"
"mU 69,69,226\n"
"mU 70,70,226\n"
"mU 71,71,226\n"
"mU 84,84,226\n"
"mU 85,85,226\n"
"mU 86,86,226\n"
"mU 87,87,226\n"
"mU 100,100,226\n"
"mU 101,101,226\n"
"mU 102,102,226\n"
"mU 103,103,226\n"
"mU 116,116,226\n"
"mU 117,117,226\n"
"mU 118,118,226\n"
"mU 119,119,226\n"
"mU 132,132,226\n"
"mU 133,133,226\n"
"mU 134,134,226\n"
"mU 135,135,226\n"
"aD 212,212,152\n"
"aD 212,212,153\n"
"aD 212,212,154\n"
"aD 212,212,155\n"
"aD 212,212,156\n"
"aD 212,212,157\n"
"aD 212,212,158\n"
"aD 212,212,159\n"
"cP 180,152,153\n"
"cP 181,154,155\n"
"cP 182,156,157\n"
"cP 183,158,159\n"
"s_nop 1\n"
"mB 20,128,180,20\n"
"mB 36,132,180,36\n"
"mB 52,136,180,52\n"
"mB 68,140,180,68\n"
"mB 84,144,180,84\n"
"mB 100,148,180,100\n"
"mB 116,152,180,116\n"
"mB 132,156,180,132\n"
"mZ 160,192,32\n"
"mA 160,196,36,160\n"
"mA 160,200,40,160\n"
"mA 160,204,44,160\n"
"mZ 164,208,32\n"
"mA 164,212,36,164\n"
"mA 164,216,40,164\n"
"mA 164,220,44,164\n"
"s_nop 3\n"
"v_max3_f32 v224, v160, v161, v162\n"
"s_nop 2\n"
"v_max3_f32 v225, v163, v164, v165\n"
"v_max3_f32 v224, v224, v166, v167\n"
"v_max3_f32 v224, v224, v225, v200\n"
"v_mov_b32 v225, v224\n"
"s_nop 1\n"
"v_permlane32_swap_b32 v224, v225\n"
"v_max_f32 v224, v224, v225\n"
"v_mov_b32 v225, v224\n"
"s_nop 1\n"
"v_permlane16_swap_b32 v224, v225\n"
"v_max_f32 v224, v224, v225\n"
"v_sub_f32 v226, v200, v224\n"
"eX 226,226\n"
"v_mov_b32 v200, v224\n"
"v_sub_f32 v160, v160, v224\n"
"v_sub_f32 v161, v161, v224\n"
"v_sub_f32 v162, v162, v224\n"
"v_sub_f32 v163, v163, v224\n"
"v_sub_f32 v164, v164, v224\n"
"v_sub_f32 v165, v165, v224\n"
"v_sub_f32 v166, v166, v224\n"
"v_sub_f32 v167, v167, v224\n"
"eX 160,160\n"
"eX 161,161\n"
"eX 162,162\n"
"eX 163,163\n"
"eX 164,164\n"
"eX 165,165\n"
"eX 166,166\n"
"eX 167,167\n"
"mU 216,216,226\n"
"mU 24,24,226\n"
"mU 25,25,226\n"
"mU 26,26,226\n"
"mU 27,27,226\n"
"mU 40,40,226\n"
"mU 41,41,226\n"
"mU 42,42,226\n"
"mU 43,43,226\n"
"mU 56,56,226\n"
"mU 57,57,226\n"
"mU 58,58,226\n"
"mU 59,59,226\n"
"mU 72,72,226\n"
"mU 73,73,226\n"
"mU 74,74,226\n"
"mU 75,75,226\n"
"mU 88,88,226\n"
"mU 89,89,226\n"
"mU 90,90,226\n"
"mU 91,91,226\n"
"mU 104,104,226\n"
"mU 105,105,226\n"
"mU 106,106,226\n"
"mU 107,107,226\n"
"mU 120,120,226\n"
"mU 121,121,226\n"
"mU 122,122,226\n"
"mU 123,123,226\n"
"mU 136,136,226\n"
"mU 137,137,226\n"
"mU 138,138,226\n"
"mU 139,139,226\n"
"aD 216,216,160\n"
"aD 216,216,161\n"
"aD 216,216,162\n"
"aD 216,216,163\n"
"aD 216,216,164\n"
"aD 216,216,165\n"
"aD 216,216,166\n"
"aD 216,216,167\n"
"cP 184,160,161\n"
"cP 185,162,163\n"
"cP 186,164,165\n"
"cP 187,166,167\n"
"s_nop 1\n"
"mB 24,128,184,24\n"
"mB 40,132,184,40\n"
"mB 56,136,184,56\n"
"mB 72,140,184,72\n"
"mB 88,144,184,88\n"
"mB 104,148,184,104\n"
"mB 120,152,184,120\n"
"mB 136,156,184,136\n"
"mZ 168,192,48\n"
"mA 168,196,52,168\n"
"mA 168,200,56,168\n"
"mA 168,204,60,168\n"
"mZ 172,208,48\n"
"mA 172,212,52,172\n"
"mA 172,216,56,172\n"
"mA 172,220,60,172\n"
"s_nop 3\n"
"v_max3_f32 v224, v168, v169, v170\n"
"s_nop 2\n"
"v_max3_f32 v225, v171, v172, v173\n"
"v_max3_f32 v224, v224, v174, v175\n"
"v_max3_f32 v224, v224, v225, v204\n"
"v_mov_b32 v225, v224\n"
"s_nop 1\n"
"v_permlane32_swap_b32 v224, v225\n"
"v_max_f32 v224, v224, v225\n"
"v_mov_b32 v225, v224\n"
"s_nop 1\n"
"v_permlane16_swap_b32 v224, v225\n"
"v_max_f32 v224, v224, v225\n"
"v_sub_f32 v226, v204, v224\n"
"eX 226,226\n"
"v_mov_b32 v204, v224\n"
"v_sub_f32 v168, v168, v224\n"
"v_sub_f32 v169, v169, v224\n"
"v_sub_f32 v170, v170, v224\n"
"v_sub_f32 v171, v171, v224\n"
"v_sub_f32 v172, v172, v224\n"
"v_sub_f32 v173, v173, v224\n"
"v_sub_f32 v174, v174, v224\n"
"v_sub_f32 v175, v175, v224\n"
"eX 168,168\n"
"eX 169,169\n"
"eX 170,170\n"
"eX 171,171\n"
"eX 172,172\n"
"eX 173,173\n"
"eX 174,174\n"
"eX 175,175\n"
"mU 220,220,226\n"
"mU 28,28,226\n"
"mU 29,29,226\n"
"mU 30,30,226\n"
"mU 31,31,226\n"
"mU 44,44,226\n"
"mU 45,45,226\n"
"mU 46,46,226\n"
"mU 47,47,226\n"
"mU 60,60,226\n"
"mU 61,61,226\n"
"mU 62,62,226\n"
"mU 63,63,226\n"
"mU 76,76,226\n"
"mU 77,77,226\n"
"mU 78,78,226\n"
"mU 79,79,226\n"
"mU 92,92,226\n"
"mU 93,93,226\n"
"mU 94,94,226\n"
"mU 95,95,226\n"
"mU 108,108,226\n"
"mU 109,109,226\n"
"mU 110,110,226\n"
"mU 111,111,226\n"
"mU 124,124,226\n"
"mU 125,125,226\n"
"mU 126,126,226\n"
"mU 127,127,226\n"
"mU 140,140,226\n"
"mU 141,141,226\n"
"mU 142,142,226\n"
"mU 143,143,226\n"
"aD 220,220,168\n"
"aD 220,220,169\n"
"aD 220,220,170\n"
"aD 220,220,171\n"
"aD 220,220,172\n"
"aD 220,220,173\n"
"aD 220,220,174\n"
"aD 220,220,175\n"
"cP 188,168,169\n"
"cP 189,170,171\n"
"cP 190,172,173\n"
"cP 191,174,175\n"
"s_nop 1\n"
"mB 28,128,188,28\n"
"mB 44,132,188,44\n"
"mB 60,136,188,60\n"
"mB 76,140,188,76\n"
"mB 92,144,188,92\n"
"mB 108,148,188,108\n"
"mB 124,152,188,124\n"
"mB 140,156,188,140\n"
"mZ 144,224,0\n"
"mA 144,228,4,144\n"
"mA 144,232,8,144\n"
"mA 144,236,12,144\n"
"mZ 148,240,0\n"
"mA 148,244,4,148\n"
"mA 148,248,8,148\n"
"mA 148,252,12,148\n"
"s_nop 3\n"
"v_max3_f32 v224, v144, v145, v146\n"
"s_nop 2\n"
"v_max3_f32 v225, v147, v148, v149\n"
"v_max3_f32 v224, v224, v150, v151\n"
"v_max3_f32 v224, v224, v225, v192\n"
"v_mov_b32 v225, v224\n"
"s_nop 1\n"
"v_permlane32_swap_b32 v224, v225\n"
"v_max_f32 v224, v224, v225\n"
"v_mov_b32 v225, v224\n"
"s_nop 1\n"
"v_permlane16_swap_b32 v224, v225\n"
"v_max_f32 v224, v224, v225\n"
"v_sub_f32 v226, v192, v224\n"
"eX 226,226\n"
"v_mov_b32 v192, v224\n"
"v_sub_f32 v144, v144, v224\n"
"v_sub_f32 v145, v145, v224\n"
"v_sub_f32 v146, v146, v224\n"
"v_sub_f32 v147, v147, v224\n"
"v_sub_f32 v148, v148, v224\n"
"v_sub_f32 v149, v149, v224\n"
"v_sub_f32 v150, v150, v224\n"
"v_sub_f32 v151, v151, v224\n"
"eX 144,144\n"
"eX 145,145\n"
"eX 146,146\n"
"eX 147,147\n"
"eX 148,148\n"
"eX 149,149\n"
"eX 150,150\n"
"eX 151,151\n"
"mU 208,208,226\n"
"mU 16,16,226\n"
"mU 17,17,226\n"
"mU 18,18,226\n"
"mU 19,19,226\n"
"mU 32,32,226\n"
"mU 33,33,226\n"
"mU 34,34,226\n"
"mU 35,35,226\n"
"mU 48,48,226\n"
"mU 49,49,226\n"
"mU 50,50,226\n"
"mU 51,51,226\n"
"mU 64,64,226\n"
"mU 65,65,226\n"
"mU 66,66,226\n"
"mU 67,67,226\n"
"mU 80,80,226\n"
"mU 81,81,226\n"
"mU 82,82,226\n"
"mU 83,83,226\n"
"mU 96,96,226\n"
"mU 97,97,226\n"
"mU 98,98,226\n"
"mU 99,99,226\n"
"mU 112,112,226\n"
"mU 113,113,226\n"
"mU 114,114,226\n"
"mU 115,115,226\n"
"mU 128,128,226\n"
"mU 129,129,226\n"
"mU 130,130,226\n"
"mU 131,131,226\n"
"aD 208,208,144\n"
"aD 208,208,145\n"
"aD 208,208,146\n"
"aD 208,208,147\n"
"aD 208,208,148\n"
"aD 208,208,149\n"
"aD 208,208,150\n"
"aD 208,208,151\n"
"cP 176,144,145\n"
"cP 177,146,147\n"
"cP 178,148,149\n"
"cP 179,150,151\n"
"s_nop 1\n"
"mB 16,160,176,16\n"
"mB 32,164,176,32\n"
"mB 48,168,176,48\n"
"mB 64,172,176,64\n"
"mB 80,176,176,80\n"
"mB 96,180,176,96\n"
"mB 112,184,176,112\n"
"mB 128,188,176,128\n"
"mZ 152,224,16\n"
"mA 152,228,20,152\n"
"mA 152,232,24,152\n"
"mA 152,236,28,152\n"
"mZ 156,240,16\n"
"mA 156,244,20,156\n"
"mA 156,248,24,156\n"
"mA 156,252,28,156\n"
"s_nop 3\n"
"v_max3_f32 v224, v152, v153, v154\n"
"s_nop 2\n"
"v_max3_f32 v225, v155, v156, v157\n"
"v_max3_f32 v224, v224, v158, v159\n"
"v_max3_f32 v224, v224, v225, v196\n"
"v_mov_b32 v225, v224\n"
"s_nop 1\n"
"v_permlane32_swap_b32 v224, v225\n"
"v_max_f32 v224, v224, v225\n"
"v_mov_b32 v225, v224\n"
"s_nop 1\n"
"v_permlane16_swap_b32 v224, v225\n"
"v_max_f32 v224, v224, v225\n"
"v_sub_f32 v226, v196, v224\n"
"eX 226,226\n"
"v_mov_b32 v196, v224\n"
"v_sub_f32 v152, v152, v224\n"
"v_sub_f32 v153, v153, v224\n"
"v_sub_f32 v154, v154, v224\n"
"v_sub_f32 v155, v155, v224\n"
"v_sub_f32 v156, v156, v224\n"
"v_sub_f32 v157, v157, v224\n"
"v_sub_f32 v158, v158, v224\n"
"v_sub_f32 v159, v159, v224\n"
"eX 152,152\n"
"eX 153,153\n"
"eX 154,154\n"
"eX 155,155\n"
"eX 156,156\n"
"eX 157,157\n"
"eX 158,158\n"
"eX 159,159\n"
"mU 212,212,226\n"
"mU 20,20,226\n"
"mU 21,21,226\n"
"mU 22,22,226\n"
"mU 23,23,226\n"
"mU 36,36,226\n"
"mU 37,37,226\n"
"mU 38,38,226\n"
"mU 39,39,226\n"
"mU 52,52,226\n"
"mU 53,53,226\n"
"mU 54,54,226\n"
"mU 55,55,226\n"
"mU 68,68,226\n"
"mU 69,69,226\n"
"mU 70,70,226\n"
"mU 71,71,226\n"
"mU 84,84,226\n"
"mU 85,85,226\n"
"mU 86,86,226\n"
"mU 87,87,226\n"
"mU 100,100,226\n"
"mU 101,101,226\n"
"mU 102,102,226\n"
"mU 103,103,226\n"
"mU 116,116,226\n"
"mU 117,117,226\n"
"mU 118,118,226\n"
"mU 119,119,226\n"
"mU 132,132,226\n"
"mU 133,133,226\n"
"mU 134,134,226\n"
"mU 135,135,226\n"
"aD 212,212,152\n"
"aD 212,212,153\n"
"aD 212,212,154\n"
"aD 212,212,155\n"
"aD 212,212,156\n"
"aD 212,212,157\n"
"aD 212,212,158\n"
"aD 212,212,159\n"
"cP 180,152,153\n"
"cP 181,154,155\n"
"cP 182,156,157\n"
"cP 183,158,159\n"
"s_nop 1\n"
"mB 20,160,180,20\n"
"mB 36,164,180,36\n"
"mB 52,168,180,52\n"
"mB 68,172,180,68\n"
"mB 84,176,180,84\n"
"mB 100,180,180,100\n"
"mB 116,184,180,116\n"
"mB 132,188,180,132\n"
"mZ 160,224,32\n"
"mA 160,228,36,160\n"
"mA 160,232,40,160\n"
"mA 160,236,44,160\n"
"mZ 164,240,32\n"
"mA 164,244,36,164\n"
"mA 164,248,40,164\n"
"mA 164,252,44,164\n"
"s_nop 3\n"
"v_max3_f32 v224, v160, v161, v162\n"
"s_nop 2\n"
"v_max3_f32 v225, v163, v164, v165\n"
"v_max3_f32 v224, v224, v166, v167\n"
"v_max3_f32 v224, v224, v225, v200\n"
"v_mov_b32 v225, v224\n"
"s_nop 1\n"
"v_permlane32_swap_b32 v224, v225\n"
"v_max_f32 v224, v224, v225\n"
"v_mov_b32 v225, v224\n"
"s_nop 1\n"
"v_permlane16_swap_b32 v224, v225\n"
"v_max_f32 v224, v224, v225\n"
"v_sub_f32 v226, v200, v224\n"
"eX 226,226\n"
"v_mov_b32 v200, v224\n"
"v_sub_f32 v160, v160, v224\n"
"v_sub_f32 v161, v161, v224\n"
"v_sub_f32 v162, v162, v224\n"
"v_sub_f32 v163, v163, v224\n"
"v_sub_f32 v164, v164, v224\n"
"v_sub_f32 v165, v165, v224\n"
"v_sub_f32 v166, v166, v224\n"
"v_sub_f32 v167, v167, v224\n"
"eX 160,160\n"
"eX 161,161\n"
"eX 162,162\n"
"eX 163,163\n"
"eX 164,164\n"
"eX 165,165\n"
"eX 166,166\n"
"eX 167,167\n"
"mU 216,216,226\n"
"mU 24,24,226\n"
"mU 25,25,226\n"
"mU 26,26,226\n"
"mU 27,27,226\n"
"mU 40,40,226\n"
"mU 41,41,226\n"
"mU 42,42,226\n"
"mU 43,43,226\n"
"mU 56,56,226\n"
"mU 57,57,226\n"
"mU 58,58,226\n"
"mU 59,59,226\n"
"mU 72,72,226\n"
"mU 73,73,226\n"
"mU 74,74,226\n"
"mU 75,75,226\n"
"mU 88,88,226\n"
"mU 89,89,226\n"
"mU 90,90,226\n"
"mU 91,91,226\n"
"mU 104,104,226\n"
"mU 105,105,226\n"
"mU 106,106,226\n"
"mU 107,107,226\n"
"mU 120,120,226\n"
"mU 121,121,226\n"
"mU 122,122,226\n"
"mU 123,123,226\n"
"mU 136,136,226\n"
"mU 137,137,226\n"
"mU 138,138,226\n"
"mU 139,139,226\n"
"aD 216,216,160\n"
"aD 216,216,161\n"
"aD 216,216,162\n"
"aD 216,216,163\n"
"aD 216,216,164\n"
"aD 216,216,165\n"
"aD 216,216,166\n"
"aD 216,216,167\n"
"cP 184,160,161\n"
"cP 185,162,163\n"
"cP 186,164,165\n"
"cP 187,166,167\n"
"s_nop 1\n"
"mB 24,160,184,24\n"
"mB 40,164,184,40\n"
"mB 56,168,184,56\n"
"mB 72,172,184,72\n"
"mB 88,176,184,88\n"
"mB 104,180,184,104\n"
"mB 120,184,184,120\n"
"mB 136,188,184,136\n"
"mZ 168,224,48\n"
"mA 168,228,52,168\n"
"mA 168,232,56,168\n"
"mA 168,236,60,168\n"
"mZ 172,240,48\n"
"mA 172,244,52,172\n"
"mA 172,248,56,172\n"
"mA 172,252,60,172\n"
"s_nop 3\n"
"v_max3_f32 v224, v168, v169, v170\n"
"s_nop 2\n"
"v_max3_f32 v225, v171, v172, v173\n"
"v_max3_f32 v224, v224, v174, v175\n"
"v_max3_f32 v224, v224, v225, v204\n"
"v_mov_b32 v225, v224\n"
"s_nop 1\n"
"v_permlane32_swap_b32 v224, v225\n"
"v_max_f32 v224, v224, v225\n"
"v_mov_b32 v225, v224\n"
"s_nop 1\n"
"v_permlane16_swap_b32 v224, v225\n"
"v_max_f32 v224, v224, v225\n"
"v_sub_f32 v226, v204, v224\n"
"eX 226,226\n"
"v_mov_b32 v204, v224\n"
"v_sub_f32 v168, v168, v224\n"
"v_sub_f32 v169, v169, v224\n"
"v_sub_f32 v170, v170, v224\n"
"v_sub_f32 v171, v171, v224\n"
"v_sub_f32 v172, v172, v224\n"
"v_sub_f32 v173, v173, v224\n"
"v_sub_f32 v174, v174, v224\n"
"v_sub_f32 v175, v175, v224\n"
"eX 168,168\n"
"eX 169,169\n"
"eX 170,170\n"
"eX 171,171\n"
"eX 172,172\n"
"eX 173,173\n"
"eX 174,174\n"
"eX 175,175\n"
"mU 220,220,226\n"
"mU 28,28,226\n"
"mU 29,29,226\n"
"mU 30,30,226\n"
"mU 31,31,226\n"
"mU 44,44,226\n"
"mU 45,45,226\n"
"mU 46,46,226\n"
"mU 47,47,226\n"
"mU 60,60,226\n"
"mU 61,61,226\n"
"mU 62,62,226\n"
"mU 63,63,226\n"
"mU 76,76,226\n"
"mU 77,77,226\n"
"mU 78,78,226\n"
"mU 79,79,226\n"
"mU 92,92,226\n"
"mU 93,93,226\n"
"mU 94,94,226\n"
"mU 95,95,226\n"
"mU 108,108,226\n"
"mU 109,109,226\n"
"mU 110,110,226\n"
"mU 111,111,226\n"
"mU 124,124,226\n"
"mU 125,125,226\n"
"mU 126,126,226\n"
"mU 127,127,226\n"
"mU 140,140,226\n"
"mU 141,141,226\n"
"mU 142,142,226\n"
"mU 143,143,226\n"
"aD 220,220,168\n"
"aD 220,220,169\n"
"aD 220,220,170\n"
"aD 220,220,171\n"
"aD 220,220,172\n"
"aD 220,220,173\n"
"aD 220,220,174\n"
"aD 220,220,175\n"
"cP 188,168,169\n"
"cP 189,170,171\n"
"cP 190,172,173\n"
"cP 191,174,175\n"
"s_nop 1\n"
"mB 28,160,188,28\n"
"mB 44,164,188,44\n"
"mB 60,168,188,60\n"
"mB 76,172,188,76\n"
"mB 92,176,188,92\n"
"mB 108,180,188,108\n"
"mB 124,184,188,124\n"
"mB 140,188,188,140\n"
"s_sub_u32 s82, s82, 1\n"
"s_cmp_lg_u32 s82, 0\n"
"s_cbranch_scc1 L_slow_%=\n"
"s_nop 11\n"
"v_mov_b32 v224, v208\n"
"s_nop 1\n"
"v_permlane32_swap_b32 v208, v224\n"
"aD 208,208,224\n"
"v_mov_b32 v224, v208\n"
"s_nop 1\n"
"v_permlane16_swap_b32 v208, v224\n"
"aD 208,208,224\n"
"v_mov_b32 v225, v212\n"
"s_nop 1\n"
"v_permlane32_swap_b32 v212, v225\n"
"aD 212,212,225\n"
"v_mov_b32 v225, v212\n"
"s_nop 1\n"
"v_permlane16_swap_b32 v212, v225\n"
"aD 212,212,225\n"
"v_mov_b32 v226, v216\n"
"s_nop 1\n"
"v_permlane32_swap_b32 v216, v226\n"
"aD 216,216,226\n"
"v_mov_b32 v226, v216\n"
"s_nop 1\n"
"v_permlane16_swap_b32 v216, v226\n"
"aD 216,216,226\n"
"v_mov_b32 v227, v220\n"
"s_nop 1\n"
"v_permlane32_swap_b32 v220, v227\n"
"aD 220,220,227\n"
"v_mov_b32 v227, v220\n"
"s_nop 1\n"
"v_permlane16_swap_b32 v220, v227\n"
"aD 220,220,227\n"
"s_lshl_b32 s68, s56, 19\n"
"s_add_u32 s69, s68, 4096\n"
"s_add_u32 s70, s68, 8192\n"
"s_add_u32 s71, s68, 12288\n"
"v_rcp_f32 v228, v208\n"
"s_nop 0\n"
"mU 16,228,16\n"
"mU 17,228,17\n"
"mU 18,228,18\n"
"mU 19,228,19\n"
"mU 32,228,32\n"
"mU 33,228,33\n"
"mU 34,228,34\n"
"mU 35,228,35\n"
"cP 16,16,17\n"
"cP 17,18,19\n"
"cP 18,32,33\n"
"cP 19,34,35\n"
"buffer_store_dwordx4 v[16:19], v7, s[48:51], s68 offen sc1\n"
"mU 48,228,48\n"
"mU 49,228,49\n"
"mU 50,228,50\n"
"mU 51,228,51\n"
"mU 64,228,64\n"
"mU 65,228,65\n"
"mU 66,228,66\n"
"mU 67,228,67\n"
"cP 48,48,49\n"
"cP 49,50,51\n"
"cP 50,64,65\n"
"cP 51,66,67\n"
"buffer_store_dwordx4 v[48:51], v7, s[48:51], s68 offen offset:1024 sc1\n"
"mU 80,228,80\n"
"mU 81,228,81\n"
"mU 82,228,82\n"
"mU 83,228,83\n"
"mU 96,228,96\n"
"mU 97,228,97\n"
"mU 98,228,98\n"
"mU 99,228,99\n"
"cP 80,80,81\n"
"cP 81,82,83\n"
"cP 82,96,97\n"
"cP 83,98,99\n"
"buffer_store_dwordx4 v[80:83], v7, s[48:51], s70 offen sc1\n"
"mU 112,228,112\n"
"mU 113,228,113\n"
"mU 114,228,114\n"
"mU 115,228,115\n"
"mU 128,228,128\n"
"mU 129,228,129\n"
"mU 130,228,130\n"
"mU 131,228,131\n"
"cP 112,112,113\n"
"cP 113,114,115\n"
"cP 114,128,129\n"
"cP 115,130,131\n"
"buffer_store_dwordx4 v[112:115], v7, s[48:51], s70 offen offset:1024 sc1\n"
"v_rcp_f32 v229, v212\n"
"s_nop 0\n"
"mU 20,229,20\n"
"mU 21,229,21\n"
"mU 22,229,22\n"
"mU 23,229,23\n"
"mU 36,229,36\n"
"mU 37,229,37\n"
"mU 38,229,38\n"
"mU 39,229,39\n"
"cP 20,20,21\n"
"cP 21,22,23\n"
"cP 22,36,37\n"
"cP 23,38,39\n"
"buffer_store_dwordx4 v[20:23], v7, s[48:51], s68 offen offset:2048 sc1\n"
"mU 52,229,52\n"
"mU 53,229,53\n"
"mU 54,229,54\n"
"mU 55,229,55\n"
"mU 68,229,68\n"
"mU 69,229,69\n"
"mU 70,229,70\n"
"mU 71,229,71\n"
"cP 52,52,53\n"
"cP 53,54,55\n"
"cP 54,68,69\n"
"cP 55,70,71\n"
"buffer_store_dwordx4 v[52:55], v7, s[48:51], s68 offen offset:3072 sc1\n"
"mU 84,229,84\n"
"mU 85,229,85\n"
"mU 86,229,86\n"
"mU 87,229,87\n"
"mU 100,229,100\n"
"mU 101,229,101\n"
"mU 102,229,102\n"
"mU 103,229,103\n"
"cP 84,84,85\n"
"cP 85,86,87\n"
"cP 86,100,101\n"
"cP 87,102,103\n"
"buffer_store_dwordx4 v[84:87], v7, s[48:51], s70 offen offset:2048 sc1\n"
"mU 116,229,116\n"
"mU 117,229,117\n"
"mU 118,229,118\n"
"mU 119,229,119\n"
"mU 132,229,132\n"
"mU 133,229,133\n"
"mU 134,229,134\n"
"mU 135,229,135\n"
"cP 116,116,117\n"
"cP 117,118,119\n"
"cP 118,132,133\n"
"cP 119,134,135\n"
"buffer_store_dwordx4 v[116:119], v7, s[48:51], s70 offen offset:3072 sc1\n"
"v_rcp_f32 v230, v216\n"
"s_nop 0\n"
"mU 24,230,24\n"
"mU 25,230,25\n"
"mU 26,230,26\n"
"mU 27,230,27\n"
"mU 40,230,40\n"
"mU 41,230,41\n"
"mU 42,230,42\n"
"mU 43,230,43\n"
"cP 24,24,25\n"
"cP 25,26,27\n"
"cP 26,40,41\n"
"cP 27,42,43\n"
"buffer_store_dwordx4 v[24:27], v7, s[48:51], s69 offen sc1\n"
"mU 56,230,56\n"
"mU 57,230,57\n"
"mU 58,230,58\n"
"mU 59,230,59\n"
"mU 72,230,72\n"
"mU 73,230,73\n"
"mU 74,230,74\n"
"mU 75,230,75\n"
"cP 56,56,57\n"
"cP 57,58,59\n"
"cP 58,72,73\n"
"cP 59,74,75\n"
"buffer_store_dwordx4 v[56:59], v7, s[48:51], s69 offen offset:1024 sc1\n"
"mU 88,230,88\n"
"mU 89,230,89\n"
"mU 90,230,90\n"
"mU 91,230,91\n"
"mU 104,230,104\n"
"mU 105,230,105\n"
"mU 106,230,106\n"
"mU 107,230,107\n"
"cP 88,88,89\n"
"cP 89,90,91\n"
"cP 90,104,105\n"
"cP 91,106,107\n"
"buffer_store_dwordx4 v[88:91], v7, s[48:51], s71 offen sc1\n"
"mU 120,230,120\n"
"mU 121,230,121\n"
"mU 122,230,122\n"
"mU 123,230,123\n"
"mU 136,230,136\n"
"mU 137,230,137\n"
"mU 138,230,138\n"
"mU 139,230,139\n"
"cP 120,120,121\n"
"cP 121,122,123\n"
"cP 122,136,137\n"
"cP 123,138,139\n"
"buffer_store_dwordx4 v[120:123], v7, s[48:51], s71 offen offset:1024 sc1\n"
"v_rcp_f32 v231, v220\n"
"s_nop 0\n"
"mU 28,231,28\n"
"mU 29,231,29\n"
"mU 30,231,30\n"
"mU 31,231,31\n"
"mU 44,231,44\n"
"mU 45,231,45\n"
"mU 46,231,46\n"
"mU 47,231,47\n"
"cP 28,28,29\n"
"cP 29,30,31\n"
"cP 30,44,45\n"
"cP 31,46,47\n"
"buffer_store_dwordx4 v[28:31], v7, s[48:51], s69 offen offset:2048 sc1\n"
"mU 60,231,60\n"
"mU 61,231,61\n"
"mU 62,231,62\n"
"mU 63,231,63\n"
"mU 76,231,76\n"
"mU 77,231,77\n"
"mU 78,231,78\n"
"mU 79,231,79\n"
"cP 60,60,61\n"
"cP 61,62,63\n"
"cP 62,76,77\n"
"cP 63,78,79\n"
"buffer_store_dwordx4 v[60:63], v7, s[48:51], s69 offen offset:3072 sc1\n"
"mU 92,231,92\n"
"mU 93,231,93\n"
"mU 94,231,94\n"
"mU 95,231,95\n"
"mU 108,231,108\n"
"mU 109,231,109\n"
"mU 110,231,110\n"
"mU 111,231,111\n"
"cP 92,92,93\n"
"cP 93,94,95\n"
"cP 94,108,109\n"
"cP 95,110,111\n"
"buffer_store_dwordx4 v[92:95], v7, s[48:51], s71 offen offset:2048 sc1\n"
"mU 124,231,124\n"
"mU 125,231,125\n"
"mU 126,231,126\n"
"mU 127,231,127\n"
"mU 140,231,140\n"
"mU 141,231,141\n"
"mU 142,231,142\n"
"mU 143,231,143\n"
"cP 124,124,125\n"
"cP 125,126,127\n"
"cP 126,140,141\n"
"cP 127,142,143\n"
"buffer_store_dwordx4 v[124:127], v7, s[48:51], s71 offen offset:3072 sc1\n"
"L_aend_%=:\n"
"s_nop 11\n"
"v_lshrrev_b32 v233, 4, v1\n"
"v_mul_u32_u24 v234, 1152, v233\n"
"v_and_b32 v232, 3, v1\n"
"v_lshl_add_u32 v234, v232, 3, v234\n"
"v_add_u32 v234, s57, v234\n"
"v_bfe_u32 v235, v1, 2, 2\n"
"v_lshl_add_u32 v4, v235, 5, v234\n"
"v_add_u32 v4, 1024, v4\n"
"v_lshl_add_u32 v234, v235, 6, v234\n"
"v_and_b32 v235, 1, v233\n"
"v_lshl_add_u32 v2, v235, 5, v234\n"
"v_xor_b32 v235, 1, v235\n"
"v_lshl_add_u32 v3, v235, 5, v234\n"
"v_add_u32 v5, 55296, v2\n"
"v_add_u32 v6, 55296, v3\n"
"v_add_u32 v8, 55296, v4\n"
"v_add_u32 v9, 110592, v2\n"
"v_add_u32 v10, 110592, v3\n"
"v_add_u32 v11, 110592, v4\n"
"s_and_b32 s62, s60, 1\n"
"s_lshl_b32 s62, s62, 3\n"
"v_add_u32 v12, s62, v7\n"
"s_mov_b32 s92, 0xbfb8aa3b\n"
"s_barrier\n"
"ds_read_b64_tr_b16 a[0:1], v2\n"
"dT 2,2,4608\n"
"ds_read_b64_tr_b16 a[4:5], v3\n"
"dT 6,3,4608\n"
"dT 8,2,256\n"
"dT 10,2,4864\n"
"dT 12,3,256\n"
"dT 14,3,4864\n"
"dT 16,2,512\n"
"dT 18,2,5120\n"
"dT 20,3,512\n"
"dT 22,3,5120\n"
"dT 24,2,768\n"
"dT 26,2,5376\n"
"dT 28,3,768\n"
"dT 30,3,5376\n"
"ds_read_b64_tr_b16 a[32:33], v4\n"
"dT 34,4,4608\n"
"s_waitcnt lgkmcnt(15)\n"
"v_mfma_f32_16x16x32_bf16 v[32:35], a[0:3], v[16:19], 0\n"
"dT 36,2,9216\n"
"v_mfma_f32_16x16x32_bf16 v[36:39], a[0:3], v[20:23], 0\n"
"v_mfma_f32_16x16x32_bf16 v[40:43], a[0:3], v[24:27], 0\n"
"dT 38,2,13824\n"
"v_mfma_f32_16x16x32_bf16 v[44:47], a[0:3], v[28:31], 0\n"
"s_waitcnt lgkmcnt(15)\n"
"v_mfma_f32_16x16x32_bf16 v[64:67], a[4:7], v[16:19], 0\n"
"dT 40,3,9216\n"
"v_mfma_f32_16x16x32_bf16 v[68:71], a[4:7], v[20:23], 0\n"
"v_mfma_f32_16x16x32_bf16 v[72:75], a[4:7], v[24:27], 0\n"
"dT 42,3,13824\n"
"v_mfma_f32_16x16x32_bf16 v[76:79], a[4:7], v[28:31], 0\n"
"s_waitcnt lgkmcnt(15)\n"
"v_mfma_f32_16x16x32_bf16 v[96:99], a[8:11], v[16:19], 0\n"
"dT 44,2,9472\n"
"v_mfma_f32_16x16x32_bf16 v[100:103], a[8:11], v[20:23], 0\n"
"v_mfma_f32_16x16x32_bf16 v[104:107], a[8:11], v[24:27], 0\n"
"dT 46,2,14080\n"
"v_mfma_f32_16x16x32_bf16 v[108:111], a[8:11], v[28:31], 0\n"
"s_waitcnt lgkmcnt(15)\n"
"v_mfma_f32_16x16x32_bf16 v[128:131], a[12:15], v[16:19], 0\n"
"dT 48,3,9472\n"
"v_mfma_f32_16x16x32_bf16 v[132:135], a[12:15], v[20:23], 0\n"
"v_mfma_f32_16x16x32_bf16 v[136:139], a[12:15], v[24:27], 0\n"
"dT 50,3,14080\n"
"v_mfma_f32_16x16x32_bf16 v[140:143], a[12:15], v[28:31], 0\n"
"s_waitcnt lgkmcnt(15)\n"
"v_mfma_f32_16x16x32_bf16 v[144:147], a[16:19], v[16:19], 0\n"
"dT 52,2,9728\n"
"v_mfma_f32_16x16x32_bf16 v[148:151], a[16:19], v[20:23], 0\n"
"v_mfma_f32_16x16x32_bf16 v[152:155], a[16:19], v[24:27], 0\n"
"dT 54,2,14336\n"
"v_mfma_f32_16x16x32_bf16 v[156:159], a[16:19], v[28:31], 0\n"
"s_waitcnt lgkmcnt(15)\n"
"v_mfma_f32_16x16x32_bf16 v[160:163], a[20:23], v[16:19], 0\n"
"dT 56,3,9728\n"
"v_mfma_f32_16x16x32_bf16 v[164:167], a[20:23], v[20:23], 0\n"
"v_mfma_f32_16x16x32_bf16 v[168:171], a[20:23], v[24:27], 0\n"
"dT 58,3,14336\n"
"v_mfma_f32_16x16x32_bf16 v[172:175], a[20:23], v[28:31], 0\n"
"s_waitcnt lgkmcnt(12)\n"
"v_mfma_f32_16x16x32_bf16 v[176:179], a[24:27], v[16:19], 0\n"
"dT 60,2,9984\n"
"v_mfma_f32_16x16x32_bf16 v[180:183], a[24:27], v[20:23], 0\n"
"v_mfma_f32_16x16x32_bf16 v[184:187], a[24:27], v[24:27], 0\n"
"dT 62,2,14592\n"
"v_mfma_f32_16x16x32_bf16 v[188:191], a[24:27], v[28:31], 0\n"
"v_mfma_f32_16x16x32_bf16 v[192:195], a[28:31], v[16:19], 0\n"
"dT 64,3,9984\n"
"v_mfma_f32_16x16x32_bf16 v[196:199], a[28:31], v[20:23], 0\n"
"v_mfma_f32_16x16x32_bf16 v[200:203], a[28:31], v[24:27], 0\n"
"dT 66,3,14592\n"
"v_mfma_f32_16x16x32_bf16 v[204:207], a[28:31], v[28:31], 0\n"
"v_mfma_f32_16x16x32_bf16 v[208:211], a[32:35], v[16:19], 0\n"
"dT 68,4,9216\n"
"v_mfma_f32_16x16x32_bf16 v[212:215], a[32:35], v[20:23], 0\n"
"v_mfma_f32_16x16x32_bf16 v[216:219], a[32:35], v[24:27], 0\n"
"dT 70,4,13824\n"
"v_mfma_f32_16x16x32_bf16 v[220:223], a[32:35], v[28:31], 0\n"
"s_lshl_b32 s62, s58, 8\n"
"s_add_u32 s62, s62, 67584\n"
"s_add_u32 s66, %[wslo], s62\n"
"s_addc_u32 s67, %[wshi], 0\n"
"s_waitcnt vmcnt(0) lgkmcnt(0)\n"
"s_barrier\n"
"s_cmp_eq_u32 s56, 0\n"
"s_cbranch_scc0 L_sg8_%=\n"
"s_lshl_b32 s62, s59, 2\n"
"s_add_u32 s62, s62, s60\n"
"s_lshl_b32 s62, s62, 3\n"
"v_mov_b32 v234, s62\n"
"v_mov_b32 v232, s98\n"
"v_mov_b32 v233, s99\n"
"s_mov_b64 exec, 1\n"
"global_store_dwordx2 v234, v[232:233], s[66:67] sc1\n"
"s_mov_b64 exec, -1\n"
"L_sg8_%=:\n"
"s_mul_i32 s62, s58, 2359296\n"
"s_mul_i32 s63, s59, 65536\n"
"s_add_u32 s62, s62, s63\n"
"s_add_u32 s36, %[wslo], 7340032\n"
"s_addc_u32 s37, %[wshi], 0\n"
"s_add_u32 s36, s36, s62\n"
"s_addc_u32 s37, s37, 0\n"
"s_and_b32 s37, s37, 0xffff\n"
"s_mov_b32 s38, 1638400\n"
"s_mov_b32 s39, 0x20000\n"
"s_mul_i32 s90, s56, 524288\n"
"s_mul_i32 s62, s58, 294912\n"
"s_mul_i32 s63, s59, 2359296\n"
"s_add_u32 s62, s62, s63\n"
"s_add_u32 s40, %[wslo], 26214400\n"
"s_addc_u32 s41, %[wshi], 0\n"
"s_add_u32 s40, s40, s62\n"
"s_addc_u32 s41, s41, 0\n"
"s_and_b32 s41, s41, 0xffff\n"
"s_mov_b32 s42, 294912\n"
"s_mov_b32 s43, 0x20000\n"
"s_lshr_b32 s62, s60, 1\n"
"s_mul_i32 s64, s62, 9\n"
"s_and_b32 s63, s60, 1\n"
"s_lshl_b32 s63, s63, 2\n"
"s_add_u32 s63, s63, s64\n"
"s_mul_i32 s65, s56, 73728\n"
"s_add_u32 s62, s63, 0\n"
"s_lshr_b32 s73, s62, 1\n"
"s_lshl_b32 s73, s73, 13\n"
"s_and_b32 s62, s62, 1\n"
"s_lshl_b32 s62, s62, 10\n"
"s_add_u32 s73, s73, s62\n"
"s_add_u32 s73, s73, s65\n"
"s_add_u32 s74, s73, 4096\n"
"s_add_u32 s62, s63, 1\n"
"s_lshr_b32 s75, s62, 1\n"
"s_lshl_b32 s75, s75, 13\n"
"s_and_b32 s62, s62, 1\n"
"s_lshl_b32 s62, s62, 10\n"
"s_add_u32 s75, s75, s62\n"
"s_add_u32 s75, s75, s65\n"
"s_add_u32 s76, s75, 4096\n"
"s_add_u32 s62, s63, 2\n"
"s_lshr_b32 s77, s62, 1\n"
"s_lshl_b32 s77, s77, 13\n"
"s_and_b32 s62, s62, 1\n"
"s_lshl_b32 s62, s62, 10\n"
"s_add_u32 s77, s77, s62\n"
"s_add_u32 s77, s77, s65\n"
"s_add_u32 s78, s77, 4096\n"
"s_add_u32 s62, s63, 3\n"
"s_lshr_b32 s79, s62, 1\n"
"s_lshl_b32 s79, s79, 13\n"
"s_and_b32 s62, s62, 1\n"
"s_lshl_b32 s62, s62, 10\n"
"s_add_u32 s79, s79, s62\n"
"s_add_u32 s79, s79, s65\n"
"s_add_u32 s80, s79, 4096\n"
"s_add_u32 s62, s64, 8\n"
"s_lshr_b32 s81, s62, 1\n"
"s_lshl_b32 s81, s81, 13\n"
"s_and_b32 s62, s62, 1\n"
"s_lshl_b32 s62, s62, 10\n"
"s_add_u32 s81, s81, s62\n"
"s_add_u32 s81, s81, s65\n"
"s_add_u32 s83, s81, 4096\n"
"mB 32,36,48,32\n"
"dT 0,2,18432\n"
"mB 36,36,52,36\n"
"s_add_u32 m0, s87, 46080\n"
"s_cmp_le_u32 s60, 0\n"
"s_cselect_b32 s65, 1, 0\n"
"s_mul_i32 s65, s65, 147456\n"
"s_add_u32 s65, s65, 36864\n"
"buffer_load_dwordx4 v248, s[52:55], s65 offen lds\n"
"mB 40,36,56,40\n"
"dT 2,2,23040\n"
"mB 44,36,60,44\n"
"mB 64,40,48,64\n"
"dT 4,3,18432\n"
"mB 68,40,52,68\n"
"mB 72,40,56,72\n"
"dT 6,3,23040\n"
"mB 76,40,60,76\n"
"s_add_u32 m0, s87, 50176\n"
"s_cmp_le_u32 s60, 0\n"
"s_cselect_b32 s65, 1, 0\n"
"s_mul_i32 s65, s65, 147456\n"
"s_add_u32 s65, s65, 36864\n"
"buffer_load_dwordx4 v249, s[52:55], s65 offen lds\n"
"mB 96,44,48,96\n"
"dT 8,2,18688\n"
"mB 100,44,52,100\n"
"mB 104,44,56,104\n"
"dT 10,2,23296\n"
"mB 108,44,60,108\n"
"mB 128,48,48,128\n"
"dT 12,3,18688\n"
"mB 132,48,52,132\n"
"s_mul_i32 s65, s88, 54272\n"
"s_add_u32 m0, s65, s89\n"
"s_cmp_le_u32 s60, 0\n"
"s_cselect_b32 s65, 1, 0\n"
"s_mul_i32 s65, s65, 147456\n"
"s_add_u32 s65, s65, 36864\n"
"buffer_load_dwordx4 v250, s[52:55], s65 offen lds\n"
"mB 136,48,56,136\n"
"dT 14,3,23296\n"
"mB 140,48,60,140\n"
"mB 144,52,48,144\n"
"dT 16,2,18944\n"
"mB 148,52,52,148\n"
"mB 152,52,56,152\n"
"dT 18,2,23552\n"
"mB 156,52,60,156\n"
"s_add_u32 m0, s87, 55296\n"
"s_cmp_le_u32 s60, 0\n"
"s_cselect_b32 s65, 1, 0\n"
"s_mul_i32 s65, s65, 147456\n"
"s_add_u32 s65, s65, 73728\n"
"buffer_load_dwordx4 v248, s[52:55], s65 offen lds\n"
"mB 160,56,48,160\n"
"dT 20,3,18944\n"
"mB 164,56,52,164\n"
"mB 168,56,56,168\n"
"dT 22,3,23552\n"
"mB 172,56,60,172\n"
"mB 176,60,48,176\n"
"dT 24,2,19200\n"
"mB 180,60,52,180\n"
"s_add_u32 m0, s87, 59392\n"
"s_cmp_le_u32 s60, 0\n"
"s_cselect_b32 s65, 1, 0\n"
"s_mul_i32 s65, s65, 147456\n"
"s_add_u32 s65, s65, 73728\n"
"buffer_load_dwordx4 v249, s[52:55], s65 offen lds\n"
"mB 184,60,56,184\n"
"dT 26,2,23808\n"
"mB 188,60,60,188\n"
"mB 192,64,48,192\n"
"dT 28,3,19200\n"
"mB 196,64,52,196\n"
"mB 200,64,56,200\n"
"dT 30,3,23808\n"
"mB 204,64,60,204\n"
"s_mul_i32 s65, s88, 63488\n"
"s_add_u32 m0, s65, s89\n"
"s_cmp_le_u32 s60, 0\n"
"s_cselect_b32 s65, 1, 0\n"
"s_mul_i32 s65, s65, 147456\n"
"s_add_u32 s65, s65, 73728\n"
"buffer_load_dwordx4 v250, s[52:55], s65 offen lds\n"
"mB 208,68,48,208\n"
"dT 32,4,18432\n"
"mB 212,68,52,212\n"
"mB 216,68,56,216\n"
"dT 34,4,23040\n"
"mB 220,68,60,220\n"
"s_add_u32 m0, s87, 64512\n"
"s_cmp_le_u32 s60, 0\n"
"s_cselect_b32 s65, 1, 0\n"
"s_mul_i32 s65, s65, 147456\n"
"s_add_u32 s65, s65, 110592\n"
"buffer_load_dwordx4 v248, s[52:55], s65 offen lds\n"
"s_add_u32 m0, s87, 68608\n"
"s_cmp_le_u32 s60, 0\n"
"s_cselect_b32 s65, 1, 0\n"
"s_mul_i32 s65, s65, 147456\n"
"s_add_u32 s65, s65, 110592\n"
"buffer_load_dwordx4 v249, s[52:55], s65 offen lds\n"
"s_mul_i32 s65, s88, 72704\n"
"s_add_u32 m0, s65, s89\n"
"s_cmp_le_u32 s60, 0\n"
"s_cselect_b32 s65, 1, 0\n"
"s_mul_i32 s65, s65, 147456\n"
"s_add_u32 s65, s65, 110592\n"
"buffer_load_dwordx4 v250, s[52:55], s65 offen lds\n"
"s_lshl_b32 s62, s58, 8\n"
"s_add_u32 s62, s62, 67584\n"
"s_add_u32 s66, %[wslo], s62\n"
"s_addc_u32 s67, %[wshi], 0\n"
"s_cmp_eq_u32 s56, 0\n"
"s_cbranch_scc0 L_wd9_%=\n"
"s_lshl_b32 s62, s59, 5\n"
"v_and_b32 v13, 3, v1\n"
"v_lshl_add_u32 v13, v13, 3, s62\n"
"s_mov_b32 s63, 131072\n"
"L_wp9_%=:\n"
"global_load_dwordx2 v[14:15], v13, s[66:67] sc1\n"
"s_waitcnt vmcnt(0)\n"
"v_cmp_eq_u64 vcc, s[98:99], v[14:15]\n"
"s_nop 1\n"
"s_cmp_eq_u64 vcc, -1\n"
"s_cbranch_scc1 L_wd9_%=\n"
"s_sleep 2\n"
"s_sub_u32 s63, s63, 1\n"
"s_cmp_lg_u32 s63, 0\n"
"s_cbranch_scc1 L_wp9_%=\n"
"L_wd9_%=:\n"
"s_barrier\n"
"s_cmp_le_u32 s60, 0\n"
"s_cselect_b32 s62, 1, 0\n"
"s_lshl_b32 s63, s62, 2\n"
"s_add_u32 s63, s63, 0\n"
"s_lshr_b32 s62, s63, 1\n"
"s_lshl_b32 s62, s62, 13\n"
"s_and_b32 s63, s63, 1\n"
"s_lshl_b32 s63, s63, 10\n"
"s_add_u32 s84, s90, s62\n"
"s_add_u32 s84, s84, s63\n"
"s_add_u32 s91, s84, 4096\n"
"buffer_load_dwordx4 a[136:139], v7, s[36:39], s84 offen sc1\n"
"buffer_load_dwordx4 a[140:143], v7, s[36:39], s84 offen offset:2048 sc1\n"
"buffer_load_dwordx4 a[144:147], v7, s[36:39], s91 offen sc1\n"
"buffer_load_dwordx4 a[148:151], v7, s[36:39], s91 offen offset:2048 sc1\n"
"s_cmp_le_u32 s60, 0\n"
"s_cselect_b32 s62, 1, 0\n"
"s_lshl_b32 s63, s62, 2\n"
"s_add_u32 s63, s63, 1\n"
"s_lshr_b32 s62, s63, 1\n"
"s_lshl_b32 s62, s62, 13\n"
"s_and_b32 s63, s63, 1\n"
"s_lshl_b32 s63, s63, 10\n"
"s_add_u32 s84, s90, s62\n"
"s_add_u32 s84, s84, s63\n"
"s_add_u32 s91, s84, 4096\n"
"buffer_load_dwordx4 a[152:155], v7, s[36:39], s84 offen sc1\n"
"buffer_load_dwordx4 a[156:159], v7, s[36:39], s84 offen offset:2048 sc1\n"
"buffer_load_dwordx4 a[160:163], v7, s[36:39], s91 offen sc1\n"
"buffer_load_dwordx4 a[164:167], v7, s[36:39], s91 offen offset:2048 sc1\n"
"s_waitcnt lgkmcnt(0)\n"
"mB 32,0,80,32\n"
"dT 36,2,27648\n"
"mB 36,0,84,36\n"
"s_cmp_le_u32 s60, 0\n"
"s_cselect_b32 s62, 1, 0\n"
"s_lshl_b32 s63, s62, 2\n"
"s_add_u32 s63, s63, 2\n"
"s_lshr_b32 s62, s63, 1\n"
"s_lshl_b32 s62, s62, 13\n"
"s_and_b32 s63, s63, 1\n"
"s_lshl_b32 s63, s63, 10\n"
"s_add_u32 s84, s90, s62\n"
"s_add_u32 s84, s84, s63\n"
"s_add_u32 s91, s84, 4096\n"
"buffer_load_dwordx4 a[168:171], v7, s[36:39], s84 offen sc1\n"
"mB 40,0,88,40\n"
"dT 38,2,32256\n"
"mB 44,0,92,44\n"
"mB 64,4,80,64\n"
"dT 40,3,27648\n"
"mB 68,4,84,68\n"
"mB 72,4,88,72\n"
"dT 42,3,32256\n"
"mB 76,4,92,76\n"
"buffer_load_dwordx4 a[172:175], v7, s[36:39], s84 offen offset:2048 sc1\n"
"mB 96,8,80,96\n"
"dT 44,2,27904\n"
"mB 100,8,84,100\n"
"mB 104,8,88,104\n"
"dT 46,2,32512\n"
"mB 108,8,92,108\n"
"mB 128,12,80,128\n"
"dT 48,3,27904\n"
"mB 132,12,84,132\n"
"buffer_load_dwordx4 a[176:179], v7, s[36:39], s91 offen sc1\n"
"mB 136,12,88,136\n"
"dT 50,3,32512\n"
"mB 140,12,92,140\n"
"mB 144,16,80,144\n"
"dT 52,2,28160\n"
"mB 148,16,84,148\n"
"mB 152,16,88,152\n"
"dT 54,2,32768\n"
"mB 156,16,92,156\n"
"buffer_load_dwordx4 a[180:183], v7, s[36:39], s91 offen offset:2048 sc1\n"
"mB 160,20,80,160\n"
"dT 56,3,28160\n"
"mB 164,20,84,164\n"
"mB 168,20,88,168\n"
"dT 58,3,32768\n"
"mB 172,20,92,172\n"
"mB 176,24,80,176\n"
"dT 60,2,28416\n"
"mB 180,24,84,180\n"
"s_add_u32 m0, s87, 110592\n"
"s_cmp_le_u32 s60, 2\n"
"s_cselect_b32 s65, 3, 2\n"
"s_mul_i32 s65, s65, 147456\n"
"s_add_u32 s65, s65, 0\n"
"buffer_load_dwordx4 v248, s[52:55], s65 offen lds\n"
"mB 184,24,88,184\n"
"dT 62,2,33024\n"
"mB 188,24,92,188\n"
"mB 192,28,80,192\n"
"dT 64,3,28416\n"
"mB 196,28,84,196\n"
"mB 200,28,88,200\n"
"dT 66,3,33024\n"
"mB 204,28,92,204\n"
"s_add_u32 m0, s87, 114688\n"
"s_cmp_le_u32 s60, 2\n"
"s_cselect_b32 s65, 3, 2\n"
"s_mul_i32 s65, s65, 147456\n"
"s_add_u32 s65, s65, 0\n"
"buffer_load_dwordx4 v249, s[52:55], s65 offen lds\n"
"mB 208,32,80,208\n"
"dT 68,4,27648\n"
"mB 212,32,84,212\n"
"mB 216,32,88,216\n"
"dT 70,4,32256\n"
"mB 220,32,92,220\n"
"s_waitcnt lgkmcnt(9)\n"
"mB 32,36,112,32\n"
"dT 0,2,36864\n"
"mB 36,36,116,36\n"
"s_mul_i32 s65, s88, 118784\n"
"s_add_u32 m0, s65, s89\n"
"s_cmp_le_u32 s60, 2\n"
"s_cselect_b32 s65, 3, 2\n"
"s_mul_i32 s65, s65, 147456\n"
"s_add_u32 s65, s65, 0\n"
"buffer_load_dwordx4 v250, s[52:55], s65 offen lds\n"
"mB 40,36,120,40\n"
"dT 2,2,41472\n"
"mB 44,36,124,44\n"
"mB 64,40,112,64\n"
"dT 4,3,36864\n"
"mB 68,40,116,68\n"
"mB 72,40,120,72\n"
"dT 6,3,41472\n"
"mB 76,40,124,76\n"
"s_cmp_le_u32 s60, 0\n"
"s_cselect_b32 s62, 1, 0\n"
"s_lshl_b32 s63, s62, 2\n"
"s_add_u32 s63, s63, 3\n"
"s_lshr_b32 s62, s63, 1\n"
"s_lshl_b32 s62, s62, 13\n"
"s_and_b32 s63, s63, 1\n"
"s_lshl_b32 s63, s63, 10\n"
"s_add_u32 s84, s90, s62\n"
"s_add_u32 s84, s84, s63\n"
"s_add_u32 s91, s84, 4096\n"
"buffer_load_dwordx4 a[184:187], v7, s[36:39], s84 offen sc1\n"
"mB 96,44,112,96\n"
"dT 8,2,37120\n"
"mB 100,44,116,100\n"
"mB 104,44,120,104\n"
"dT 10,2,41728\n"
"mB 108,44,124,108\n"
"mB 128,48,112,128\n"
"dT 12,3,37120\n"
"mB 132,48,116,132\n"
"buffer_load_dwordx4 a[188:191], v7, s[36:39], s84 offen offset:2048 sc1\n"
"mB 136,48,120,136\n"
"dT 14,3,41728\n"
"mB 140,48,124,140\n"
"s_waitcnt lgkmcnt(7)\n"
"mB 144,52,112,144\n"
"dT 16,2,37376\n"
"mB 148,52,116,148\n"
"mB 152,52,120,152\n"
"dT 18,2,41984\n"
"mB 156,52,124,156\n"
"buffer_load_dwordx4 a[192:195], v7, s[36:39], s91 offen sc1\n"
"mB 160,56,112,160\n"
"dT 20,3,37376\n"
"mB 164,56,116,164\n"
"mB 168,56,120,168\n"
"dT 22,3,41984\n"
"mB 172,56,124,172\n"
"mB 176,60,112,176\n"
"dT 24,2,37632\n"
"mB 180,60,116,180\n"
"buffer_load_dwordx4 a[196:199], v7, s[36:39], s91 offen offset:2048 sc1\n"
"mB 184,60,120,184\n"
"dT 26,2,42240\n"
"mB 188,60,124,188\n"
"mB 192,64,112,192\n"
"dT 28,3,37632\n"
"mB 196,64,116,196\n"
"mB 200,64,120,200\n"
"dT 30,3,42240\n"
"mB 204,64,124,204\n"
"s_add_u32 m0, s87, 119808\n"
"s_cmp_le_u32 s60, 2\n"
"s_cselect_b32 s65, 3, 2\n"
"s_mul_i32 s65, s65, 147456\n"
"s_add_u32 s65, s65, 36864\n"
"buffer_load_dwordx4 v248, s[52:55], s65 offen lds\n"
"mB 208,68,112,208\n"
"dT 32,4,36864\n"
"mB 212,68,116,212\n"
"mB 216,68,120,216\n"
"dT 34,4,41472\n"
"mB 220,68,124,220\n"
"s_waitcnt vmcnt(16)\n"
"s_barrier\n"
"s_waitcnt lgkmcnt(10)\n"
"mA 32,0,136,32\n"
"dT 36,2,46080\n"
"mA 36,0,140,36\n"
"s_add_u32 m0, s87, 123904\n"
"s_cmp_le_u32 s60, 2\n"
"s_cselect_b32 s65, 3, 2\n"
"s_mul_i32 s65, s65, 147456\n"
"s_add_u32 s65, s65, 36864\n"
"buffer_load_dwordx4 v249, s[52:55], s65 offen lds\n"
"mA 40,0,144,40\n"
"dT 38,2,50688\n"
"mA 44,0,148,44\n"
"mA 64,4,136,64\n"
"dT 40,3,46080\n"
"mA 68,4,140,68\n"
"mA 72,4,144,72\n"
"dT 42,3,50688\n"
"mA 76,4,148,76\n"
"s_mul_i32 s65, s88, 128000\n"
"s_add_u32 m0, s65, s89\n"
"s_cmp_le_u32 s60, 2\n"
"s_cselect_b32 s65, 3, 2\n"
"s_mul_i32 s65, s65, 147456\n"
"s_add_u32 s65, s65, 36864\n"
"buffer_load_dwordx4 v250, s[52:55], s65 offen lds\n"
"mA 96,8,136,96\n"
"dT 44,2,46336\n"
"mA 100,8,140,100\n"
"mA 104,8,144,104\n"
"dT 46,2,50944\n"
"mA 108,8,148,108\n"
"mA 128,12,136,128\n"
"dT 48,3,46336\n"
"mA 132,12,140,132\n"
"s_cmp_le_u32 s60, 1\n"
"s_cselect_b32 s62, 2, 1\n"
"s_lshl_b32 s63, s62, 2\n"
"s_add_u32 s63, s63, 0\n"
"s_lshr_b32 s62, s63, 1\n"
"s_lshl_b32 s62, s62, 13\n"
"s_and_b32 s63, s63, 1\n"
"s_lshl_b32 s63, s63, 10\n"
"s_add_u32 s84, s90, s62\n"
"s_add_u32 s84, s84, s63\n"
"s_add_u32 s91, s84, 4096\n"
"buffer_load_dwordx4 a[200:203], v7, s[36:39], s84 offen sc1\n"
"mA 136,12,144,136\n"
"dT 50,3,50944\n"
"mA 140,12,148,140\n"
"s_waitcnt lgkmcnt(7)\n"
"mA 144,16,136,144\n"
"dT 52,2,46592\n"
"mA 148,16,140,148\n"
"mA 152,16,144,152\n"
"dT 54,2,51200\n"
"mA 156,16,148,156\n"
"buffer_load_dwordx4 a[204:207], v7, s[36:39], s84 offen offset:2048 sc1\n"
"mA 160,20,136,160\n"
"dT 56,3,46592\n"
"mA 164,20,140,164\n"
"mA 168,20,144,168\n"
"dT 58,3,51200\n"
"mA 172,20,148,172\n"
"mA 176,24,136,176\n"
"dT 60,2,46848\n"
"mA 180,24,140,180\n"
"buffer_load_dwordx4 a[208:211], v7, s[36:39], s91 offen sc1\n"
"mA 184,24,144,184\n"
"dT 62,2,51456\n"
"mA 188,24,148,188\n"
"mA 192,28,136,192\n"
"dT 64,3,46848\n"
"mA 196,28,140,196\n"
"mA 200,28,144,200\n"
"dT 66,3,51456\n"
"mA 204,28,148,204\n"
"buffer_load_dwordx4 a[212:215], v7, s[36:39], s91 offen offset:2048 sc1\n"
"mA 208,32,136,208\n"
"dT 68,4,46080\n"
"mA 212,32,140,212\n"
"mA 216,32,144,216\n"
"dT 70,4,50688\n"
"mA 220,32,148,220\n"
"s_waitcnt vmcnt(21) lgkmcnt(11)\n"
"mA 32,36,152,32\n"
"ds_read_b64_tr_b16 a[0:1], v5\n"
"s_waitcnt vmcnt(20)\n"
"mA 36,36,156,36\n"
"s_add_u32 m0, s87, 129024\n"
"s_cmp_le_u32 s60, 2\n"
"s_cselect_b32 s65, 3, 2\n"
"s_mul_i32 s65, s65, 147456\n"
"s_add_u32 s65, s65, 73728\n"
"buffer_load_dwordx4 v248, s[52:55], s65 offen lds\n"
"s_waitcnt vmcnt(20)\n"
"mA 40,36,160,40\n"
"dT 2,5,4608\n"
"s_waitcnt vmcnt(19)\n"
"mA 44,36,164,44\n"
"mA 64,40,152,64\n"
"ds_read_b64_tr_b16 a[4:5], v6\n"
"mA 68,40,156,68\n"
"mA 72,40,160,72\n"
"dT 6,6,4608\n"
"mA 76,40,164,76\n"
"s_add_u32 m0, s87, 133120\n"
"s_cmp_le_u32 s60, 2\n"
"s_cselect_b32 s65, 3, 2\n"
"s_mul_i32 s65, s65, 147456\n"
"s_add_u32 s65, s65, 73728\n"
"buffer_load_dwordx4 v249, s[52:55], s65 offen lds\n"
"mA 96,44,152,96\n"
"dT 8,5,256\n"
"mA 100,44,156,100\n"
"mA 104,44,160,104\n"
"dT 10,5,4864\n"
"mA 108,44,164,108\n"
"s_waitcnt lgkmcnt(8)\n"
"mA 128,48,152,128\n"
"dT 12,6,256\n"
"mA 132,48,156,132\n"
"s_mul_i32 s65, s88, 137216\n"
"s_add_u32 m0, s65, s89\n"
"s_cmp_le_u32 s60, 2\n"
"s_cselect_b32 s65, 3, 2\n"
"s_mul_i32 s65, s65, 147456\n"
"s_add_u32 s65, s65, 73728\n"
"buffer_load_dwordx4 v250, s[52:55], s65 offen lds\n"
"mA 136,48,160,136\n"
"dT 14,6,4864\n"
"mA 140,48,164,140\n"
"mA 144,52,152,144\n"
"dT 16,5,512\n"
"mA 148,52,156,148\n"
"mA 152,52,160,152\n"
"dT 18,5,5120\n"
"mA 156,52,164,156\n"
"s_cmp_le_u32 s60, 1\n"
"s_cselect_b32 s62, 2, 1\n"
"s_lshl_b32 s63, s62, 2\n"
"s_add_u32 s63, s63, 1\n"
"s_lshr_b32 s62, s63, 1\n"
"s_lshl_b32 s62, s62, 13\n"
"s_and_b32 s63, s63, 1\n"
"s_lshl_b32 s63, s63, 10\n"
"s_add_u32 s84, s90, s62\n"
"s_add_u32 s84, s84, s63\n"
"s_add_u32 s91, s84, 4096\n"
"buffer_load_dwordx4 a[216:219], v7, s[36:39], s84 offen sc1\n"
"mA 160,56,152,160\n"
"dT 20,6,512\n"
"mA 164,56,156,164\n"
"mA 168,56,160,168\n"
"dT 22,6,5120\n"
"mA 172,56,164,172\n"
"mA 176,60,152,176\n"
"dT 24,5,768\n"
"mA 180,60,156,180\n"
"buffer_load_dwordx4 a[220:223], v7, s[36:39], s84 offen offset:2048 sc1\n"
"mA 184,60,160,184\n"
"dT 26,5,5376\n"
"mA 188,60,164,188\n"
"mA 192,64,152,192\n"
"dT 28,6,768\n"
"mA 196,64,156,196\n"
"mA 200,64,160,200\n"
"dT 30,6,5376\n"
"mA 204,64,164,204\n"
"buffer_load_dwordx4 a[224:227], v7, s[36:39], s91 offen sc1\n"
"s_waitcnt lgkmcnt(9)\n"
"mA 208,68,152,208\n"
"ds_read_b64_tr_b16 a[32:33], v8\n"
"mA 212,68,156,212\n"
"mA 216,68,160,216\n"
"dT 34,8,4608\n"
"mA 220,68,164,220\n"
"s_waitcnt vmcnt(23)\n"
"mA 32,0,168,32\n"
"dT 36,5,9216\n"
"s_waitcnt vmcnt(22)\n"
"mA 36,0,172,36\n"
"buffer_load_dwordx4 a[228:231], v7, s[36:39], s91 offen offset:2048 sc1\n"
"s_waitcnt vmcnt(22)\n"
"mA 40,0,176,40\n"
"dT 38,5,13824\n"
"s_waitcnt vmcnt(21)\n"
"mA 44,0,180,44\n"
"mA 64,4,168,64\n"
"dT 40,6,9216\n"
"mA 68,4,172,68\n"
"mA 72,4,176,72\n"
"dT 42,6,13824\n"
"mA 76,4,180,76\n"
"s_cmp_le_u32 s60, 1\n"
"s_cselect_b32 s62, 2, 1\n"
"s_lshl_b32 s63, s62, 2\n"
"s_add_u32 s63, s63, 2\n"
"s_lshr_b32 s62, s63, 1\n"
"s_lshl_b32 s62, s62, 13\n"
"s_and_b32 s63, s63, 1\n"
"s_lshl_b32 s63, s63, 10\n"
"s_add_u32 s84, s90, s62\n"
"s_add_u32 s84, s84, s63\n"
"s_add_u32 s91, s84, 4096\n"
"buffer_load_dwordx4 a[232:235], v7, s[36:39], s84 offen sc1\n"
"mA 96,8,168,96\n"
"dT 44,5,9472\n"
"mA 100,8,172,100\n"
"mA 104,8,176,104\n"
"dT 46,5,14080\n"
"mA 108,8,180,108\n"
"s_waitcnt lgkmcnt(8)\n"
"mA 128,12,168,128\n"
"dT 48,6,9472\n"
"mA 132,12,172,132\n"
"buffer_load_dwordx4 a[236:239], v7, s[36:39], s84 offen offset:2048 sc1\n"
"mA 136,12,176,136\n"
"dT 50,6,14080\n"
"mA 140,12,180,140\n"
"mA 144,16,168,144\n"
"dT 52,5,9728\n"
"mA 148,16,172,148\n"
"mA 152,16,176,152\n"
"dT 54,5,14336\n"
"mA 156,16,180,156\n"
"buffer_load_dwordx4 a[240:243], v7, s[36:39], s91 offen sc1\n"
"mA 160,20,168,160\n"
"dT 56,6,9728\n"
"mA 164,20,172,164\n"
"mA 168,20,176,168\n"
"dT 58,6,14336\n"
"mA 172,20,180,172\n"
"mA 176,24,168,176\n"
"dT 60,5,9984\n"
"mA 180,24,172,180\n"
"buffer_load_dwordx4 a[244:247], v7, s[36:39], s91 offen offset:2048 sc1\n"
"mA 184,24,176,184\n"
"dT 62,5,14592\n"
"mA 188,24,180,188\n"
"mA 192,28,168,192\n"
"dT 64,6,9984\n"
"mA 196,28,172,196\n"
"mA 200,28,176,200\n"
"dT 66,6,14592\n"
"mA 204,28,180,204\n"
"s_cmp_le_u32 s60, 1\n"
"s_cselect_b32 s62, 2, 1\n"
"s_lshl_b32 s63, s62, 2\n"
"s_add_u32 s63, s63, 3\n"
"s_lshr_b32 s62, s63, 1\n"
"s_lshl_b32 s62, s62, 13\n"
"s_and_b32 s63, s63, 1\n"
"s_lshl_b32 s63, s63, 10\n"
"s_add_u32 s84, s90, s62\n"
"s_add_u32 s84, s84, s63\n"
"s_add_u32 s91, s84, 4096\n"
"buffer_load_dwordx4 a[72:75], v7, s[36:39], s84 offen sc1\n"
"s_waitcnt lgkmcnt(9)\n"
"mA 208,32,168,208\n"
"dT 68,8,9216\n"
"mA 212,32,172,212\n"
"mA 216,32,176,216\n"
"dT 70,8,13824\n"
"mA 220,32,180,220\n"
"s_waitcnt vmcnt(22)\n"
"mA 32,36,184,32\n"
"dT 0,5,18432\n"
"s_waitcnt vmcnt(21)\n"
"mA 36,36,188,36\n"
"buffer_load_dwordx4 a[76:79], v7, s[36:39], s84 offen offset:2048 sc1\n"
"s_waitcnt vmcnt(21)\n"
"mA 40,36,192,40\n"
"dT 2,5,23040\n"
"s_waitcnt vmcnt(20)\n"
"mA 44,36,196,44\n"
"mA 64,40,184,64\n"
"dT 4,6,18432\n"
"mA 68,40,188,68\n"
"mA 72,40,192,72\n"
"dT 6,6,23040\n"
"mA 76,40,196,76\n"
"buffer_load_dwordx4 a[80:83], v7, s[36:39], s91 offen sc1\n"
"mA 96,44,184,96\n"
"dT 8,5,18688\n"
"mA 100,44,188,100\n"
"mA 104,44,192,104\n"
"dT 10,5,23296\n"
"mA 108,44,196,108\n"
"s_waitcnt lgkmcnt(8)\n"
"mA 128,48,184,128\n"
"dT 12,6,18688\n"
"mA 132,48,188,132\n"
"buffer_load_dwordx4 a[84:87], v7, s[36:39], s91 offen offset:2048 sc1\n"
"mA 136,48,192,136\n"
"dT 14,6,23296\n"
"mA 140,48,196,140\n"
"mA 144,52,184,144\n"
"dT 16,5,18944\n"
"mA 148,52,188,148\n"
"mA 152,52,192,152\n"
"dT 18,5,23552\n"
"mA 156,52,196,156\n"
"s_cmp_le_u32 s60, 2\n"
"s_cselect_b32 s62, 3, 2\n"
"s_lshl_b32 s63, s62, 2\n"
"s_add_u32 s63, s63, 0\n"
"s_lshr_b32 s62, s63, 1\n"
"s_lshl_b32 s62, s62, 13\n"
"s_and_b32 s63, s63, 1\n"
"s_lshl_b32 s63, s63, 10\n"
"s_add_u32 s84, s90, s62\n"
"s_add_u32 s84, s84, s63\n"
"s_add_u32 s91, s84, 4096\n"
"buffer_load_dwordx4 a[88:91], v7, s[36:39], s84 offen sc1\n"
"mA 160,56,184,160\n"
"dT 20,6,18944\n"
"mA 164,56,188,164\n"
"mA 168,56,192,168\n"
"dT 22,6,23552\n"
"mA 172,56,196,172\n"
"mA 176,60,184,176\n"
"dT 24,5,19200\n"
"mA 180,60,188,180\n"
"buffer_load_dwordx4 a[92:95], v7, s[36:39], s84 offen offset:2048 sc1\n"
"mA 184,60,192,184\n"
"dT 26,5,23808\n"
"mA 188,60,196,188\n"
"mA 192,64,184,192\n"
"dT 28,6,19200\n"
"mA 196,64,188,196\n"
"mA 200,64,192,200\n"
"dT 30,6,23808\n"
"mA 204,64,196,204\n"
"buffer_load_dwordx4 a[96:99], v7, s[36:39], s91 offen sc1\n"
"s_waitcnt lgkmcnt(9)\n"
"mA 208,68,184,208\n"
"dT 32,8,18432\n"
"mA 212,68,188,212\n"
"mA 216,68,192,216\n"
"dT 34,8,23040\n"
"mA 220,68,196,220\n"
"s_waitcnt vmcnt(21)\n"
"mA 32,0,200,32\n"
"dT 36,5,27648\n"
"s_waitcnt vmcnt(20)\n"
"mA 36,0,204,36\n"
"buffer_load_dwordx4 a[100:103], v7, s[36:39], s91 offen offset:2048 sc1\n"
"s_waitcnt vmcnt(20)\n"
"mA 40,0,208,40\n"
"dT 38,5,32256\n"
"s_waitcnt vmcnt(19)\n"
"mA 44,0,212,44\n"
"mA 64,4,200,64\n"
"dT 40,6,27648\n"
"mA 68,4,204,68\n"
"mA 72,4,208,72\n"
"dT 42,6,32256\n"
"mA 76,4,212,76\n"
"s_cmp_le_u32 s60, 2\n"
"s_cselect_b32 s62, 3, 2\n"
"s_lshl_b32 s63, s62, 2\n"
"s_add_u32 s63, s63, 1\n"
"s_lshr_b32 s62, s63, 1\n"
"s_lshl_b32 s62, s62, 13\n"
"s_and_b32 s63, s63, 1\n"
"s_lshl_b32 s63, s63, 10\n"
"s_add_u32 s84, s90, s62\n"
"s_add_u32 s84, s84, s63\n"
"s_add_u32 s91, s84, 4096\n"
"buffer_load_dwordx4 a[104:107], v7, s[36:39], s84 offen sc1\n"
"mA 96,8,200,96\n"
"dT 44,5,27904\n"
"mA 100,8,204,100\n"
"mA 104,8,208,104\n"
"dT 46,5,32512\n"
"mA 108,8,212,108\n"
"s_waitcnt lgkmcnt(8)\n"
"mA 128,12,200,128\n"
"dT 48,6,27904\n"
"mA 132,12,204,132\n"
"buffer_load_dwordx4 a[108:111], v7, s[36:39], s84 offen offset:2048 sc1\n"
"mA 136,12,208,136\n"
"dT 50,6,32512\n"
"mA 140,12,212,140\n"
"mA 144,16,200,144\n"
"dT 52,5,28160\n"
"mA 148,16,204,148\n"
"mA 152,16,208,152\n"
"dT 54,5,32768\n"
"mA 156,16,212,156\n"
"buffer_load_dwordx4 a[112:115], v7, s[36:39], s91 offen sc1\n"
"mA 160,20,200,160\n"
"dT 56,6,28160\n"
"mA 164,20,204,164\n"
"mA 168,20,208,168\n"
"dT 58,6,32768\n"
"mA 172,20,212,172\n"
"mA 176,24,200,176\n"
"dT 60,5,28416\n"
"mA 180,24,204,180\n"
"buffer_load_dwordx4 a[116:119], v7, s[36:39], s91 offen offset:2048 sc1\n"
"mA 184,24,208,184\n"
"dT 62,5,33024\n"
"mA 188,24,212,188\n"
"mA 192,28,200,192\n"
"dT 64,6,28416\n"
"mA 196,28,204,196\n"
"mA 200,28,208,200\n"
"dT 66,6,33024\n"
"mA 204,28,212,204\n"
"s_cmp_le_u32 s60, 2\n"
"s_cselect_b32 s62, 3, 2\n"
"s_lshl_b32 s63, s62, 2\n"
"s_add_u32 s63, s63, 2\n"
"s_lshr_b32 s62, s63, 1\n"
"s_lshl_b32 s62, s62, 13\n"
"s_and_b32 s63, s63, 1\n"
"s_lshl_b32 s63, s63, 10\n"
"s_add_u32 s84, s90, s62\n"
"s_add_u32 s84, s84, s63\n"
"s_add_u32 s91, s84, 4096\n"
"buffer_load_dwordx4 a[120:123], v7, s[36:39], s84 offen sc1\n"
"s_waitcnt lgkmcnt(9)\n"
"mA 208,32,200,208\n"
"dT 68,8,27648\n"
"mA 212,32,204,212\n"
"mA 216,32,208,216\n"
"dT 70,8,32256\n"
"mA 220,32,212,220\n"
"s_waitcnt vmcnt(21)\n"
"s_barrier\n"
"s_waitcnt lgkmcnt(0)\n"
"s_barrier\n"
"s_waitcnt vmcnt(20)\n"
"mA 32,36,216,32\n"
"dT 0,5,36864\n"
"s_waitcnt vmcnt(19)\n"
"mA 36,36,220,36\n"
"buffer_load_dwordx4 a[124:127], v7, s[36:39], s84 offen offset:2048 sc1\n"
"s_waitcnt vmcnt(19)\n"
"mA 40,36,224,40\n"
"dT 2,5,41472\n"
"s_waitcnt vmcnt(18)\n"
"mA 44,36,228,44\n"
"mA 64,40,216,64\n"
"dT 4,5,46080\n"
"mA 68,40,220,68\n"
"mA 72,40,224,72\n"
"dT 6,5,50688\n"
"mA 76,40,228,76\n"
"buffer_load_dwordx4 a[128:131], v7, s[36:39], s91 offen sc1\n"
"mA 96,44,216,96\n"
"ds_read_b64_tr_b16 a[8:9], v9\n"
"mA 100,44,220,100\n"
"mA 104,44,224,104\n"
"dT 10,9,4608\n"
"mA 108,44,228,108\n"
"mA 128,48,216,128\n"
"dT 12,9,9216\n"
"mA 132,48,220,132\n"
"buffer_load_dwordx4 a[132:135], v7, s[36:39], s91 offen offset:2048 sc1\n"
"mA 136,48,224,136\n"
"dT 14,9,13824\n"
"mA 140,48,228,140\n"
"mA 144,52,216,144\n"
"dT 16,9,18432\n"
"mA 148,52,220,148\n"
"mA 152,52,224,152\n"
"dT 18,9,23040\n"
"mA 156,52,228,156\n"
"s_cmp_le_u32 s60, 2\n"
"s_cselect_b32 s62, 3, 2\n"
"s_lshl_b32 s63, s62, 2\n"
"s_add_u32 s63, s63, 3\n"
"s_lshr_b32 s62, s63, 1\n"
"s_lshl_b32 s62, s62, 13\n"
"s_and_b32 s63, s63, 1\n"
"s_lshl_b32 s63, s63, 10\n"
"s_add_u32 s84, s90, s62\n"
"s_add_u32 s84, s84, s63\n"
"s_add_u32 s91, s84, 4096\n"
"buffer_load_dwordx4 a[136:139], v7, s[36:39], s84 offen sc1\n"
"mA 160,56,216,160\n"
"dT 20,9,27648\n"
"mA 164,56,220,164\n"
"mA 168,56,224,168\n"
"dT 22,9,32256\n"
"mA 172,56,228,172\n"
"mA 176,60,216,176\n"
"mA 180,60,220,180\n"
"buffer_load_dwordx4 a[140:143], v7, s[36:39], s84 offen offset:2048 sc1\n"
"mA 184,60,224,184\n"
"mA 188,60,228,188\n"
"mA 192,64,216,192\n"
"mA 196,64,220,196\n"
"mA 200,64,224,200\n"
"mA 204,64,228,204\n"
"buffer_load_dwordx4 a[144:147], v7, s[36:39], s91 offen sc1\n"
"mA 208,68,216,208\n"
"mA 212,68,220,212\n"
"mA 216,68,224,216\n"
"mA 220,68,228,220\n"
"buffer_load_dwordx4 a[148:151], v7, s[36:39], s91 offen offset:2048 sc1\n"
"s_mov_b32 s64, 9216\n"
"v_mul_lo_u32 v248, v252, s64\n"
"v_bfe_u32 v232, v255, 0, 10\n"
"v_add_u32 v248, v248, v232\n"
"v_mul_lo_u32 v249, v253, s64\n"
"v_bfe_u32 v232, v255, 10, 10\n"
"v_add_u32 v249, v249, v232\n"
"v_mul_lo_u32 v250, v254, s64\n"
"v_bfe_u32 v232, v255, 20, 10\n"
"v_add_u32 v250, v250, v232\n"
"s_cmp_eq_u32 s56, 0\n"
"s_cselect_b32 s62, 0, 0x7f000000\n"
"v_or_b32 v250, s62, v250\n"
"v_cmp_lt_u32 vcc, 15, v253\n"
"v_mov_b32 v232, 1179648\n"
"s_nop 0\n"
"v_cndmask_b32 v232, 0, v232, vcc\n"
"v_add_u32 v251, v249, v232\n"
"s_lshl_b32 s62, s59, 2\n"
"s_add_u32 s62, s62, s60\n"
"s_mul_i32 s62, s62, 288\n"
"s_add_u32 s52, %[w1lo], 0\n"
"s_addc_u32 s53, %[w1hi], 0\n"
"s_add_u32 s52, s52, s62\n"
"s_addc_u32 s53, s53, 0\n"
"s_and_b32 s53, s53, 0xffff\n"
"s_mov_b32 s54, 5308416\n"
"s_mov_b32 s55, 0x20000\n"
"s_sub_u32 s54, s54, s62\n"
"s_waitcnt vmcnt(23) lgkmcnt(0)\n"
"mA 32,0,232,32\n"
"dT 36,6,36864\n"
"s_waitcnt vmcnt(19)\n"
"mA 32,4,72,32\n"
"s_add_u32 m0, s87, 0\n"
"s_mov_b32 s65, 0\n"
"buffer_load_dwordx4 v248, s[52:55], s65 offen lds\n"
"s_waitcnt vmcnt(16)\n"
"mA 32,8,88,32\n"
"dT 38,6,41472\n"
"s_waitcnt vmcnt(12)\n"
"mA 32,12,104,32\n"
"s_waitcnt vmcnt(8)\n"
"mA 32,16,120,32\n"
"dT 40,6,46080\n"
"s_waitcnt vmcnt(4)\n"
"mA 32,20,136,32\n"
"s_add_u32 m0, s87, 4096\n"
"s_mov_b32 s65, 0\n"
"buffer_load_dwordx4 v249, s[52:55], s65 offen lds\n"
"mA 36,0,236,36\n"
"dT 42,6,50688\n"
"mA 36,4,76,36\n"
"mA 36,8,92,36\n"
"ds_read_b64_tr_b16 a[44:45], v10\n"
"mA 36,12,108,36\n"
"mA 36,16,124,36\n"
"dT 46,10,4608\n"
"s_waitcnt vmcnt(4)\n"
"mA 36,20,140,36\n"
"mA 40,0,240,40\n"
"dT 48,10,9216\n"
"mA 40,4,80,40\n"
"mA 40,8,96,40\n"
"dT 50,10,13824\n"
"mA 40,12,112,40\n"
"mA 40,16,128,40\n"
"dT 52,10,18432\n"
"s_waitcnt vmcnt(3)\n"
"mA 40,20,144,40\n"
"mA 44,0,244,44\n"
"dT 54,10,23040\n"
"mA 44,4,84,44\n"
"mA 44,8,100,44\n"
"dT 56,10,27648\n"
"mA 44,12,116,44\n"
"mA 44,16,132,44\n"
"dT 58,10,32256\n"
"s_waitcnt vmcnt(2)\n"
"mA 44,20,148,44\n"
"s_waitcnt lgkmcnt(10)\n"
"mA 64,36,232,64\n"
"dT 0,5,37120\n"
"s_waitcnt lgkmcnt(9)\n"
"mA 64,40,72,64\n"
"s_mul_i32 s65, s88, 8192\n"
"s_add_u32 m0, s65, s89\n"
"s_mov_b32 s65, 0\n"
"buffer_load_dwordx4 v250, s[52:55], s65 offen lds\n"
"cP 32,32,33\n"
"s_waitcnt lgkmcnt(7)\n"
"mA 64,44,88,64\n"
"dT 2,5,41728\n"
"cP 33,34,35\n"
"s_waitcnt lgkmcnt(6)\n"
"mA 64,48,104,64\n"
"cP 36,36,37\n"
"s_waitcnt lgkmcnt(4)\n"
"mA 64,52,120,64\n"
"dT 4,5,46336\n"
"cP 37,38,39\n"
"s_waitcnt lgkmcnt(3)\n"
"mA 64,56,136,64\n"
"s_add_u32 m0, s87, 9216\n"
"s_mov_b32 s65, 294912\n"
"buffer_load_dwordx4 v248, s[52:55], s65 offen lds\n"
"cP 40,40,41\n"
"mA 68,36,236,68\n"
"dT 6,5,50944\n"
"cP 41,42,43\n"
"mA 68,40,76,68\n"
"cP 44,44,45\n"
"mA 68,44,92,68\n"
"dT 8,9,256\n"
"cP 45,46,47\n"
"mA 68,48,108,68\n"
"mA 68,52,124,68\n"
"dT 10,9,4864\n"
"mA 68,56,140,68\n"
"mA 72,36,240,72\n"
"dT 12,9,9472\n"
"mA 72,40,80,72\n"
"mA 72,44,96,72\n"
"dT 14,9,14080\n"
"mA 72,48,112,72\n"
"mA 72,52,128,72\n"
"dT 16,9,18688\n"
"mA 72,56,144,72\n"
"mA 76,36,244,76\n"
"dT 18,9,23296\n"
"mA 76,40,84,76\n"
"mA 76,44,100,76\n"
"dT 20,9,27904\n"
"mA 76,48,116,76\n"
"mA 76,52,132,76\n"
"dT 22,9,32512\n"
"mA 76,56,148,76\n"
"s_waitcnt lgkmcnt(10)\n"
"mA 96,0,232,96\n"
"dT 36,6,37120\n"
"s_waitcnt lgkmcnt(9)\n"
"mA 96,4,72,96\n"
"s_add_u32 m0, s87, 13312\n"
"s_mov_b32 s65, 294912\n"
"buffer_load_dwordx4 v249, s[52:55], s65 offen lds\n"
"cP 34,64,65\n"
"s_waitcnt lgkmcnt(7)\n"
"mA 96,8,88,96\n"
"dT 38,6,41728\n"
"cP 35,66,67\n"
"s_waitcnt lgkmcnt(6)\n"
"mA 96,12,104,96\n"
"buffer_store_dwordx4 v[32:35], v7, s[40:43], s73 offen sc1\n"
"s_waitcnt lgkmcnt(4)\n"
"mA 96,16,120,96\n"
"dT 40,6,46336\n"
"cP 38,68,69\n"
"s_waitcnt lgkmcnt(3)\n"
"mA 96,20,136,96\n"
"s_mul_i32 s65, s88, 17408\n"
"s_add_u32 m0, s65, s89\n"
"s_mov_b32 s65, 294912\n"
"buffer_load_dwordx4 v250, s[52:55], s65 offen lds\n"
"cP 39,70,71\n"
"mA 100,0,236,100\n"
"dT 42,6,50944\n"
"buffer_store_dwordx4 v[36:39], v7, s[40:43], s73 offen offset:2048 sc1\n"
"mA 100,4,76,100\n"
"cP 42,72,73\n"
"mA 100,8,92,100\n"
"dT 44,10,256\n"
"cP 43,74,75\n"
"mA 100,12,108,100\n"
"buffer_store_dwordx4 v[40:43], v7, s[40:43], s74 offen sc1\n"
"mA 100,16,124,100\n"
"dT 46,10,4864\n"
"cP 46,76,77\n"
"mA 100,20,140,100\n"
"cP 47,78,79\n"
"mA 104,0,240,104\n"
"dT 48,10,9472\n"
"buffer_store_dwordx4 v[44:47], v7, s[40:43], s74 offen offset:2048 sc1\n"
"mA 104,4,80,104\n"
"mA 104,8,96,104\n"
"dT 50,10,14080\n"
"mA 104,12,112,104\n"
"mA 104,16,128,104\n"
"dT 52,10,18688\n"
"mA 104,20,144,104\n"
"mA 108,0,244,108\n"
"dT 54,10,23296\n"
"mA 108,4,84,108\n"
"mA 108,8,100,108\n"
"dT 56,10,27904\n"
"mA 108,12,116,108\n"
"mA 108,16,132,108\n"
"dT 58,10,32512\n"
"mA 108,20,148,108\n"
"s_waitcnt lgkmcnt(9)\n"
"mA 128,36,232,128\n"
"dT 0,5,37376\n"
"s_waitcnt lgkmcnt(9)\n"
"mA 128,40,72,128\n"
"s_add_u32 m0, s87, 18432\n"
"s_mov_b32 s65, 589824\n"
"buffer_load_dwordx4 v248, s[52:55], s65 offen lds\n"
"cP 96,96,97\n"
"s_waitcnt lgkmcnt(7)\n"
"mA 128,44,88,128\n"
"dT 2,5,41984\n"
"cP 97,98,99\n"
"s_waitcnt lgkmcnt(6)\n"
"mA 128,48,104,128\n"
"cP 100,100,101\n"
"s_waitcnt lgkmcnt(4)\n"
"mA 128,52,120,128\n"
"dT 4,5,46592\n"
"cP 101,102,103\n"
"s_waitcnt lgkmcnt(3)\n"
"mA 128,56,136,128\n"
"cP 104,104,105\n"
"mA 132,36,236,132\n"
"dT 6,5,51200\n"
"cP 105,106,107\n"
"mA 132,40,76,132\n"
"cP 108,108,109\n"
"mA 132,44,92,132\n"
"dT 8,9,512\n"
"cP 109,110,111\n"
"mA 132,48,108,132\n"
"mA 132,52,124,132\n"
"dT 10,9,5120\n"
"mA 132,56,140,132\n"
"mA 136,36,240,136\n"
"dT 12,9,9728\n"
"mA 136,40,80,136\n"
"mA 136,44,96,136\n"
"dT 14,9,14336\n"
"mA 136,48,112,136\n"
"mA 136,52,128,136\n"
"dT 16,9,18944\n"
"mA 136,56,144,136\n"
"mA 140,36,244,140\n"
"dT 18,9,23552\n"
"mA 140,40,84,140\n"
"mA 140,44,100,140\n"
"dT 20,9,28160\n"
"mA 140,48,116,140\n"
"mA 140,52,132,140\n"
"dT 22,9,32768\n"
"mA 140,56,148,140\n"
"s_waitcnt lgkmcnt(10)\n"
"mA 144,0,232,144\n"
"dT 36,6,37376\n"
"s_waitcnt lgkmcnt(9)\n"
"mA 144,4,72,144\n"
"s_add_u32 m0, s87, 22528\n"
"s_mov_b32 s65, 589824\n"
"buffer_load_dwordx4 v249, s[52:55], s65 offen lds\n"
"cP 98,128,129\n"
"s_waitcnt lgkmcnt(7)\n"
"mA 144,8,88,144\n"
"dT 38,6,41984\n"
"cP 99,130,131\n"
"s_waitcnt lgkmcnt(6)\n"
"mA 144,12,104,144\n"
"buffer_store_dwordx4 v[96:99], v7, s[40:43], s75 offen sc1\n"
"s_waitcnt lgkmcnt(4)\n"
"mA 144,16,120,144\n"
"dT 40,6,46592\n"
"cP 102,132,133\n"
"s_waitcnt lgkmcnt(3)\n"
"mA 144,20,136,144\n"
"cP 103,134,135\n"
"mA 148,0,236,148\n"
"dT 42,6,51200\n"
"buffer_store_dwordx4 v[100:103], v7, s[40:43], s75 offen offset:2048 sc1\n"
"mA 148,4,76,148\n"
"cP 106,136,137\n"
"mA 148,8,92,148\n"
"dT 44,10,512\n"
"cP 107,138,139\n"
"mA 148,12,108,148\n"
"buffer_store_dwordx4 v[104:107], v7, s[40:43], s76 offen sc1\n"
"mA 148,16,124,148\n"
"dT 46,10,5120\n"
"cP 110,140,141\n"
"mA 148,20,140,148\n"
"cP 111,142,143\n"
"mA 152,0,240,152\n"
"dT 48,10,9728\n"
"buffer_store_dwordx4 v[108:111], v7, s[40:43], s76 offen offset:2048 sc1\n"
"mA 152,4,80,152\n"
"mA 152,8,96,152\n"
"dT 50,10,14336\n"
"mA 152,12,112,152\n"
"mA 152,16,128,152\n"
"dT 52,10,18944\n"
"mA 152,20,144,152\n"
"mA 156,0,244,156\n"
"dT 54,10,23552\n"
"mA 156,4,84,156\n"
"mA 156,8,100,156\n"
"dT 56,10,28160\n"
"mA 156,12,116,156\n"
"mA 156,16,132,156\n"
"dT 58,10,32768\n"
"mA 156,20,148,156\n"
"s_waitcnt lgkmcnt(10)\n"
"mA 160,36,232,160\n"
"dT 0,5,37632\n"
"s_waitcnt lgkmcnt(9)\n"
"mA 160,40,72,160\n"
"s_mul_i32 s65, s88, 26624\n"
"s_add_u32 m0, s65, s89\n"
"s_mov_b32 s65, 589824\n"
"buffer_load_dwordx4 v250, s[52:55], s65 offen lds\n"
"cP 144,144,145\n"
"s_waitcnt lgkmcnt(7)\n"
"mA 160,44,88,160\n"
"dT 2,5,42240\n"
"cP 145,146,147\n"
"s_waitcnt lgkmcnt(6)\n"
"mA 160,48,104,160\n"
"cP 148,148,149\n"
"s_waitcnt lgkmcnt(4)\n"
"mA 160,52,120,160\n"
"dT 4,5,46848\n"
"cP 149,150,151\n"
"s_waitcnt lgkmcnt(3)\n"
"mA 160,56,136,160\n"
"cP 152,152,153\n"
"mA 164,36,236,164\n"
"dT 6,5,51456\n"
"cP 153,154,155\n"
"mA 164,40,76,164\n"
"cP 156,156,157\n"
"mA 164,44,92,164\n"
"dT 8,9,768\n"
"cP 157,158,159\n"
"mA 164,48,108,164\n"
"mA 164,52,124,164\n"
"dT 10,9,5376\n"
"mA 164,56,140,164\n"
"mA 168,36,240,168\n"
"dT 12,9,9984\n"
"mA 168,40,80,168\n"
"mA 168,44,96,168\n"
"dT 14,9,14592\n"
"mA 168,48,112,168\n"
"mA 168,52,128,168\n"
"dT 16,9,19200\n"
"mA 168,56,144,168\n"
"mA 172,36,244,172\n"
"dT 18,9,23808\n"
"mA 172,40,84,172\n"
"mA 172,44,100,172\n"
"dT 20,9,28416\n"
"mA 172,48,116,172\n"
"mA 172,52,132,172\n"
"dT 22,9,33024\n"
"mA 172,56,148,172\n"
"s_waitcnt lgkmcnt(10)\n"
"mA 176,0,232,176\n"
"dT 36,6,37632\n"
"s_waitcnt lgkmcnt(9)\n"
"mA 176,4,72,176\n"
"s_add_u32 m0, s87, 27648\n"
"s_mov_b32 s65, 884736\n"
"buffer_load_dwordx4 v248, s[52:55], s65 offen lds\n"
"cP 146,160,161\n"
"s_waitcnt lgkmcnt(7)\n"
"mA 176,8,88,176\n"
"dT 38,6,42240\n"
"cP 147,162,163\n"
"s_waitcnt lgkmcnt(6)\n"
"mA 176,12,104,176\n"
"buffer_store_dwordx4 v[144:147], v7, s[40:43], s77 offen sc1\n"
"s_waitcnt lgkmcnt(4)\n"
"mA 176,16,120,176\n"
"dT 40,6,46848\n"
"cP 150,164,165\n"
"s_waitcnt lgkmcnt(3)\n"
"mA 176,20,136,176\n"
"cP 151,166,167\n"
"mA 180,0,236,180\n"
"dT 42,6,51456\n"
"buffer_store_dwordx4 v[148:151], v7, s[40:43], s77 offen offset:2048 sc1\n"
"mA 180,4,76,180\n"
"cP 154,168,169\n"
"mA 180,8,92,180\n"
"dT 44,10,768\n"
"cP 155,170,171\n"
"mA 180,12,108,180\n"
"buffer_store_dwordx4 v[152:155], v7, s[40:43], s78 offen sc1\n"
"mA 180,16,124,180\n"
"dT 46,10,5376\n"
"cP 158,172,173\n"
"mA 180,20,140,180\n"
"cP 159,174,175\n"
"mA 184,0,240,184\n"
"dT 48,10,9984\n"
"buffer_store_dwordx4 v[156:159], v7, s[40:43], s78 offen offset:2048 sc1\n"
"mA 184,4,80,184\n"
"mA 184,8,96,184\n"
"dT 50,10,14592\n"
"mA 184,12,112,184\n"
"mA 184,16,128,184\n"
"dT 52,10,19200\n"
"mA 184,20,144,184\n"
"mA 188,0,244,188\n"
"dT 54,10,23808\n"
"mA 188,4,84,188\n"
"mA 188,8,100,188\n"
"dT 56,10,28416\n"
"mA 188,12,116,188\n"
"mA 188,16,132,188\n"
"dT 58,10,33024\n"
"mA 188,20,148,188\n"
"s_waitcnt lgkmcnt(10)\n"
"mA 192,36,232,192\n"
"dT 0,8,36864\n"
"s_waitcnt lgkmcnt(9)\n"
"mA 192,40,72,192\n"
"s_add_u32 m0, s87, 31744\n"
"s_mov_b32 s65, 884736\n"
"buffer_load_dwordx4 v249, s[52:55], s65 offen lds\n"
"cP 176,176,177\n"
"s_waitcnt lgkmcnt(7)\n"
"mA 192,44,88,192\n"
"dT 2,8,41472\n"
"cP 177,178,179\n"
"s_waitcnt lgkmcnt(6)\n"
"mA 192,48,104,192\n"
"cP 180,180,181\n"
"s_waitcnt lgkmcnt(4)\n"
"mA 192,52,120,192\n"
"dT 4,8,46080\n"
"cP 181,182,183\n"
"s_waitcnt lgkmcnt(3)\n"
"mA 192,56,136,192\n"
"cP 184,184,185\n"
"mA 196,36,236,196\n"
"dT 6,8,50688\n"
"cP 185,186,187\n"
"mA 196,40,76,196\n"
"cP 188,188,189\n"
"mA 196,44,92,196\n"
"ds_read_b64_tr_b16 a[8:9], v11\n"
"cP 189,190,191\n"
"mA 196,48,108,196\n"
"mA 196,52,124,196\n"
"dT 10,11,4608\n"
"mA 196,56,140,196\n"
"mA 200,36,240,200\n"
"dT 12,11,9216\n"
"mA 200,40,80,200\n"
"mA 200,44,96,200\n"
"dT 14,11,13824\n"
"mA 200,48,112,200\n"
"mA 200,52,128,200\n"
"dT 16,11,18432\n"
"mA 200,56,144,200\n"
"mA 204,36,244,204\n"
"dT 18,11,23040\n"
"mA 204,40,84,204\n"
"mA 204,44,100,204\n"
"dT 20,11,27648\n"
"mA 204,48,116,204\n"
"mA 204,52,132,204\n"
"dT 22,11,32256\n"
"mA 204,56,148,204\n"
"s_waitcnt lgkmcnt(10)\n"
"mA 208,0,232,208\n"
"s_waitcnt lgkmcnt(8)\n"
"mA 208,4,72,208\n"
"s_mul_i32 s65, s88, 35840\n"
"s_add_u32 m0, s65, s89\n"
"s_mov_b32 s65, 884736\n"
"buffer_load_dwordx4 v250, s[52:55], s65 offen lds\n"
"cP 178,192,193\n"
"s_waitcnt lgkmcnt(6)\n"
"mA 208,8,88,208\n"
"cP 179,194,195\n"
"s_waitcnt lgkmcnt(4)\n"
"mA 208,12,104,208\n"
"buffer_store_dwordx4 v[176:179], v7, s[40:43], s79 offen sc1\n"
"s_waitcnt lgkmcnt(2)\n"
"mA 208,16,120,208\n"
"cP 182,196,197\n"
"s_waitcnt lgkmcnt(0)\n"
"mA 208,20,136,208\n"
"cP 183,198,199\n"
"mA 212,0,236,212\n"
"buffer_store_dwordx4 v[180:183], v7, s[40:43], s79 offen offset:2048 sc1\n"
"mA 212,4,76,212\n"
"cP 186,200,201\n"
"mA 212,8,92,212\n"
"cP 187,202,203\n"
"mA 212,12,108,212\n"
"buffer_store_dwordx4 v[184:187], v7, s[40:43], s80 offen sc1\n"
"mA 212,16,124,212\n"
"cP 190,204,205\n"
"mA 212,20,140,212\n"
"cP 191,206,207\n"
"mA 216,0,240,216\n"
"buffer_store_dwordx4 v[188:191], v7, s[40:43], s80 offen offset:2048 sc1\n"
"mA 216,4,80,216\n"
"mA 216,8,96,216\n"
"mA 216,12,112,216\n"
"mA 216,16,128,216\n"
"mA 216,20,144,216\n"
"mA 220,0,244,220\n"
"mA 220,4,84,220\n"
"mA 220,8,100,220\n"
"mA 220,12,116,220\n"
"mA 220,16,132,220\n"
"mA 220,20,148,220\n"
"cP 208,208,209\n"
"cP 209,210,211\n"
"buffer_store_dwordx2 v[208:209], v12, s[40:43], s81 offen sc1\n"
"cP 212,212,213\n"
"cP 213,214,215\n"
"buffer_store_dwordx2 v[212:213], v12, s[40:43], s81 offen offset:2048 sc1\n"
"cP 216,216,217\n"
"cP 217,218,219\n"
"buffer_store_dwordx2 v[216:217], v12, s[40:43], s83 offen sc1\n"
"cP 220,220,221\n"
"cP 221,222,223\n"
"buffer_store_dwordx2 v[220:221], v12, s[40:43], s83 offen offset:2048 sc1\n"
"s_add_u32 s62, s57, 158224\n"
"v_mov_b32 v13, s62\n"
"v_mov_b32 v14, 0\n"
"ds_write_b32 v13, v14\n"
"s_lshl_b32 s62, s58, 8\n"
"s_add_u32 s62, s62, 69632\n"
"s_add_u32 s66, %[wslo], s62\n"
"s_addc_u32 s67, %[wshi], 0\n"
"s_waitcnt vmcnt(0) lgkmcnt(0)\n"
"s_barrier\n"
"s_cmp_eq_u32 s56, 0\n"
"s_cbranch_scc0 L_sg10_%=\n"
"s_lshl_b32 s62, s59, 2\n"
"s_add_u32 s62, s62, s60\n"
"s_lshl_b32 s62, s62, 3\n"
"v_mov_b32 v234, s62\n"
"v_mov_b32 v232, s98\n"
"v_mov_b32 v233, s99\n"
"s_mov_b64 exec, 1\n"
"global_store_dwordx2 v234, v[232:233], s[66:67] sc1\n"
"s_mov_b64 exec, -1\n"
"L_sg10_%=:\n"
"v_and_b32 v36, 7, v1\n"
"v_lshrrev_b32 v37, 4, v1\n"
"v_bfe_u32 v38, v1, 3, 1\n"
"v_lshl_add_u32 v16, v37, 4, v36\n"
"v_lshlrev_b32 v16, 4, v16\n"
"v_lshl_add_u32 v16, v38, 10, v16\n"
"s_lshr_b32 s62, s61, 3\n"
"s_mul_i32 s93, s62, 73728\n"
"s_bfe_u32 s62, s61, 0x20001\n"
"s_lshl_b32 s62, s62, 11\n"
"s_add_u32 s93, s93, s62\n"
"s_and_b32 s62, s61, 1\n"
"s_lshl_b32 s62, s62, 7\n"
"s_add_u32 s93, s93, s62\n"
"s_lshl_b32 s62, s56, 13\n"
"s_add_u32 s93, s93, s62\n"
"s_mul_i32 s63, s58, 294912\n"
"s_add_u32 s44, %[wslo], 26214400\n"
"s_addc_u32 s45, %[wshi], 0\n"
"s_add_u32 s44, s44, s63\n"
"s_addc_u32 s45, s45, 0\n"
"s_and_b32 s45, s45, 0xffff\n"
"s_mov_b32 s46, 16809984\n"
"s_mov_b32 s47, 0x20000\n"
"s_add_u32 s48, %[wslo], 2621440\n"
"s_addc_u32 s49, %[wshi], 0\n"
"s_add_u32 s48, s48, s63\n"
"s_addc_u32 s49, s49, 0\n"
"s_and_b32 s49, s49, 0xffff\n"
"s_mov_b32 s50, 294912\n"
"s_mov_b32 s51, 0x20000\n"
"s_add_u32 s62, s57, 158224\n"
"v_mov_b32 v13, s62\n"
"s_cmp_eq_u32 s56, 3\n"
"s_cbranch_scc1 L_r311_%=\n"
"s_lshl_b32 s62, s56, 1\n"
"v_add_u32 v39, s62, v38\n"
"v_lshlrev_b32 v17, 7, v39\n"
"v_lshl_add_u32 v17, v37, 5, v17\n"
"s_add_u32 s40, %[wslo], 0\n"
"s_addc_u32 s41, %[wshi], 0\n"
"s_and_b32 s41, s41, 0xffff\n"
"s_mov_b32 s42, 13824\n"
"s_mov_b32 s43, 0x20000\n"
"s_mov_b32 s62, 6912\n"
"buffer_load_dwordx4 v[164:167], v17, s[40:43], s62 offen sc1\n"
"buffer_load_dwordx4 v[168:171], v17, s[40:43], s62 offen offset:16 sc1\n"
"buffer_load_dwordx4 v[172:175], v17, s[40:43], s62 offen offset:768 sc1\n"
"buffer_load_dwordx4 v[176:179], v17, s[40:43], s62 offen offset:784 sc1\n"
"buffer_load_dwordx4 v[180:183], v17, s[40:43], s62 offen offset:1536 sc1\n"
"buffer_load_dwordx4 v[184:187], v17, s[40:43], s62 offen offset:1552 sc1\n"
"s_mov_b32 s62, 9216\n"
"buffer_load_dwordx4 v[188:191], v17, s[40:43], s62 offen sc1\n"
"buffer_load_dwordx4 v[192:195], v17, s[40:43], s62 offen offset:16 sc1\n"
"buffer_load_dwordx4 v[196:199], v17, s[40:43], s62 offen offset:768 sc1\n"
"buffer_load_dwordx4 v[200:203], v17, s[40:43], s62 offen offset:784 sc1\n"
"buffer_load_dwordx4 v[204:207], v17, s[40:43], s62 offen offset:1536 sc1\n"
"buffer_load_dwordx4 v[208:211], v17, s[40:43], s62 offen offset:1552 sc1\n"
"s_mov_b32 s62, 4608\n"
"buffer_load_dwordx4 v[212:215], v17, s[40:43], s62 offen sc1\n"
"buffer_load_dwordx4 v[216:219], v17, s[40:43], s62 offen offset:16 sc1\n"
"buffer_load_dwordx4 v[220:223], v17, s[40:43], s62 offen offset:768 sc1\n"
"buffer_load_dwordx4 v[224:227], v17, s[40:43], s62 offen offset:784 sc1\n"
"buffer_load_dwordx4 v[228:231], v17, s[40:43], s62 offen offset:1536 sc1\n"
"buffer_load_dwordx4 v[232:235], v17, s[40:43], s62 offen offset:1552 sc1\n"
"s_lshl_b32 s62, s58, 5\n"
"s_add_u32 s62, s62, s61\n"
"s_mul_i32 s62, s62, 9216\n"
"s_add_u32 s36, %[xlo], 0\n"
"s_addc_u32 s37, %[xhi], 0\n"
"s_add_u32 s36, s36, s62\n"
"s_addc_u32 s37, s37, 0\n"
"s_and_b32 s37, s37, 0xffff\n"
"s_mov_b32 s38, 9216\n"
"s_mov_b32 s39, 0x20000\n"
"s_lshl_b32 s62, s56, 6\n"
"s_add_u32 s62, s62, s57\n"
"s_add_u32 s62, s62, 157696\n"
"v_lshlrev_b32 v19, 3, v36\n"
"v_add_u32 v18, s62, v19\n"
"s_add_u32 s62, s57, 157696\n"
"v_add_u32 v19, s62, v19\n"
"v_bfe_u32 v32, v1, 2, 2\n"
"v_cmp_eq_u32 vcc, v32, v37\n"
"v_and_b32 v33, 1, v1\n"
"v_lshlrev_b32 v33, 4, v33\n"
"v_mov_b32 v34, 0x3f80\n"
"v_lshlrev_b32 v34, v33, v34\n"
"v_cndmask_b32 v34, 0, v34, vcc\n"
"v_and_b32 v32, 2, v1\n"
"v_cmp_eq_u32 vcc, 0, v32\n"
"s_nop 1\n"
"v_cndmask_b32 v22, 0, v34, vcc\n"
"v_sub_u32 v23, v34, v22\n"
"v_mov_b32 v20, 0\n"
"v_mov_b32 v21, 0\n"
"v_mov_b32 v24, 0\n"
"v_mov_b32 v25, 0\n"
"v_mul_u32_u24 v36, 1152, v36\n"
"v_lshl_add_u32 v36, v37, 3, v36\n"
"v_add_u32 v32, 0, v39\n"
"v_cmp_lt_u32 vcc, 8, v32\n"
"v_mov_b32 v34, 9\n"
"s_nop 0\n"
"v_cndmask_b32 v33, 0, v34, vcc\n"
"v_sub_u32 v32, v32, v33\n"
"v_lshlrev_b32 v26, 6, v33\n"
"v_lshl_add_u32 v26, v32, 6, v26\n"
"v_cmp_lt_u32 vcc, 3, v32\n"
"v_mov_b32 v34, 32\n"
"s_nop 0\n"
"v_cndmask_b32 v34, 0, v34, vcc\n"
"v_add_u32 v26, v26, v34\n"
"v_cmp_eq_u32 vcc, 8, v32\n"
"v_mov_b32 v34, 288\n"
"s_nop 0\n"
"v_cndmask_b32 v34, 0, v34, vcc\n"
"v_sub_u32 v26, v26, v34\n"
"v_add_u32 v26, v26, v36\n"
"v_mov_b32 v34, 288\n"
"v_cndmask_b32 v34, 32, v34, vcc\n"
"v_add_u32 v27, v26, v34\n"
"buffer_load_dwordx2 v[152:153], v26, s[36:39], 0 offen\n"
"buffer_load_dwordx2 v[154:155], v27, s[36:39], 0 offen\n"
"v_add_u32 v32, 6, v39\n"
"v_cmp_lt_u32 vcc, 8, v32\n"
"v_mov_b32 v34, 9\n"
"s_nop 0\n"
"v_cndmask_b32 v33, 0, v34, vcc\n"
"v_sub_u32 v32, v32, v33\n"
"v_lshlrev_b32 v28, 6, v33\n"
"v_lshl_add_u32 v28, v32, 6, v28\n"
"v_cmp_lt_u32 vcc, 3, v32\n"
"v_mov_b32 v34, 32\n"
"s_nop 0\n"
"v_cndmask_b32 v34, 0, v34, vcc\n"
"v_add_u32 v28, v28, v34\n"
"v_cmp_eq_u32 vcc, 8, v32\n"
"v_mov_b32 v34, 288\n"
"s_nop 0\n"
"v_cndmask_b32 v34, 0, v34, vcc\n"
"v_sub_u32 v28, v28, v34\n"
"v_add_u32 v28, v28, v36\n"
"v_mov_b32 v34, 288\n"
"v_cndmask_b32 v34, 32, v34, vcc\n"
"v_add_u32 v29, v28, v34\n"
"buffer_load_dwordx2 v[156:157], v28, s[36:39], 0 offen\n"
"buffer_load_dwordx2 v[158:159], v29, s[36:39], 0 offen\n"
"v_add_u32 v32, 12, v39\n"
"v_cmp_lt_u32 vcc, 8, v32\n"
"v_mov_b32 v34, 9\n"
"s_nop 0\n"
"v_cndmask_b32 v33, 0, v34, vcc\n"
"v_sub_u32 v32, v32, v33\n"
"v_lshlrev_b32 v30, 6, v33\n"
"v_lshl_add_u32 v30, v32, 6, v30\n"
"v_cmp_lt_u32 vcc, 3, v32\n"
"v_mov_b32 v34, 32\n"
"s_nop 0\n"
"v_cndmask_b32 v34, 0, v34, vcc\n"
"v_add_u32 v30, v30, v34\n"
"v_cmp_eq_u32 vcc, 8, v32\n"
"v_mov_b32 v34, 288\n"
"s_nop 0\n"
"v_cndmask_b32 v34, 0, v34, vcc\n"
"v_sub_u32 v30, v30, v34\n"
"v_add_u32 v30, v30, v36\n"
"v_mov_b32 v34, 288\n"
"v_cndmask_b32 v34, 32, v34, vcc\n"
"v_add_u32 v31, v30, v34\n"
"buffer_load_dwordx2 v[160:161], v30, s[36:39], 0 offen\n"
"buffer_load_dwordx2 v[162:163], v31, s[36:39], 0 offen\n"
"s_mov_b32 s68, 0\n"
"L_el11_%=:\n"
"ds_read_b32 v14, v13\n"
"s_waitcnt lgkmcnt(0)\n"
"v_readfirstlane_b32 s69, v14\n"
"s_nop 1\n"
"s_andn2_b32 s69, s69, s68\n"
"s_cbranch_scc0 L_el11_%=\n"
"s_or_b32 s68, s68, s69\n"
"s_bitcmp1_b32 s69, 0\n"
"s_cbranch_scc0 L_es11_0_%=\n"
"s_add_u32 s62, s93, 0\n"
"buffer_load_dwordx4 v[56:59], v16, s[44:47], s62 offen sc1\n"
"s_add_u32 s62, s93, 24576\n"
"buffer_load_dwordx4 v[88:91], v16, s[44:47], s62 offen sc1\n"
"s_add_u32 s62, s93, 49152\n"
"buffer_load_dwordx4 v[120:123], v16, s[44:47], s62 offen sc1\n"
"L_es11_0_%=:\n"
"s_bitcmp1_b32 s69, 4\n"
"s_cbranch_scc0 L_es11_1_%=\n"
"s_add_u32 s62, s93, 2359296\n"
"buffer_load_dwordx4 v[60:63], v16, s[44:47], s62 offen sc1\n"
"s_add_u32 s62, s93, 2383872\n"
"buffer_load_dwordx4 v[92:95], v16, s[44:47], s62 offen sc1\n"
"s_add_u32 s62, s93, 2408448\n"
"buffer_load_dwordx4 v[124:127], v16, s[44:47], s62 offen sc1\n"
"L_es11_1_%=:\n"
"s_bitcmp1_b32 s69, 8\n"
"s_cbranch_scc0 L_es11_2_%=\n"
"s_add_u32 s62, s93, 4718592\n"
"buffer_load_dwordx4 v[64:67], v16, s[44:47], s62 offen sc1\n"
"s_add_u32 s62, s93, 4743168\n"
"buffer_load_dwordx4 v[96:99], v16, s[44:47], s62 offen sc1\n"
"s_add_u32 s62, s93, 4767744\n"
"buffer_load_dwordx4 v[128:131], v16, s[44:47], s62 offen sc1\n"
"L_es11_2_%=:\n"
"s_bitcmp1_b32 s69, 12\n"
"s_cbranch_scc0 L_es11_3_%=\n"
"s_add_u32 s62, s93, 7077888\n"
"buffer_load_dwordx4 v[68:71], v16, s[44:47], s62 offen sc1\n"
"s_add_u32 s62, s93, 7102464\n"
"buffer_load_dwordx4 v[100:103], v16, s[44:47], s62 offen sc1\n"
"s_add_u32 s62, s93, 7127040\n"
"buffer_load_dwordx4 v[132:135], v16, s[44:47], s62 offen sc1\n"
"L_es11_3_%=:\n"
"s_bitcmp1_b32 s69, 16\n"
"s_cbranch_scc0 L_es11_4_%=\n"
"s_add_u32 s62, s93, 9437184\n"
"buffer_load_dwordx4 v[72:75], v16, s[44:47], s62 offen sc1\n"
"s_add_u32 s62, s93, 9461760\n"
"buffer_load_dwordx4 v[104:107], v16, s[44:47], s62 offen sc1\n"
"s_add_u32 s62, s93, 9486336\n"
"buffer_load_dwordx4 v[136:139], v16, s[44:47], s62 offen sc1\n"
"L_es11_4_%=:\n"
"s_bitcmp1_b32 s69, 20\n"
"s_cbranch_scc0 L_es11_5_%=\n"
"s_add_u32 s62, s93, 11796480\n"
"buffer_load_dwordx4 v[76:79], v16, s[44:47], s62 offen sc1\n"
"s_add_u32 s62, s93, 11821056\n"
"buffer_load_dwordx4 v[108:111], v16, s[44:47], s62 offen sc1\n"
"s_add_u32 s62, s93, 11845632\n"
"buffer_load_dwordx4 v[140:143], v16, s[44:47], s62 offen sc1\n"
"L_es11_5_%=:\n"
"s_bitcmp1_b32 s69, 24\n"
"s_cbranch_scc0 L_es11_6_%=\n"
"s_add_u32 s62, s93, 14155776\n"
"buffer_load_dwordx4 v[80:83], v16, s[44:47], s62 offen sc1\n"
"s_add_u32 s62, s93, 14180352\n"
"buffer_load_dwordx4 v[112:115], v16, s[44:47], s62 offen sc1\n"
"s_add_u32 s62, s93, 14204928\n"
"buffer_load_dwordx4 v[144:147], v16, s[44:47], s62 offen sc1\n"
"L_es11_6_%=:\n"
"s_bitcmp1_b32 s69, 28\n"
"s_cbranch_scc0 L_es11_7_%=\n"
"s_add_u32 s62, s93, 16515072\n"
"buffer_load_dwordx4 v[84:87], v16, s[44:47], s62 offen sc1\n"
"s_add_u32 s62, s93, 16539648\n"
"buffer_load_dwordx4 v[116:119], v16, s[44:47], s62 offen sc1\n"
"s_add_u32 s62, s93, 16564224\n"
"buffer_load_dwordx4 v[148:151], v16, s[44:47], s62 offen sc1\n"
"L_es11_7_%=:\n"
"s_cmp_eq_u32 s68, 0x11111111\n"
"s_cbranch_scc0 L_el11_%=\n"
"s_waitcnt vmcnt(0)\n"
"v_mfma_f32_16x16x32_bf16 v[32:35], v[22:25], v[56:59], 0\n"
"v_mfma_f32_16x16x32_bf16 v[36:39], v[20:23], v[56:59], 0\n"
"v_mfma_f32_16x16x32_bf16 v[32:35], v[22:25], v[60:63], v[32:35]\n"
"v_mfma_f32_16x16x32_bf16 v[36:39], v[20:23], v[60:63], v[36:39]\n"
"v_mfma_f32_16x16x32_bf16 v[32:35], v[22:25], v[64:67], v[32:35]\n"
"v_mfma_f32_16x16x32_bf16 v[36:39], v[20:23], v[64:67], v[36:39]\n"
"v_mfma_f32_16x16x32_bf16 v[32:35], v[22:25], v[68:71], v[32:35]\n"
"v_mfma_f32_16x16x32_bf16 v[36:39], v[20:23], v[68:71], v[36:39]\n"
"v_mfma_f32_16x16x32_bf16 v[32:35], v[22:25], v[72:75], v[32:35]\n"
"v_mfma_f32_16x16x32_bf16 v[36:39], v[20:23], v[72:75], v[36:39]\n"
"v_mfma_f32_16x16x32_bf16 v[32:35], v[22:25], v[76:79], v[32:35]\n"
"v_mfma_f32_16x16x32_bf16 v[36:39], v[20:23], v[76:79], v[36:39]\n"
"v_mfma_f32_16x16x32_bf16 v[32:35], v[22:25], v[80:83], v[32:35]\n"
"v_mfma_f32_16x16x32_bf16 v[36:39], v[20:23], v[80:83], v[36:39]\n"
"v_mfma_f32_16x16x32_bf16 v[32:35], v[22:25], v[84:87], v[32:35]\n"
"v_mfma_f32_16x16x32_bf16 v[36:39], v[20:23], v[84:87], v[36:39]\n"
"v_mfma_f32_16x16x32_bf16 v[40:43], v[22:25], v[88:91], 0\n"
"v_mfma_f32_16x16x32_bf16 v[44:47], v[20:23], v[88:91], 0\n"
"v_mfma_f32_16x16x32_bf16 v[40:43], v[22:25], v[92:95], v[40:43]\n"
"v_mfma_f32_16x16x32_bf16 v[44:47], v[20:23], v[92:95], v[44:47]\n"
"v_mfma_f32_16x16x32_bf16 v[40:43], v[22:25], v[96:99], v[40:43]\n"
"v_mfma_f32_16x16x32_bf16 v[44:47], v[20:23], v[96:99], v[44:47]\n"
"v_mfma_f32_16x16x32_bf16 v[40:43], v[22:25], v[100:103], v[40:43]\n"
"v_mfma_f32_16x16x32_bf16 v[44:47], v[20:23], v[100:103], v[44:47]\n"
"v_mfma_f32_16x16x32_bf16 v[40:43], v[22:25], v[104:107], v[40:43]\n"
"v_mfma_f32_16x16x32_bf16 v[44:47], v[20:23], v[104:107], v[44:47]\n"
"v_mfma_f32_16x16x32_bf16 v[40:43], v[22:25], v[108:111], v[40:43]\n"
"v_mfma_f32_16x16x32_bf16 v[44:47], v[20:23], v[108:111], v[44:47]\n"
"v_mfma_f32_16x16x32_bf16 v[40:43], v[22:25], v[112:115], v[40:43]\n"
"v_mfma_f32_16x16x32_bf16 v[44:47], v[20:23], v[112:115], v[44:47]\n"
"v_mfma_f32_16x16x32_bf16 v[40:43], v[22:25], v[116:119], v[40:43]\n"
"v_mfma_f32_16x16x32_bf16 v[44:47], v[20:23], v[116:119], v[44:47]\n"
"v_mfma_f32_16x16x32_bf16 v[48:51], v[22:25], v[120:123], 0\n"
"v_mfma_f32_16x16x32_bf16 v[52:55], v[20:23], v[120:123], 0\n"
"v_mfma_f32_16x16x32_bf16 v[48:51], v[22:25], v[124:127], v[48:51]\n"
"v_mfma_f32_16x16x32_bf16 v[52:55], v[20:23], v[124:127], v[52:55]\n"
"v_mfma_f32_16x16x32_bf16 v[48:51], v[22:25], v[128:131], v[48:51]\n"
"v_mfma_f32_16x16x32_bf16 v[52:55], v[20:23], v[128:131], v[52:55]\n"
"v_mfma_f32_16x16x32_bf16 v[48:51], v[22:25], v[132:135], v[48:51]\n"
"v_mfma_f32_16x16x32_bf16 v[52:55], v[20:23], v[132:135], v[52:55]\n"
"v_mfma_f32_16x16x32_bf16 v[48:51], v[22:25], v[136:139], v[48:51]\n"
"v_mfma_f32_16x16x32_bf16 v[52:55], v[20:23], v[136:139], v[52:55]\n"
"v_mfma_f32_16x16x32_bf16 v[48:51], v[22:25], v[140:143], v[48:51]\n"
"v_mfma_f32_16x16x32_bf16 v[52:55], v[20:23], v[140:143], v[52:55]\n"
"v_mfma_f32_16x16x32_bf16 v[48:51], v[22:25], v[144:147], v[48:51]\n"
"v_mfma_f32_16x16x32_bf16 v[52:55], v[20:23], v[144:147], v[52:55]\n"
"v_mfma_f32_16x16x32_bf16 v[48:51], v[22:25], v[148:151], v[48:51]\n"
"v_mfma_f32_16x16x32_bf16 v[52:55], v[20:23], v[148:151], v[52:55]\n"
"mU 32,212,32\n"
"mU 33,213,33\n"
"mU 34,214,34\n"
"mU 35,215,35\n"
"mU 36,216,36\n"
"mU 37,217,37\n"
"mU 38,218,38\n"
"mU 39,219,39\n"
"cP 56,32,33\n"
"v_lshlrev_b32 v32, 16, v56\n"
"v_and_b32 v33, 0xffff0000, v56\n"
"cP 57,34,35\n"
"v_lshlrev_b32 v34, 16, v57\n"
"v_and_b32 v35, 0xffff0000, v57\n"
"cP 58,36,37\n"
"v_lshlrev_b32 v36, 16, v58\n"
"v_and_b32 v37, 0xffff0000, v58\n"
"cP 59,38,39\n"
"v_lshlrev_b32 v38, 16, v59\n"
"v_and_b32 v39, 0xffff0000, v59\n"
"v_lshlrev_b32 v60, 16, v152\n"
"v_and_b32 v61, 0xffff0000, v152\n"
"v_lshlrev_b32 v62, 16, v153\n"
"v_and_b32 v63, 0xffff0000, v153\n"
"v_lshlrev_b32 v64, 16, v154\n"
"v_and_b32 v65, 0xffff0000, v154\n"
"v_lshlrev_b32 v66, 16, v155\n"
"v_and_b32 v67, 0xffff0000, v155\n"
"aD 32,32,60\n"
"aD 33,33,61\n"
"aD 34,34,62\n"
"aD 35,35,63\n"
"aD 36,36,64\n"
"aD 37,37,65\n"
"aD 38,38,66\n"
"aD 39,39,67\n"
"cP 236,32,33\n"
"cP 237,34,35\n"
"cP 238,36,37\n"
"cP 239,38,39\n"
"v_lshlrev_b32 v32, 16, v236\n"
"v_and_b32 v33, 0xffff0000, v236\n"
"v_lshlrev_b32 v34, 16, v237\n"
"v_and_b32 v35, 0xffff0000, v237\n"
"v_lshlrev_b32 v36, 16, v238\n"
"v_and_b32 v37, 0xffff0000, v238\n"
"v_lshlrev_b32 v38, 16, v239\n"
"v_and_b32 v39, 0xffff0000, v239\n"
"mU 40,220,40\n"
"mU 41,221,41\n"
"mU 42,222,42\n"
"mU 43,223,43\n"
"mU 44,224,44\n"
"mU 45,225,45\n"
"mU 46,226,46\n"
"mU 47,227,47\n"
"cP 88,40,41\n"
"v_lshlrev_b32 v40, 16, v88\n"
"v_and_b32 v41, 0xffff0000, v88\n"
"cP 89,42,43\n"
"v_lshlrev_b32 v42, 16, v89\n"
"v_and_b32 v43, 0xffff0000, v89\n"
"cP 90,44,45\n"
"v_lshlrev_b32 v44, 16, v90\n"
"v_and_b32 v45, 0xffff0000, v90\n"
"cP 91,46,47\n"
"v_lshlrev_b32 v46, 16, v91\n"
"v_and_b32 v47, 0xffff0000, v91\n"
"v_lshlrev_b32 v92, 16, v156\n"
"v_and_b32 v93, 0xffff0000, v156\n"
"v_lshlrev_b32 v94, 16, v157\n"
"v_and_b32 v95, 0xffff0000, v157\n"
"v_lshlrev_b32 v96, 16, v158\n"
"v_and_b32 v97, 0xffff0000, v158\n"
"v_lshlrev_b32 v98, 16, v159\n"
"v_and_b32 v99, 0xffff0000, v159\n"
"aD 40,40,92\n"
"aD 41,41,93\n"
"aD 42,42,94\n"
"aD 43,43,95\n"
"aD 44,44,96\n"
"aD 45,45,97\n"
"aD 46,46,98\n"
"aD 47,47,99\n"
"cP 240,40,41\n"
"cP 241,42,43\n"
"cP 242,44,45\n"
"cP 243,46,47\n"
"v_lshlrev_b32 v40, 16, v240\n"
"v_and_b32 v41, 0xffff0000, v240\n"
"v_lshlrev_b32 v42, 16, v241\n"
"v_and_b32 v43, 0xffff0000, v241\n"
"v_lshlrev_b32 v44, 16, v242\n"
"v_and_b32 v45, 0xffff0000, v242\n"
"v_lshlrev_b32 v46, 16, v243\n"
"v_and_b32 v47, 0xffff0000, v243\n"
"mU 48,228,48\n"
"mU 49,229,49\n"
"mU 50,230,50\n"
"mU 51,231,51\n"
"mU 52,232,52\n"
"mU 53,233,53\n"
"mU 54,234,54\n"
"mU 55,235,55\n"
"cP 120,48,49\n"
"v_lshlrev_b32 v48, 16, v120\n"
"v_and_b32 v49, 0xffff0000, v120\n"
"cP 121,50,51\n"
"v_lshlrev_b32 v50, 16, v121\n"
"v_and_b32 v51, 0xffff0000, v121\n"
"cP 122,52,53\n"
"v_lshlrev_b32 v52, 16, v122\n"
"v_and_b32 v53, 0xffff0000, v122\n"
"cP 123,54,55\n"
"v_lshlrev_b32 v54, 16, v123\n"
"v_and_b32 v55, 0xffff0000, v123\n"
"v_lshlrev_b32 v124, 16, v160\n"
"v_and_b32 v125, 0xffff0000, v160\n"
"v_lshlrev_b32 v126, 16, v161\n"
"v_and_b32 v127, 0xffff0000, v161\n"
"v_lshlrev_b32 v128, 16, v162\n"
"v_and_b32 v129, 0xffff0000, v162\n"
"v_lshlrev_b32 v130, 16, v163\n"
"v_and_b32 v131, 0xffff0000, v163\n"
"aD 48,48,124\n"
"aD 49,49,125\n"
"aD 50,50,126\n"
"aD 51,51,127\n"
"aD 52,52,128\n"
"aD 53,53,129\n"
"aD 54,54,130\n"
"aD 55,55,131\n"
"cP 244,48,49\n"
"cP 245,50,51\n"
"cP 246,52,53\n"
"cP 247,54,55\n"
"v_lshlrev_b32 v48, 16, v244\n"
"v_and_b32 v49, 0xffff0000, v244\n"
"v_lshlrev_b32 v50, 16, v245\n"
"v_and_b32 v51, 0xffff0000, v245\n"
"v_lshlrev_b32 v52, 16, v246\n"
"v_and_b32 v53, 0xffff0000, v246\n"
"v_lshlrev_b32 v54, 16, v247\n"
"v_and_b32 v55, 0xffff0000, v247\n"
"aD 56,32,33\n"
"aD 58,34,35\n"
"aD 56,56,36\n"
"aD 58,58,37\n"
"aD 56,56,38\n"
"aD 58,58,39\n"
"aD 56,56,40\n"
"aD 58,58,41\n"
"aD 56,56,42\n"
"aD 58,58,43\n"
"aD 56,56,44\n"
"aD 58,58,45\n"
"aD 56,56,46\n"
"aD 58,58,47\n"
"aD 56,56,48\n"
"aD 58,58,49\n"
"aD 56,56,50\n"
"aD 58,58,51\n"
"aD 56,56,52\n"
"aD 58,58,53\n"
"aD 56,56,54\n"
"aD 58,58,55\n"
"aD 56,56,58\n"
"s_nop 1\n"
"v_add_f32_dpp v56, v56, v56 row_ror:8 row_mask:0xf bank_mask:0xf\n"
"v_mov_b32 v58, v56\n"
"s_nop 1\n"
"v_permlane16_swap_b32 v56, v58\n"
"aD 56,56,58\n"
"v_mov_b32 v58, v56\n"
"s_nop 1\n"
"v_permlane32_swap_b32 v56, v58\n"
"aD 56,56,58\n"
"v_mul_f32 v56, 0x3baaaaab, v56\n"
"v_sub_f32 v68, v32, v56\n"
"v_sub_f32 v69, v33, v56\n"
"v_sub_f32 v70, v34, v56\n"
"v_sub_f32 v71, v35, v56\n"
"v_sub_f32 v72, v36, v56\n"
"v_sub_f32 v73, v37, v56\n"
"v_sub_f32 v74, v38, v56\n"
"v_sub_f32 v75, v39, v56\n"
"v_sub_f32 v76, v40, v56\n"
"v_sub_f32 v77, v41, v56\n"
"v_sub_f32 v78, v42, v56\n"
"v_sub_f32 v79, v43, v56\n"
"v_sub_f32 v80, v44, v56\n"
"v_sub_f32 v81, v45, v56\n"
"v_sub_f32 v82, v46, v56\n"
"v_sub_f32 v83, v47, v56\n"
"v_sub_f32 v84, v48, v56\n"
"v_sub_f32 v85, v49, v56\n"
"v_sub_f32 v86, v50, v56\n"
"v_sub_f32 v87, v51, v56\n"
"v_sub_f32 v88, v52, v56\n"
"v_sub_f32 v89, v53, v56\n"
"v_sub_f32 v90, v54, v56\n"
"v_sub_f32 v91, v55, v56\n"
"mU 57,68,68\n"
"mU 58,69,69\n"
"v_fmac_f32 v57, v70, v70\n"
"v_fmac_f32 v58, v71, v71\n"
"v_fmac_f32 v57, v72, v72\n"
"v_fmac_f32 v58, v73, v73\n"
"v_fmac_f32 v57, v74, v74\n"
"v_fmac_f32 v58, v75, v75\n"
"v_fmac_f32 v57, v76, v76\n"
"v_fmac_f32 v58, v77, v77\n"
"v_fmac_f32 v57, v78, v78\n"
"v_fmac_f32 v58, v79, v79\n"
"v_fmac_f32 v57, v80, v80\n"
"v_fmac_f32 v58, v81, v81\n"
"v_fmac_f32 v57, v82, v82\n"
"v_fmac_f32 v58, v83, v83\n"
"v_fmac_f32 v57, v84, v84\n"
"v_fmac_f32 v58, v85, v85\n"
"v_fmac_f32 v57, v86, v86\n"
"v_fmac_f32 v58, v87, v87\n"
"v_fmac_f32 v57, v88, v88\n"
"v_fmac_f32 v58, v89, v89\n"
"v_fmac_f32 v57, v90, v90\n"
"v_fmac_f32 v58, v91, v91\n"
"aD 57,57,58\n"
"s_nop 1\n"
"v_add_f32_dpp v57, v57, v57 row_ror:8 row_mask:0xf bank_mask:0xf\n"
"v_mov_b32 v58, v57\n"
"s_nop 1\n"
"v_permlane16_swap_b32 v57, v58\n"
"aD 57,57,58\n"
"v_mov_b32 v58, v57\n"
"s_nop 1\n"
"v_permlane32_swap_b32 v57, v58\n"
"aD 57,57,58\n"
"ds_write_b64 v18, v[56:57]\n"
"s_waitcnt lgkmcnt(0)\n"
"s_barrier\n"
"ds_read_b64 v[60:61], v19\n"
"ds_read_b64 v[62:63], v19 offset:64\n"
"ds_read_b64 v[64:65], v19 offset:128\n"
"s_waitcnt lgkmcnt(1)\n"
"aD 56,60,62\n"
"s_waitcnt lgkmcnt(0)\n"
"aD 56,56,64\n"
"v_mul_f32 v56, 0x3eaaaaab, v56\n"
"aD 57,61,63\n"
"aD 57,57,65\n"
"v_sub_f32 v58, v60, v56\n"
"mU 59,58,58\n"
"v_sub_f32 v58, v62, v56\n"
"v_fmac_f32 v59, v58, v58\n"
"v_sub_f32 v58, v64, v56\n"
"v_fmac_f32 v59, v58, v58\n"
"v_mul_f32 v59, 0x3eaaaaab, v59\n"
"s_mov_b32 s62, 0x3ae38e39\n"
"v_fma_f32 v59, v57, s62, v59\n"
"cP 59,59,59\n"
"v_and_b32 v59, 0xffff0000, v59\n"
"v_add_f32 v59, 0x3727c5ac, v59\n"
"cP 59,59,59\n"
"v_and_b32 v59, 0xffff0000, v59\n"
"v_sqrt_f32 v59, v59\n"
"s_nop 0\n"
"cP 59,59,59\n"
"v_and_b32 v59, 0xffff0000, v59\n"
"v_rcp_f32 v57, v59\n"
"cP 56,56,56\n"
"v_and_b32 v56, 0xffff0000, v56\n"
"v_sub_f32 v32, v32, v56\n"
"v_sub_f32 v33, v33, v56\n"
"v_sub_f32 v34, v34, v56\n"
"v_sub_f32 v35, v35, v56\n"
"v_sub_f32 v36, v36, v56\n"
"v_sub_f32 v37, v37, v56\n"
"v_sub_f32 v38, v38, v56\n"
"v_sub_f32 v39, v39, v56\n"
"cP 68,32,33\n"
"v_lshlrev_b32 v32, 16, v68\n"
"v_and_b32 v33, 0xffff0000, v68\n"
"cP 69,34,35\n"
"v_lshlrev_b32 v34, 16, v69\n"
"v_and_b32 v35, 0xffff0000, v69\n"
"cP 70,36,37\n"
"v_lshlrev_b32 v36, 16, v70\n"
"v_and_b32 v37, 0xffff0000, v70\n"
"cP 71,38,39\n"
"v_lshlrev_b32 v38, 16, v71\n"
"v_and_b32 v39, 0xffff0000, v71\n"
"mU 32,32,57\n"
"mU 33,33,57\n"
"mU 34,34,57\n"
"mU 35,35,57\n"
"mU 36,36,57\n"
"mU 37,37,57\n"
"mU 38,38,57\n"
"mU 39,39,57\n"
"cP 68,32,33\n"
"v_lshlrev_b32 v32, 16, v68\n"
"v_and_b32 v33, 0xffff0000, v68\n"
"cP 69,34,35\n"
"v_lshlrev_b32 v34, 16, v69\n"
"v_and_b32 v35, 0xffff0000, v69\n"
"cP 70,36,37\n"
"v_lshlrev_b32 v36, 16, v70\n"
"v_and_b32 v37, 0xffff0000, v70\n"
"cP 71,38,39\n"
"v_lshlrev_b32 v38, 16, v71\n"
"v_and_b32 v39, 0xffff0000, v71\n"
"mU 32,32,188\n"
"mU 33,33,189\n"
"mU 34,34,190\n"
"mU 35,35,191\n"
"mU 36,36,192\n"
"mU 37,37,193\n"
"mU 38,38,194\n"
"mU 39,39,195\n"
"cP 72,32,33\n"
"v_lshlrev_b32 v32, 16, v72\n"
"v_and_b32 v33, 0xffff0000, v72\n"
"cP 73,34,35\n"
"v_lshlrev_b32 v34, 16, v73\n"
"v_and_b32 v35, 0xffff0000, v73\n"
"cP 74,36,37\n"
"v_lshlrev_b32 v36, 16, v74\n"
"v_and_b32 v37, 0xffff0000, v74\n"
"cP 75,38,39\n"
"v_lshlrev_b32 v38, 16, v75\n"
"v_and_b32 v39, 0xffff0000, v75\n"
"aD 32,32,164\n"
"aD 33,33,165\n"
"aD 34,34,166\n"
"aD 35,35,167\n"
"aD 36,36,168\n"
"aD 37,37,169\n"
"aD 38,38,170\n"
"aD 39,39,171\n"
"cP 72,32,33\n"
"cP 73,34,35\n"
"cP 74,36,37\n"
"cP 75,38,39\n"
"s_add_u32 s62, s93, 0\n"
"buffer_store_dwordx4 v[72:75], v16, s[48:51], s62 offen sc1\n"
"v_sub_f32 v40, v40, v56\n"
"v_sub_f32 v41, v41, v56\n"
"v_sub_f32 v42, v42, v56\n"
"v_sub_f32 v43, v43, v56\n"
"v_sub_f32 v44, v44, v56\n"
"v_sub_f32 v45, v45, v56\n"
"v_sub_f32 v46, v46, v56\n"
"v_sub_f32 v47, v47, v56\n"
"cP 76,40,41\n"
"v_lshlrev_b32 v40, 16, v76\n"
"v_and_b32 v41, 0xffff0000, v76\n"
"cP 77,42,43\n"
"v_lshlrev_b32 v42, 16, v77\n"
"v_and_b32 v43, 0xffff0000, v77\n"
"cP 78,44,45\n"
"v_lshlrev_b32 v44, 16, v78\n"
"v_and_b32 v45, 0xffff0000, v78\n"
"cP 79,46,47\n"
"v_lshlrev_b32 v46, 16, v79\n"
"v_and_b32 v47, 0xffff0000, v79\n"
"mU 40,40,57\n"
"mU 41,41,57\n"
"mU 42,42,57\n"
"mU 43,43,57\n"
"mU 44,44,57\n"
"mU 45,45,57\n"
"mU 46,46,57\n"
"mU 47,47,57\n"
"cP 76,40,41\n"
"v_lshlrev_b32 v40, 16, v76\n"
"v_and_b32 v41, 0xffff0000, v76\n"
"cP 77,42,43\n"
"v_lshlrev_b32 v42, 16, v77\n"
"v_and_b32 v43, 0xffff0000, v77\n"
"cP 78,44,45\n"
"v_lshlrev_b32 v44, 16, v78\n"
"v_and_b32 v45, 0xffff0000, v78\n"
"cP 79,46,47\n"
"v_lshlrev_b32 v46, 16, v79\n"
"v_and_b32 v47, 0xffff0000, v79\n"
"mU 40,40,196\n"
"mU 41,41,197\n"
"mU 42,42,198\n"
"mU 43,43,199\n"
"mU 44,44,200\n"
"mU 45,45,201\n"
"mU 46,46,202\n"
"mU 47,47,203\n"
"cP 80,40,41\n"
"v_lshlrev_b32 v40, 16, v80\n"
"v_and_b32 v41, 0xffff0000, v80\n"
"cP 81,42,43\n"
"v_lshlrev_b32 v42, 16, v81\n"
"v_and_b32 v43, 0xffff0000, v81\n"
"cP 82,44,45\n"
"v_lshlrev_b32 v44, 16, v82\n"
"v_and_b32 v45, 0xffff0000, v82\n"
"cP 83,46,47\n"
"v_lshlrev_b32 v46, 16, v83\n"
"v_and_b32 v47, 0xffff0000, v83\n"
"aD 40,40,172\n"
"aD 41,41,173\n"
"aD 42,42,174\n"
"aD 43,43,175\n"
"aD 44,44,176\n"
"aD 45,45,177\n"
"aD 46,46,178\n"
"aD 47,47,179\n"
"cP 80,40,41\n"
"cP 81,42,43\n"
"cP 82,44,45\n"
"cP 83,46,47\n"
"s_add_u32 s62, s93, 24576\n"
"buffer_store_dwordx4 v[80:83], v16, s[48:51], s62 offen sc1\n"
"v_sub_f32 v48, v48, v56\n"
"v_sub_f32 v49, v49, v56\n"
"v_sub_f32 v50, v50, v56\n"
"v_sub_f32 v51, v51, v56\n"
"v_sub_f32 v52, v52, v56\n"
"v_sub_f32 v53, v53, v56\n"
"v_sub_f32 v54, v54, v56\n"
"v_sub_f32 v55, v55, v56\n"
"cP 84,48,49\n"
"v_lshlrev_b32 v48, 16, v84\n"
"v_and_b32 v49, 0xffff0000, v84\n"
"cP 85,50,51\n"
"v_lshlrev_b32 v50, 16, v85\n"
"v_and_b32 v51, 0xffff0000, v85\n"
"cP 86,52,53\n"
"v_lshlrev_b32 v52, 16, v86\n"
"v_and_b32 v53, 0xffff0000, v86\n"
"cP 87,54,55\n"
"v_lshlrev_b32 v54, 16, v87\n"
"v_and_b32 v55, 0xffff0000, v87\n"
"mU 48,48,57\n"
"mU 49,49,57\n"
"mU 50,50,57\n"
"mU 51,51,57\n"
"mU 52,52,57\n"
"mU 53,53,57\n"
"mU 54,54,57\n"
"mU 55,55,57\n"
"cP 84,48,49\n"
"v_lshlrev_b32 v48, 16, v84\n"
"v_and_b32 v49, 0xffff0000, v84\n"
"cP 85,50,51\n"
"v_lshlrev_b32 v50, 16, v85\n"
"v_and_b32 v51, 0xffff0000, v85\n"
"cP 86,52,53\n"
"v_lshlrev_b32 v52, 16, v86\n"
"v_and_b32 v53, 0xffff0000, v86\n"
"cP 87,54,55\n"
"v_lshlrev_b32 v54, 16, v87\n"
"v_and_b32 v55, 0xffff0000, v87\n"
"mU 48,48,204\n"
"mU 49,49,205\n"
"mU 50,50,206\n"
"mU 51,51,207\n"
"mU 52,52,208\n"
"mU 53,53,209\n"
"mU 54,54,210\n"
"mU 55,55,211\n"
"cP 88,48,49\n"
"v_lshlrev_b32 v48, 16, v88\n"
"v_and_b32 v49, 0xffff0000, v88\n"
"cP 89,50,51\n"
"v_lshlrev_b32 v50, 16, v89\n"
"v_and_b32 v51, 0xffff0000, v89\n"
"cP 90,52,53\n"
"v_lshlrev_b32 v52, 16, v90\n"
"v_and_b32 v53, 0xffff0000, v90\n"
"cP 91,54,55\n"
"v_lshlrev_b32 v54, 16, v91\n"
"v_and_b32 v55, 0xffff0000, v91\n"
"aD 48,48,180\n"
"aD 49,49,181\n"
"aD 50,50,182\n"
"aD 51,51,183\n"
"aD 52,52,184\n"
"aD 53,53,185\n"
"aD 54,54,186\n"
"aD 55,55,187\n"
"cP 88,48,49\n"
"cP 89,50,51\n"
"cP 90,52,53\n"
"cP 91,54,55\n"
"s_add_u32 s62, s93, 49152\n"
"buffer_store_dwordx4 v[88:91], v16, s[48:51], s62 offen sc1\n"
"s_waitcnt vmcnt(0) lgkmcnt(0)\n"
"s_branch L_rj11_%=\n"
"L_r311_%=:\n"
"s_lshl_b32 s62, s58, 8\n"
"s_add_u32 s62, s62, 69632\n"
"s_add_u32 s66, %[wslo], s62\n"
"s_addc_u32 s67, %[wshi], 0\n"
"v_and_b32 v16, 31, v1\n"
"v_lshlrev_b32 v16, 3, v16\n"
"s_mov_b32 s63, 131072\n"
"L_p812_%=:\n"
"global_load_dwordx2 v[14:15], v16, s[66:67] sc1\n"
"s_waitcnt vmcnt(0)\n"
"v_cmp_eq_u64 vcc, s[98:99], v[14:15]\n"
"s_nop 1\n"
"s_lshr_b32 s62, vcc_lo, 1\n"
"s_and_b32 s62, s62, vcc_lo\n"
"s_lshr_b32 s64, s62, 2\n"
"s_and_b32 s62, s62, s64\n"
"s_and_b32 s62, s62, 0x11111111\n"
"s_sub_u32 s63, s63, 1\n"
"s_cmp_eq_u32 s63, 0\n"
"s_cselect_b32 s62, 0x11111111, s62\n"
"v_mov_b32 v14, s62\n"
"ds_write_b32 v13, v14\n"
"s_sleep 2\n"
"s_cmp_eq_u32 s62, 0x11111111\n"
"s_cbranch_scc0 L_p812_%=\n"
"s_barrier\n"
"L_rj11_%=:\n"
"s_nop 11\n"
"s_lshl_b32 s62, s58, 8\n"
"s_add_u32 s62, s62, 71680\n"
"s_add_u32 s66, %[wslo], s62\n"
"s_addc_u32 s67, %[wshi], 0\n"
"s_barrier\n"
"s_cmp_eq_u32 s56, 0\n"
"s_cbranch_scc0 L_sg13_%=\n"
"s_lshl_b32 s62, s59, 2\n"
"s_add_u32 s62, s62, s60\n"
"s_lshl_b32 s62, s62, 3\n"
"v_mov_b32 v234, s62\n"
"v_mov_b32 v232, s98\n"
"v_mov_b32 v233, s99\n"
"s_mov_b64 exec, 1\n"
"global_store_dwordx2 v234, v[232:233], s[66:67] sc1\n"
"s_mov_b64 exec, -1\n"
"L_sg13_%=:\n"
"s_add_u32 m0, s87, 36864\n"
"s_mov_b32 s65, 1327104\n"
"buffer_load_dwordx4 v248, s[52:55], s65 offen lds\n"
"s_add_u32 m0, s87, 40960\n"
"s_mov_b32 s65, 1327104\n"
"buffer_load_dwordx4 v249, s[52:55], s65 offen lds\n"
"s_mul_i32 s65, s88, 45056\n"
"s_add_u32 m0, s65, s89\n"
"s_mov_b32 s65, 1327104\n"
"buffer_load_dwordx4 v250, s[52:55], s65 offen lds\n"
"s_add_u32 m0, s87, 46080\n"
"s_mov_b32 s65, 1622016\n"
"buffer_load_dwordx4 v248, s[52:55], s65 offen lds\n"
"s_add_u32 m0, s87, 50176\n"
"s_mov_b32 s65, 1622016\n"
"buffer_load_dwordx4 v249, s[52:55], s65 offen lds\n"
"s_mul_i32 s65, s88, 54272\n"
"s_add_u32 m0, s65, s89\n"
"s_mov_b32 s65, 1622016\n"
"buffer_load_dwordx4 v250, s[52:55], s65 offen lds\n"
"s_add_u32 m0, s87, 55296\n"
"s_mov_b32 s65, 1916928\n"
"buffer_load_dwordx4 v248, s[52:55], s65 offen lds\n"
"s_add_u32 m0, s87, 59392\n"
"s_mov_b32 s65, 1916928\n"
"buffer_load_dwordx4 v249, s[52:55], s65 offen lds\n"
"s_mul_i32 s65, s88, 63488\n"
"s_add_u32 m0, s65, s89\n"
"s_mov_b32 s65, 1916928\n"
"buffer_load_dwordx4 v250, s[52:55], s65 offen lds\n"
"s_add_u32 m0, s87, 64512\n"
"s_mov_b32 s65, 2211840\n"
"buffer_load_dwordx4 v248, s[52:55], s65 offen lds\n"
"s_add_u32 m0, s87, 68608\n"
"s_mov_b32 s65, 2211840\n"
"buffer_load_dwordx4 v249, s[52:55], s65 offen lds\n"
"s_mul_i32 s65, s88, 72704\n"
"s_add_u32 m0, s65, s89\n"
"s_mov_b32 s65, 2211840\n"
"buffer_load_dwordx4 v250, s[52:55], s65 offen lds\n"
"s_add_u32 m0, s87, 73728\n"
"s_mov_b32 s65, 1179648\n"
"buffer_load_dwordx4 v248, s[52:55], s65 offen lds\n"
"s_add_u32 m0, s87, 77824\n"
"s_mov_b32 s65, 1179648\n"
"buffer_load_dwordx4 v251, s[52:55], s65 offen lds\n"
"s_mul_i32 s65, s88, 81920\n"
"s_add_u32 m0, s65, s89\n"
"s_mov_b32 s65, 2359296\n"
"buffer_load_dwordx4 v250, s[52:55], s65 offen lds\n"
"s_add_u32 m0, s87, 82944\n"
"s_mov_b32 s65, 2654208\n"
"buffer_load_dwordx4 v248, s[52:55], s65 offen lds\n"
"s_add_u32 m0, s87, 87040\n"
"s_mov_b32 s65, 2654208\n"
"buffer_load_dwordx4 v249, s[52:55], s65 offen lds\n"
"s_mul_i32 s65, s88, 91136\n"
"s_add_u32 m0, s65, s89\n"
"s_mov_b32 s65, 2654208\n"
"buffer_load_dwordx4 v250, s[52:55], s65 offen lds\n"
"s_add_u32 m0, s87, 147456\n"
"s_mov_b32 s65, 2949120\n"
"buffer_load_dwordx4 v248, s[52:55], s65 offen lds\n"
"s_add_u32 m0, s87, 151552\n"
"s_mov_b32 s65, 2949120\n"
"buffer_load_dwordx4 v249, s[52:55], s65 offen lds\n"
"s_mul_i32 s65, s88, 155648\n"
"s_add_u32 m0, s65, s89\n"
"s_mov_b32 s65, 2949120\n"
"buffer_load_dwordx4 v250, s[52:55], s65 offen lds\n"
"s_add_u32 m0, s87, 92160\n"
"s_mov_b32 s65, 3244032\n"
"buffer_load_dwordx4 v248, s[52:55], s65 offen lds\n"
"s_add_u32 m0, s87, 96256\n"
"s_mov_b32 s65, 3244032\n"
"buffer_load_dwordx4 v249, s[52:55], s65 offen lds\n"
"s_mul_i32 s65, s88, 100352\n"
"s_add_u32 m0, s65, s89\n"
"s_mov_b32 s65, 3244032\n"
"buffer_load_dwordx4 v250, s[52:55], s65 offen lds\n"
"s_add_u32 m0, s87, 101376\n"
"s_mov_b32 s65, 3538944\n"
"buffer_load_dwordx4 v248, s[52:55], s65 offen lds\n"
"s_add_u32 m0, s87, 105472\n"
"s_mov_b32 s65, 3538944\n"
"buffer_load_dwordx4 v249, s[52:55], s65 offen lds\n"
"s_mul_i32 s65, s88, 109568\n"
"s_add_u32 m0, s65, s89\n"
"s_mov_b32 s65, 3538944\n"
"buffer_load_dwordx4 v250, s[52:55], s65 offen lds\n"
"s_add_u32 m0, s87, 110592\n"
"s_mov_b32 s65, 3981312\n"
"buffer_load_dwordx4 v248, s[52:55], s65 offen lds\n"
"s_add_u32 m0, s87, 114688\n"
"s_mov_b32 s65, 3981312\n"
"buffer_load_dwordx4 v249, s[52:55], s65 offen lds\n"
"s_mul_i32 s65, s88, 118784\n"
"s_add_u32 m0, s65, s89\n"
"s_mov_b32 s65, 3981312\n"
"buffer_load_dwordx4 v250, s[52:55], s65 offen lds\n"
"s_add_u32 m0, s87, 119808\n"
"s_mov_b32 s65, 4276224\n"
"buffer_load_dwordx4 v248, s[52:55], s65 offen lds\n"
"s_add_u32 m0, s87, 123904\n"
"s_mov_b32 s65, 4276224\n"
"buffer_load_dwordx4 v249, s[52:55], s65 offen lds\n"
"s_mul_i32 s65, s88, 128000\n"
"s_add_u32 m0, s65, s89\n"
"s_mov_b32 s65, 4276224\n"
"buffer_load_dwordx4 v250, s[52:55], s65 offen lds\n"
"s_add_u32 m0, s87, 129024\n"
"s_mov_b32 s65, 4571136\n"
"buffer_load_dwordx4 v248, s[52:55], s65 offen lds\n"
"s_add_u32 m0, s87, 133120\n"
"s_mov_b32 s65, 4571136\n"
"buffer_load_dwordx4 v249, s[52:55], s65 offen lds\n"
"s_mul_i32 s65, s88, 137216\n"
"s_add_u32 m0, s65, s89\n"
"s_mov_b32 s65, 4571136\n"
"buffer_load_dwordx4 v250, s[52:55], s65 offen lds\n"
"s_add_u32 m0, s87, 138240\n"
"s_mov_b32 s65, 4866048\n"
"buffer_load_dwordx4 v248, s[52:55], s65 offen lds\n"
"s_add_u32 m0, s87, 142336\n"
"s_mov_b32 s65, 4866048\n"
"buffer_load_dwordx4 v249, s[52:55], s65 offen lds\n"
"s_mul_i32 s65, s88, 146432\n"
"s_add_u32 m0, s65, s89\n"
"s_mov_b32 s65, 4866048\n"
"buffer_load_dwordx4 v250, s[52:55], s65 offen lds\n"
"s_mul_i32 s62, s58, 294912\n"
"s_add_u32 s36, %[wslo], 2621440\n"
"s_addc_u32 s37, %[wshi], 0\n"
"s_add_u32 s36, s36, s62\n"
"s_addc_u32 s37, s37, 0\n"
"s_and_b32 s37, s37, 0xffff\n"
"s_mov_b32 s38, 294912\n"
"s_mov_b32 s39, 0x20000\n"
"s_mul_i32 s90, s56, 73728\n"
"s_mul_i32 s62, s58, 2359296\n"
"s_add_u32 s40, %[wslo], 7340032\n"
"s_addc_u32 s41, %[wshi], 0\n"
"s_add_u32 s40, s40, s62\n"
"s_addc_u32 s41, s41, 0\n"
"s_and_b32 s41, s41, 0xffff\n"
"s_mov_b32 s42, 2359296\n"
"s_mov_b32 s43, 0x20000\n"
"s_lshr_b32 s62, s60, 1\n"
"s_lshl_b32 s63, s59, 1\n"
"s_add_u32 s62, s62, s63\n"
"s_mul_i32 s64, s62, 9\n"
"s_and_b32 s63, s60, 1\n"
"s_lshl_b32 s63, s63, 2\n"
"s_add_u32 s63, s63, s64\n"
"s_mul_i32 s65, s56, 589824\n"
"s_add_u32 s62, s63, 0\n"
"s_lshr_b32 s73, s62, 1\n"
"s_lshl_b32 s73, s73, 13\n"
"s_and_b32 s62, s62, 1\n"
"s_lshl_b32 s62, s62, 10\n"
"s_add_u32 s73, s73, s62\n"
"s_add_u32 s73, s73, s65\n"
"s_add_u32 s74, s73, 4096\n"
"s_add_u32 s62, s63, 1\n"
"s_lshr_b32 s75, s62, 1\n"
"s_lshl_b32 s75, s75, 13\n"
"s_and_b32 s62, s62, 1\n"
"s_lshl_b32 s62, s62, 10\n"
"s_add_u32 s75, s75, s62\n"
"s_add_u32 s75, s75, s65\n"
"s_add_u32 s76, s75, 4096\n"
"s_add_u32 s62, s63, 2\n"
"s_lshr_b32 s77, s62, 1\n"
"s_lshl_b32 s77, s77, 13\n"
"s_and_b32 s62, s62, 1\n"
"s_lshl_b32 s62, s62, 10\n"
"s_add_u32 s77, s77, s62\n"
"s_add_u32 s77, s77, s65\n"
"s_add_u32 s78, s77, 4096\n"
"s_add_u32 s62, s63, 3\n"
"s_lshr_b32 s79, s62, 1\n"
"s_lshl_b32 s79, s79, 13\n"
"s_and_b32 s62, s62, 1\n"
"s_lshl_b32 s62, s62, 10\n"
"s_add_u32 s79, s79, s62\n"
"s_add_u32 s79, s79, s65\n"
"s_add_u32 s80, s79, 4096\n"
"s_add_u32 s62, s64, 8\n"
"s_lshr_b32 s81, s62, 1\n"
"s_lshl_b32 s81, s81, 13\n"
"s_and_b32 s62, s62, 1\n"
"s_lshl_b32 s62, s62, 10\n"
"s_add_u32 s81, s81, s62\n"
"s_add_u32 s81, s81, s65\n"
"s_add_u32 s83, s81, 4096\n"
"s_lshl_b32 s62, s58, 8\n"
"s_add_u32 s62, s62, 71680\n"
"s_add_u32 s66, %[wslo], s62\n"
"s_addc_u32 s67, %[wshi], 0\n"
"s_cmp_eq_u32 s56, 0\n"
"s_cbranch_scc0 L_wd14_%=\n"
"v_and_b32 v13, 31, v1\n"
"v_lshlrev_b32 v13, 3, v13\n"
"s_mov_b32 s63, 131072\n"
"L_wp14_%=:\n"
"global_load_dwordx2 v[14:15], v13, s[66:67] sc1\n"
"s_waitcnt vmcnt(0)\n"
"v_cmp_eq_u64 vcc, s[98:99], v[14:15]\n"
"s_nop 1\n"
"s_cmp_eq_u64 vcc, -1\n"
"s_cbranch_scc1 L_wd14_%=\n"
"s_sleep 2\n"
"s_sub_u32 s63, s63, 1\n"
"s_cmp_lg_u32 s63, 0\n"
"s_cbranch_scc1 L_wp14_%=\n"
"L_wd14_%=:\n"
"s_barrier\n"
"s_add_u32 s84, s90, 0\n"
"s_add_u32 s91, s84, 4096\n"
"buffer_load_dwordx4 a[72:75], v7, s[36:39], s84 offen sc1\n"
"buffer_load_dwordx4 a[76:79], v7, s[36:39], s84 offen offset:2048 sc1\n"
"buffer_load_dwordx4 a[80:83], v7, s[36:39], s91 offen sc1\n"
"buffer_load_dwordx4 a[84:87], v7, s[36:39], s91 offen offset:2048 sc1\n"
"s_add_u32 s84, s90, 1024\n"
"s_add_u32 s91, s84, 4096\n"
"buffer_load_dwordx4 a[88:91], v7, s[36:39], s84 offen sc1\n"
"buffer_load_dwordx4 a[92:95], v7, s[36:39], s84 offen offset:2048 sc1\n"
"buffer_load_dwordx4 a[96:99], v7, s[36:39], s91 offen sc1\n"
"buffer_load_dwordx4 a[100:103], v7, s[36:39], s91 offen offset:2048 sc1\n"
"s_waitcnt vmcnt(4)\n"
"s_barrier\n"
"ds_read_b64_tr_b16 a[0:1], v2\n"
"dT 2,2,4608\n"
"ds_read_b64_tr_b16 a[4:5], v3\n"
"dT 6,3,4608\n"
"dT 8,2,256\n"
"dT 10,2,4864\n"
"dT 12,3,256\n"
"dT 14,3,4864\n"
"dT 16,2,512\n"
"dT 18,2,5120\n"
"dT 20,3,512\n"
"dT 22,3,5120\n"
"dT 24,2,768\n"
"dT 26,2,5376\n"
"dT 28,3,768\n"
"dT 30,3,5376\n"
"ds_read_b64_tr_b16 a[32:33], v4\n"
"dT 34,4,4608\n"
"s_waitcnt lgkmcnt(15)\n"
"mZ 16,0,72\n"
"dT 36,2,9216\n"
"mZ 20,0,76\n"
"mZ 24,0,80\n"
"dT 38,2,13824\n"
"mZ 28,0,84\n"
"s_waitcnt lgkmcnt(15)\n"
"mZ 32,4,72\n"
"dT 40,3,9216\n"
"mZ 36,4,76\n"
"s_add_u32 s84, s90, 8192\n"
"s_add_u32 s91, s84, 4096\n"
"buffer_load_dwordx4 a[104:107], v7, s[36:39], s84 offen sc1\n"
"mZ 40,4,80\n"
"dT 42,3,13824\n"
"mZ 44,4,84\n"
"s_waitcnt lgkmcnt(15)\n"
"mZ 48,8,72\n"
"dT 44,2,9472\n"
"mZ 52,8,76\n"
"mZ 56,8,80\n"
"dT 46,2,14080\n"
"mZ 60,8,84\n"
"buffer_load_dwordx4 a[108:111], v7, s[36:39], s84 offen offset:2048 sc1\n"
"s_waitcnt lgkmcnt(15)\n"
"mZ 64,12,72\n"
"dT 48,3,9472\n"
"mZ 68,12,76\n"
"mZ 72,12,80\n"
"dT 50,3,14080\n"
"mZ 76,12,84\n"
"s_waitcnt lgkmcnt(15)\n"
"mZ 80,16,72\n"
"dT 52,2,9728\n"
"mZ 84,16,76\n"
"buffer_load_dwordx4 a[112:115], v7, s[36:39], s91 offen sc1\n"
"mZ 88,16,80\n"
"dT 54,2,14336\n"
"mZ 92,16,84\n"
"s_waitcnt lgkmcnt(10)\n"
"mZ 96,20,72\n"
"dT 56,3,9728\n"
"mZ 100,20,76\n"
"mZ 104,20,80\n"
"dT 58,3,14336\n"
"mZ 108,20,84\n"
"buffer_load_dwordx4 a[116:119], v7, s[36:39], s91 offen offset:2048 sc1\n"
"mZ 112,24,72\n"
"dT 60,2,9984\n"
"mZ 116,24,76\n"
"mZ 120,24,80\n"
"dT 62,2,14592\n"
"mZ 124,24,84\n"
"mZ 128,28,72\n"
"dT 64,3,9984\n"
"mZ 132,28,76\n"
"s_add_u32 s84, s90, 9216\n"
"s_add_u32 s91, s84, 4096\n"
"buffer_load_dwordx4 a[120:123], v7, s[36:39], s84 offen sc1\n"
"mZ 136,28,80\n"
"dT 66,3,14592\n"
"mZ 140,28,84\n"
"mZ 144,32,72\n"
"dT 68,4,9216\n"
"mZ 148,32,76\n"
"mZ 152,32,80\n"
"dT 70,4,13824\n"
"mZ 156,32,84\n"
"buffer_load_dwordx4 a[124:127], v7, s[36:39], s84 offen offset:2048 sc1\n"
"s_waitcnt lgkmcnt(0)\n"
"s_barrier\n"
"s_waitcnt vmcnt(9)\n"
"mA 16,36,88,16\n"
"dT 0,2,18432\n"
"s_waitcnt vmcnt(8)\n"
"mA 20,36,92,20\n"
"s_waitcnt vmcnt(7)\n"
"mA 24,36,96,24\n"
"dT 2,2,23040\n"
"s_waitcnt vmcnt(6)\n"
"mA 28,36,100,28\n"
"mA 32,40,88,32\n"
"dT 4,3,18432\n"
"mA 36,40,92,36\n"
"buffer_load_dwordx4 a[128:131], v7, s[36:39], s91 offen sc1\n"
"mA 40,40,96,40\n"
"dT 6,3,23040\n"
"mA 44,40,100,44\n"
"mA 48,44,88,48\n"
"dT 8,2,18688\n"
"mA 52,44,92,52\n"
"mA 56,44,96,56\n"
"dT 10,2,23296\n"
"mA 60,44,100,60\n"
"buffer_load_dwordx4 a[132:135], v7, s[36:39], s91 offen offset:2048 sc1\n"
"mA 64,48,88,64\n"
"dT 12,3,18688\n"
"mA 68,48,92,68\n"
"mA 72,48,96,72\n"
"dT 14,3,23296\n"
"mA 76,48,100,76\n"
"mA 80,52,88,80\n"
"dT 16,2,18944\n"
"mA 84,52,92,84\n"
"s_add_u32 s84, s90, 16384\n"
"s_add_u32 s91, s84, 4096\n"
"buffer_load_dwordx4 a[136:139], v7, s[36:39], s84 offen sc1\n"
"mA 88,52,96,88\n"
"dT 18,2,23552\n"
"mA 92,52,100,92\n"
"mA 96,56,88,96\n"
"dT 20,3,18944\n"
"mA 100,56,92,100\n"
"mA 104,56,96,104\n"
"dT 22,3,23552\n"
"mA 108,56,100,108\n"
"buffer_load_dwordx4 a[140:143], v7, s[36:39], s84 offen offset:2048 sc1\n"
"mA 112,60,88,112\n"
"dT 24,2,19200\n"
"mA 116,60,92,116\n"
"mA 120,60,96,120\n"
"dT 26,2,23808\n"
"mA 124,60,100,124\n"
"mA 128,64,88,128\n"
"dT 28,3,19200\n"
"mA 132,64,92,132\n"
"buffer_load_dwordx4 a[144:147], v7, s[36:39], s91 offen sc1\n"
"mA 136,64,96,136\n"
"dT 30,3,23808\n"
"mA 140,64,100,140\n"
"mA 144,68,88,144\n"
"dT 32,4,18432\n"
"mA 148,68,92,148\n"
"mA 152,68,96,152\n"
"dT 34,4,23040\n"
"mA 156,68,100,156\n"
"buffer_load_dwordx4 a[148:151], v7, s[36:39], s91 offen offset:2048 sc1\n"
"s_waitcnt vmcnt(11) lgkmcnt(11)\n"
"mA 16,0,104,16\n"
"dT 36,2,27648\n"
"s_waitcnt vmcnt(10)\n"
"mA 20,0,108,20\n"
"s_waitcnt vmcnt(9)\n"
"mA 24,0,112,24\n"
"dT 38,2,32256\n"
"s_waitcnt vmcnt(8)\n"
"mA 28,0,116,28\n"
"mA 32,4,104,32\n"
"dT 40,3,27648\n"
"mA 36,4,108,36\n"
"s_add_u32 s84, s90, 17408\n"
"s_add_u32 s91, s84, 4096\n"
"buffer_load_dwordx4 a[152:155], v7, s[36:39], s84 offen sc1\n"
"mA 40,4,112,40\n"
"dT 42,3,32256\n"
"mA 44,4,116,44\n"
"mA 48,8,104,48\n"
"dT 44,2,27904\n"
"mA 52,8,108,52\n"
"mA 56,8,112,56\n"
"dT 46,2,32512\n"
"mA 60,8,116,60\n"
"buffer_load_dwordx4 a[156:159], v7, s[36:39], s84 offen offset:2048 sc1\n"
"s_waitcnt lgkmcnt(10)\n"
"mA 64,12,104,64\n"
"dT 48,3,27904\n"
"mA 68,12,108,68\n"
"mA 72,12,112,72\n"
"dT 50,3,32512\n"
"mA 76,12,116,76\n"
"mA 80,16,104,80\n"
"dT 52,2,28160\n"
"mA 84,16,108,84\n"
"buffer_load_dwordx4 a[160:163], v7, s[36:39], s91 offen sc1\n"
"mA 88,16,112,88\n"
"dT 54,2,32768\n"
"mA 92,16,116,92\n"
"mA 96,20,104,96\n"
"dT 56,3,28160\n"
"mA 100,20,108,100\n"
"mA 104,20,112,104\n"
"dT 58,3,32768\n"
"mA 108,20,116,108\n"
"buffer_load_dwordx4 a[164:167], v7, s[36:39], s91 offen offset:2048 sc1\n"
"mA 112,24,104,112\n"
"dT 60,2,28416\n"
"mA 116,24,108,116\n"
"mA 120,24,112,120\n"
"dT 62,2,33024\n"
"mA 124,24,116,124\n"
"s_waitcnt lgkmcnt(11)\n"
"mA 128,28,104,128\n"
"dT 64,3,28416\n"
"mA 132,28,108,132\n"
"s_add_u32 m0, s87, 0\n"
"s_mov_b32 s65, 3833856\n"
"buffer_load_dwordx4 v248, s[52:55], s65 offen lds\n"
"mA 136,28,112,136\n"
"dT 66,3,33024\n"
"mA 140,28,116,140\n"
"mA 144,32,104,144\n"
"dT 68,4,27648\n"
"mA 148,32,108,148\n"
"mA 152,32,112,152\n"
"dT 70,4,32256\n"
"mA 156,32,116,156\n"
"s_add_u32 m0, s87, 4096\n"
"s_mov_b32 s65, 3833856\n"
"buffer_load_dwordx4 v251, s[52:55], s65 offen lds\n"
"s_waitcnt vmcnt(13)\n"
"mA 16,36,120,16\n"
"dT 0,2,36864\n"
"s_waitcnt vmcnt(12)\n"
"mA 20,36,124,20\n"
"s_waitcnt vmcnt(11)\n"
"mA 24,36,128,24\n"
"dT 2,2,41472\n"
"s_waitcnt vmcnt(10)\n"
"mA 28,36,132,28\n"
"s_waitcnt lgkmcnt(9)\n"
"mA 32,40,120,32\n"
"dT 4,3,36864\n"
"mA 36,40,124,36\n"
"s_mul_i32 s65, s88, 8192\n"
"s_add_u32 m0, s65, s89\n"
"s_mov_b32 s65, 5013504\n"
"buffer_load_dwordx4 v250, s[52:55], s65 offen lds\n"
"mA 40,40,128,40\n"
"dT 6,3,41472\n"
"mA 44,40,132,44\n"
"mA 48,44,120,48\n"
"dT 8,2,37120\n"
"mA 52,44,124,52\n"
"mA 56,44,128,56\n"
"dT 10,2,41728\n"
"mA 60,44,132,60\n"
"s_add_u32 s84, s90, 24576\n"
"s_add_u32 s91, s84, 4096\n"
"buffer_load_dwordx4 a[168:171], v7, s[36:39], s84 offen sc1\n"
"mA 64,48,120,64\n"
"dT 12,3,37120\n"
"mA 68,48,124,68\n"
"mA 72,48,128,72\n"
"dT 14,3,41728\n"
"mA 76,48,132,76\n"
"mA 80,52,120,80\n"
"dT 16,2,37376\n"
"mA 84,52,124,84\n"
"buffer_load_dwordx4 a[172:175], v7, s[36:39], s84 offen offset:2048 sc1\n"
"mA 88,52,128,88\n"
"dT 18,2,41984\n"
"mA 92,52,132,92\n"
"s_waitcnt lgkmcnt(9)\n"
"mA 96,56,120,96\n"
"dT 20,3,37376\n"
"mA 100,56,124,100\n"
"mA 104,56,128,104\n"
"dT 22,3,41984\n"
"mA 108,56,132,108\n"
"buffer_load_dwordx4 a[176:179], v7, s[36:39], s91 offen sc1\n"
"mA 112,60,120,112\n"
"dT 24,2,37632\n"
"mA 116,60,124,116\n"
"mA 120,60,128,120\n"
"dT 26,2,42240\n"
"mA 124,60,132,124\n"
"mA 128,64,120,128\n"
"dT 28,3,37632\n"
"mA 132,64,124,132\n"
"buffer_load_dwordx4 a[180:183], v7, s[36:39], s91 offen offset:2048 sc1\n"
"mA 136,64,128,136\n"
"dT 30,3,42240\n"
"mA 140,64,132,140\n"
"mA 144,68,120,144\n"
"dT 32,4,36864\n"
"mA 148,68,124,148\n"
"mA 152,68,128,152\n"
"dT 34,4,41472\n"
"mA 156,68,132,156\n"
"s_add_u32 s84, s90, 25600\n"
"s_add_u32 s91, s84, 4096\n"
"buffer_load_dwordx4 a[184:187], v7, s[36:39], s84 offen sc1\n"
"s_waitcnt vmcnt(15) lgkmcnt(11)\n"
"mA 16,0,136,16\n"
"dT 36,2,46080\n"
"s_waitcnt vmcnt(14)\n"
"mA 20,0,140,20\n"
"s_waitcnt vmcnt(13)\n"
"mA 24,0,144,24\n"
"dT 38,2,50688\n"
"s_waitcnt vmcnt(12)\n"
"mA 28,0,148,28\n"
"mA 32,4,136,32\n"
"dT 40,3,46080\n"
"mA 36,4,140,36\n"
"buffer_load_dwordx4 a[188:191], v7, s[36:39], s84 offen offset:2048 sc1\n"
"mA 40,4,144,40\n"
"dT 42,3,50688\n"
"mA 44,4,148,44\n"
"mA 48,8,136,48\n"
"dT 44,2,46336\n"
"mA 52,8,140,52\n"
"mA 56,8,144,56\n"
"dT 46,2,50944\n"
"mA 60,8,148,60\n"
"buffer_load_dwordx4 a[192:195], v7, s[36:39], s91 offen sc1\n"
"s_waitcnt lgkmcnt(10)\n"
"mA 64,12,136,64\n"
"dT 48,3,46336\n"
"mA 68,12,140,68\n"
"mA 72,12,144,72\n"
"dT 50,3,50944\n"
"mA 76,12,148,76\n"
"mA 80,16,136,80\n"
"dT 52,2,46592\n"
"mA 84,16,140,84\n"
"buffer_load_dwordx4 a[196:199], v7, s[36:39], s91 offen offset:2048 sc1\n"
"mA 88,16,144,88\n"
"dT 54,2,51200\n"
"mA 92,16,148,92\n"
"mA 96,20,136,96\n"
"dT 56,3,46592\n"
"mA 100,20,140,100\n"
"mA 104,20,144,104\n"
"dT 58,3,51200\n"
"mA 108,20,148,108\n"
"s_add_u32 s84, s90, 32768\n"
"s_add_u32 s91, s84, 4096\n"
"buffer_load_dwordx4 a[200:203], v7, s[36:39], s84 offen sc1\n"
"mA 112,24,136,112\n"
"dT 60,2,46848\n"
"mA 116,24,140,116\n"
"mA 120,24,144,120\n"
"dT 62,2,51456\n"
"mA 124,24,148,124\n"
"s_waitcnt lgkmcnt(11)\n"
"mA 128,28,136,128\n"
"dT 64,3,46848\n"
"mA 132,28,140,132\n"
"buffer_load_dwordx4 a[204:207], v7, s[36:39], s84 offen offset:2048 sc1\n"
"mA 136,28,144,136\n"
"dT 66,3,51456\n"
"mA 140,28,148,140\n"
"mA 144,32,136,144\n"
"dT 68,4,46080\n"
"mA 148,32,140,148\n"
"mA 152,32,144,152\n"
"dT 70,4,50688\n"
"mA 156,32,148,156\n"
"buffer_load_dwordx4 a[208:211], v7, s[36:39], s91 offen sc1\n"
"s_waitcnt vmcnt(17)\n"
"mA 16,36,152,16\n"
"ds_read_b64_tr_b16 a[0:1], v5\n"
"s_waitcnt vmcnt(16)\n"
"mA 20,36,156,20\n"
"s_waitcnt vmcnt(15)\n"
"mA 24,36,160,24\n"
"dT 2,5,4608\n"
"s_waitcnt vmcnt(14)\n"
"mA 28,36,164,28\n"
"s_waitcnt lgkmcnt(10)\n"
"mA 32,40,152,32\n"
"ds_read_b64_tr_b16 a[4:5], v6\n"
"mA 36,40,156,36\n"
"buffer_load_dwordx4 a[212:215], v7, s[36:39], s91 offen offset:2048 sc1\n"
"mA 40,40,160,40\n"
"dT 6,6,4608\n"
"mA 44,40,164,44\n"
"mA 48,44,152,48\n"
"dT 8,5,256\n"
"mA 52,44,156,52\n"
"mA 56,44,160,56\n"
"dT 10,5,4864\n"
"mA 60,44,164,60\n"
"s_add_u32 s84, s90, 33792\n"
"s_add_u32 s91, s84, 4096\n"
"buffer_load_dwordx4 a[216:219], v7, s[36:39], s84 offen sc1\n"
"mA 64,48,152,64\n"
"dT 12,6,256\n"
"mA 68,48,156,68\n"
"mA 72,48,160,72\n"
"dT 14,6,4864\n"
"mA 76,48,164,76\n"
"mA 80,52,152,80\n"
"dT 16,5,512\n"
"mA 84,52,156,84\n"
"buffer_load_dwordx4 a[220:223], v7, s[36:39], s84 offen offset:2048 sc1\n"
"mA 88,52,160,88\n"
"dT 18,5,5120\n"
"mA 92,52,164,92\n"
"s_waitcnt lgkmcnt(10)\n"
"mA 96,56,152,96\n"
"dT 20,6,512\n"
"mA 100,56,156,100\n"
"mA 104,56,160,104\n"
"dT 22,6,5120\n"
"mA 108,56,164,108\n"
"buffer_load_dwordx4 a[224:227], v7, s[36:39], s91 offen sc1\n"
"mA 112,60,152,112\n"
"dT 24,5,768\n"
"mA 116,60,156,116\n"
"mA 120,60,160,120\n"
"dT 26,5,5376\n"
"mA 124,60,164,124\n"
"mA 128,64,152,128\n"
"dT 28,6,768\n"
"mA 132,64,156,132\n"
"buffer_load_dwordx4 a[228:231], v7, s[36:39], s91 offen offset:2048 sc1\n"
"mA 136,64,160,136\n"
"dT 30,6,5376\n"
"mA 140,64,164,140\n"
"mA 144,68,152,144\n"
"ds_read_b64_tr_b16 a[32:33], v8\n"
"mA 148,68,156,148\n"
"mA 152,68,160,152\n"
"dT 34,8,4608\n"
"mA 156,68,164,156\n"
"s_add_u32 s84, s90, 40960\n"
"s_add_u32 s91, s84, 4096\n"
"buffer_load_dwordx4 a[232:235], v7, s[36:39], s84 offen sc1\n"
"s_waitcnt vmcnt(16) lgkmcnt(11)\n"
"mA 16,0,168,16\n"
"dT 36,5,9216\n"
"s_waitcnt vmcnt(15)\n"
"mA 20,0,172,20\n"
"s_waitcnt vmcnt(14)\n"
"mA 24,0,176,24\n"
"dT 38,5,13824\n"
"s_waitcnt vmcnt(13)\n"
"mA 28,0,180,28\n"
"mA 32,4,168,32\n"
"dT 40,6,9216\n"
"mA 36,4,172,36\n"
"buffer_load_dwordx4 a[236:239], v7, s[36:39], s84 offen offset:2048 sc1\n"
"mA 40,4,176,40\n"
"dT 42,6,13824\n"
"mA 44,4,180,44\n"
"mA 48,8,168,48\n"
"dT 44,5,9472\n"
"mA 52,8,172,52\n"
"mA 56,8,176,56\n"
"dT 46,5,14080\n"
"mA 60,8,180,60\n"
"buffer_load_dwordx4 a[240:243], v7, s[36:39], s91 offen sc1\n"
"s_waitcnt lgkmcnt(10)\n"
"mA 64,12,168,64\n"
"dT 48,6,9472\n"
"mA 68,12,172,68\n"
"mA 72,12,176,72\n"
"dT 50,6,14080\n"
"mA 76,12,180,76\n"
"mA 80,16,168,80\n"
"dT 52,5,9728\n"
"mA 84,16,172,84\n"
"buffer_load_dwordx4 a[244:247], v7, s[36:39], s91 offen offset:2048 sc1\n"
"mA 88,16,176,88\n"
"dT 54,5,14336\n"
"mA 92,16,180,92\n"
"mA 96,20,168,96\n"
"dT 56,6,9728\n"
"mA 100,20,172,100\n"
"mA 104,20,176,104\n"
"dT 58,6,14336\n"
"mA 108,20,180,108\n"
"s_add_u32 s84, s90, 41984\n"
"s_add_u32 s91, s84, 4096\n"
"buffer_load_dwordx4 a[72:75], v7, s[36:39], s84 offen sc1\n"
"mA 112,24,168,112\n"
"dT 60,5,9984\n"
"mA 116,24,172,116\n"
"mA 120,24,176,120\n"
"dT 62,5,14592\n"
"mA 124,24,180,124\n"
"s_waitcnt lgkmcnt(11)\n"
"mA 128,28,168,128\n"
"dT 64,6,9984\n"
"mA 132,28,172,132\n"
"buffer_load_dwordx4 a[76:79], v7, s[36:39], s84 offen offset:2048 sc1\n"
"mA 136,28,176,136\n"
"dT 66,6,14592\n"
"mA 140,28,180,140\n"
"mA 144,32,168,144\n"
"dT 68,8,9216\n"
"mA 148,32,172,148\n"
"mA 152,32,176,152\n"
"dT 70,8,13824\n"
"mA 156,32,180,156\n"
"buffer_load_dwordx4 a[80:83], v7, s[36:39], s91 offen sc1\n"
"s_waitcnt vmcnt(18)\n"
"mA 16,36,184,16\n"
"dT 0,5,18432\n"
"s_waitcnt vmcnt(17)\n"
"mA 20,36,188,20\n"
"s_waitcnt vmcnt(16)\n"
"mA 24,36,192,24\n"
"dT 2,5,23040\n"
"s_waitcnt vmcnt(15)\n"
"mA 28,36,196,28\n"
"s_waitcnt lgkmcnt(10)\n"
"mA 32,40,184,32\n"
"dT 4,6,18432\n"
"mA 36,40,188,36\n"
"buffer_load_dwordx4 a[84:87], v7, s[36:39], s91 offen offset:2048 sc1\n"
"mA 40,40,192,40\n"
"dT 6,6,23040\n"
"mA 44,40,196,44\n"
"mA 48,44,184,48\n"
"dT 8,5,18688\n"
"mA 52,44,188,52\n"
"mA 56,44,192,56\n"
"dT 10,5,23296\n"
"mA 60,44,196,60\n"
"s_add_u32 s84, s90, 49152\n"
"s_add_u32 s91, s84, 4096\n"
"buffer_load_dwordx4 a[88:91], v7, s[36:39], s84 offen sc1\n"
"mA 64,48,184,64\n"
"dT 12,6,18688\n"
"mA 68,48,188,68\n"
"mA 72,48,192,72\n"
"dT 14,6,23296\n"
"mA 76,48,196,76\n"
"mA 80,52,184,80\n"
"dT 16,5,18944\n"
"mA 84,52,188,84\n"
"buffer_load_dwordx4 a[92:95], v7, s[36:39], s84 offen offset:2048 sc1\n"
"mA 88,52,192,88\n"
"dT 18,5,23552\n"
"mA 92,52,196,92\n"
"s_waitcnt lgkmcnt(10)\n"
"mA 96,56,184,96\n"
"dT 20,6,18944\n"
"mA 100,56,188,100\n"
"mA 104,56,192,104\n"
"dT 22,6,23552\n"
"mA 108,56,196,108\n"
"buffer_load_dwordx4 a[96:99], v7, s[36:39], s91 offen sc1\n"
"mA 112,60,184,112\n"
"dT 24,5,19200\n"
"mA 116,60,188,116\n"
"mA 120,60,192,120\n"
"dT 26,5,23808\n"
"mA 124,60,196,124\n"
"mA 128,64,184,128\n"
"dT 28,6,19200\n"
"mA 132,64,188,132\n"
"buffer_load_dwordx4 a[100:103], v7, s[36:39], s91 offen offset:2048 sc1\n"
"mA 136,64,192,136\n"
"dT 30,6,23808\n"
"mA 140,64,196,140\n"
"mA 144,68,184,144\n"
"dT 32,8,18432\n"
"mA 148,68,188,148\n"
"mA 152,68,192,152\n"
"dT 34,8,23040\n"
"mA 156,68,196,156\n"
"s_add_u32 s84, s90, 50176\n"
"s_add_u32 s91, s84, 4096\n"
"buffer_load_dwordx4 a[104:107], v7, s[36:39], s84 offen sc1\n"
"s_waitcnt vmcnt(20) lgkmcnt(11)\n"
"mA 16,0,200,16\n"
"dT 36,5,27648\n"
"s_waitcnt vmcnt(19)\n"
"mA 20,0,204,20\n"
"s_waitcnt vmcnt(18)\n"
"mA 24,0,208,24\n"
"dT 38,5,32256\n"
"s_waitcnt vmcnt(17)\n"
"mA 28,0,212,28\n"
"mA 32,4,200,32\n"
"dT 40,6,27648\n"
"mA 36,4,204,36\n"
"buffer_load_dwordx4 a[108:111], v7, s[36:39], s84 offen offset:2048 sc1\n"
"mA 40,4,208,40\n"
"dT 42,6,32256\n"
"mA 44,4,212,44\n"
"mA 48,8,200,48\n"
"dT 44,5,27904\n"
"mA 52,8,204,52\n"
"mA 56,8,208,56\n"
"dT 46,5,32512\n"
"mA 60,8,212,60\n"
"buffer_load_dwordx4 a[112:115], v7, s[36:39], s91 offen sc1\n"
"s_waitcnt lgkmcnt(10)\n"
"mA 64,12,200,64\n"
"dT 48,6,27904\n"
"mA 68,12,204,68\n"
"mA 72,12,208,72\n"
"dT 50,6,32512\n"
"mA 76,12,212,76\n"
"mA 80,16,200,80\n"
"dT 52,5,28160\n"
"mA 84,16,204,84\n"
"buffer_load_dwordx4 a[116:119], v7, s[36:39], s91 offen offset:2048 sc1\n"
"mA 88,16,208,88\n"
"dT 54,5,32768\n"
"mA 92,16,212,92\n"
"mA 96,20,200,96\n"
"dT 56,6,28160\n"
"mA 100,20,204,100\n"
"mA 104,20,208,104\n"
"dT 58,6,32768\n"
"mA 108,20,212,108\n"
"s_add_u32 s84, s90, 57344\n"
"s_add_u32 s91, s84, 4096\n"
"buffer_load_dwordx4 a[120:123], v7, s[36:39], s84 offen sc1\n"
"mA 112,24,200,112\n"
"dT 60,5,28416\n"
"mA 116,24,204,116\n"
"mA 120,24,208,120\n"
"dT 62,5,33024\n"
"mA 124,24,212,124\n"
"s_waitcnt lgkmcnt(11)\n"
"mA 128,28,200,128\n"
"dT 64,6,28416\n"
"mA 132,28,204,132\n"
"buffer_load_dwordx4 a[124:127], v7, s[36:39], s84 offen offset:2048 sc1\n"
"mA 136,28,208,136\n"
"dT 66,6,33024\n"
"mA 140,28,212,140\n"
"mA 144,32,200,144\n"
"dT 68,8,27648\n"
"mA 148,32,204,148\n"
"mA 152,32,208,152\n"
"dT 70,8,32256\n"
"mA 156,32,212,156\n"
"buffer_load_dwordx4 a[128:131], v7, s[36:39], s91 offen sc1\n"
"s_waitcnt lgkmcnt(0)\n"
"s_barrier\n"
"s_mov_b32 s64, 1152\n"
"v_mul_lo_u32 v248, v252, s64\n"
"v_bfe_u32 v232, v255, 0, 10\n"
"v_add_u32 v248, v248, v232\n"
"v_mul_lo_u32 v249, v253, s64\n"
"v_bfe_u32 v232, v255, 10, 10\n"
"v_add_u32 v249, v249, v232\n"
"v_mul_lo_u32 v250, v254, s64\n"
"v_bfe_u32 v232, v255, 20, 10\n"
"v_add_u32 v250, v250, v232\n"
"s_cmp_eq_u32 s56, 0\n"
"s_cselect_b32 s62, 0, 0x7f000000\n"
"v_or_b32 v250, s62, v250\n"
"v_cmp_lt_u32 vcc, 15, v253\n"
"v_mov_b32 v232, 147456\n"
"s_nop 0\n"
"v_cndmask_b32 v232, 0, v232, vcc\n"
"v_add_u32 v251, v249, v232\n"
"s_mul_i32 s62, s59, 663552\n"
"s_mul_i32 s63, s60, 288\n"
"s_add_u32 s62, s62, s63\n"
"s_add_u32 s52, %[w2lo], 0\n"
"s_addc_u32 s53, %[w2hi], 0\n"
"s_add_u32 s52, s52, s62\n"
"s_addc_u32 s53, s53, 0\n"
"s_and_b32 s53, s53, 0xffff\n"
"s_mov_b32 s54, 663552\n"
"s_mov_b32 s55, 0x20000\n"
"s_sub_u32 s54, s54, s63\n"
"s_waitcnt vmcnt(22)\n"
"mA 16,36,216,16\n"
"dT 0,9,36864\n"
"s_waitcnt vmcnt(21)\n"
"mA 20,36,220,20\n"
"s_waitcnt vmcnt(20)\n"
"mA 24,36,224,24\n"
"dT 2,9,41472\n"
"s_waitcnt vmcnt(19)\n"
"mA 28,36,228,28\n"
"mA 32,40,216,32\n"
"dT 4,10,36864\n"
"mA 36,40,220,36\n"
"buffer_load_dwordx4 a[132:135], v7, s[36:39], s91 offen offset:2048 sc1\n"
"mA 40,40,224,40\n"
"dT 6,10,41472\n"
"mA 44,40,228,44\n"
"mA 48,44,216,48\n"
"dT 8,9,37120\n"
"mA 52,44,220,52\n"
"mA 56,44,224,56\n"
"dT 10,9,41728\n"
"mA 60,44,228,60\n"
"s_add_u32 s84, s90, 58368\n"
"s_add_u32 s91, s84, 4096\n"
"buffer_load_dwordx4 a[136:139], v7, s[36:39], s84 offen sc1\n"
"mA 64,48,216,64\n"
"dT 12,10,37120\n"
"mA 68,48,220,68\n"
"mA 72,48,224,72\n"
"dT 14,10,41728\n"
"mA 76,48,228,76\n"
"mA 80,52,216,80\n"
"dT 16,9,37376\n"
"mA 84,52,220,84\n"
"buffer_load_dwordx4 a[140:143], v7, s[36:39], s84 offen offset:2048 sc1\n"
"mA 88,52,224,88\n"
"dT 18,9,41984\n"
"mA 92,52,228,92\n"
"mA 96,56,216,96\n"
"dT 20,10,37376\n"
"mA 100,56,220,100\n"
"mA 104,56,224,104\n"
"dT 22,10,41984\n"
"mA 108,56,228,108\n"
"buffer_load_dwordx4 a[144:147], v7, s[36:39], s91 offen sc1\n"
"mA 112,60,216,112\n"
"dT 24,9,37632\n"
"mA 116,60,220,116\n"
"mA 120,60,224,120\n"
"dT 26,9,42240\n"
"mA 124,60,228,124\n"
"mA 128,64,216,128\n"
"dT 28,10,37632\n"
"mA 132,64,220,132\n"
"buffer_load_dwordx4 a[148:151], v7, s[36:39], s91 offen offset:2048 sc1\n"
"mA 136,64,224,136\n"
"dT 30,10,42240\n"
"mA 140,64,228,140\n"
"mA 144,68,216,144\n"
"dT 32,11,36864\n"
"mA 148,68,220,148\n"
"mA 152,68,224,152\n"
"dT 34,11,41472\n"
"mA 156,68,228,156\n"
"s_add_u32 s84, s90, 65536\n"
"s_add_u32 s91, s84, 4096\n"
"buffer_load_dwordx4 a[152:155], v7, s[36:39], s84 offen sc1\n"
"s_waitcnt vmcnt(24) lgkmcnt(11)\n"
"mA 16,0,232,16\n"
"dT 36,5,36864\n"
"s_waitcnt vmcnt(23)\n"
"mA 20,0,236,20\n"
"s_waitcnt vmcnt(22)\n"
"mA 24,0,240,24\n"
"dT 38,5,41472\n"
"s_waitcnt vmcnt(21)\n"
"mA 28,0,244,28\n"
"mA 32,4,232,32\n"
"dT 40,6,36864\n"
"mA 36,4,236,36\n"
"buffer_load_dwordx4 a[156:159], v7, s[36:39], s84 offen offset:2048 sc1\n"
"mA 40,4,240,40\n"
"dT 42,6,41472\n"
"mA 44,4,244,44\n"
"mA 48,8,232,48\n"
"dT 44,5,37120\n"
"mA 52,8,236,52\n"
"mA 56,8,240,56\n"
"dT 46,5,41728\n"
"mA 60,8,244,60\n"
"buffer_load_dwordx4 a[160:163], v7, s[36:39], s91 offen sc1\n"
"s_waitcnt lgkmcnt(10)\n"
"mA 64,12,232,64\n"
"dT 48,6,37120\n"
"mA 68,12,236,68\n"
"mA 72,12,240,72\n"
"dT 50,6,41728\n"
"mA 76,12,244,76\n"
"mA 80,16,232,80\n"
"dT 52,5,37376\n"
"mA 84,16,236,84\n"
"buffer_load_dwordx4 a[164:167], v7, s[36:39], s91 offen offset:2048 sc1\n"
"mA 88,16,240,88\n"
"dT 54,5,41984\n"
"mA 92,16,244,92\n"
"mA 96,20,232,96\n"
"dT 56,6,37376\n"
"mA 100,20,236,100\n"
"mA 104,20,240,104\n"
"dT 58,6,41984\n"
"mA 108,20,244,108\n"
"s_add_u32 s84, s90, 66560\n"
"s_add_u32 s91, s84, 4096\n"
"buffer_load_dwordx4 a[168:171], v7, s[36:39], s84 offen sc1\n"
"mA 112,24,232,112\n"
"dT 60,5,37632\n"
"mA 116,24,236,116\n"
"mA 120,24,240,120\n"
"dT 62,5,42240\n"
"mA 124,24,244,124\n"
"s_waitcnt lgkmcnt(11)\n"
"mA 128,28,232,128\n"
"dT 64,6,37632\n"
"mA 132,28,236,132\n"
"buffer_load_dwordx4 a[172:175], v7, s[36:39], s84 offen offset:2048 sc1\n"
"mA 136,28,240,136\n"
"dT 66,6,42240\n"
"mA 140,28,244,140\n"
"mA 144,32,232,144\n"
"dT 68,8,36864\n"
"mA 148,32,236,148\n"
"mA 152,32,240,152\n"
"dT 70,8,41472\n"
"mA 156,32,244,156\n"
"buffer_load_dwordx4 a[176:179], v7, s[36:39], s91 offen sc1\n"
"s_waitcnt vmcnt(26)\n"
"mA 16,36,72,16\n"
"dT 0,5,46080\n"
"s_waitcnt vmcnt(25)\n"
"mA 20,36,76,20\n"
"s_waitcnt vmcnt(24)\n"
"mA 24,36,80,24\n"
"dT 2,5,50688\n"
"s_waitcnt vmcnt(23)\n"
"mA 28,36,84,28\n"
"s_waitcnt lgkmcnt(10)\n"
"mA 32,40,72,32\n"
"dT 4,6,46080\n"
"mA 36,40,76,36\n"
"buffer_load_dwordx4 a[180:183], v7, s[36:39], s91 offen offset:2048 sc1\n"
"mA 40,40,80,40\n"
"dT 6,6,50688\n"
"mA 44,40,84,44\n"
"mA 48,44,72,48\n"
"dT 8,5,46336\n"
"mA 52,44,76,52\n"
"mA 56,44,80,56\n"
"dT 10,5,50944\n"
"mA 60,44,84,60\n"
"s_add_u32 m0, s87, 9216\n"
"s_mov_b32 s65, s60\n"
"s_mul_i32 s65, s65, 165888\n"
"s_add_u32 s65, s65, 0\n"
"buffer_load_dwordx4 v248, s[52:55], s65 offen lds\n"
"mA 64,48,72,64\n"
"dT 12,6,46336\n"
"mA 68,48,76,68\n"
"mA 72,48,80,72\n"
"dT 14,6,50944\n"
"mA 76,48,84,76\n"
"mA 80,52,72,80\n"
"dT 16,5,46592\n"
"mA 84,52,76,84\n"
"s_add_u32 m0, s87, 13312\n"
"s_mov_b32 s65, s60\n"
"s_mul_i32 s65, s65, 165888\n"
"s_add_u32 s65, s65, 0\n"
"buffer_load_dwordx4 v249, s[52:55], s65 offen lds\n"
"s_mul_i32 s65, s88, 17408\n"
"s_add_u32 m0, s65, s89\n"
"s_mov_b32 s65, s60\n"
"s_mul_i32 s65, s65, 165888\n"
"s_add_u32 s65, s65, 0\n"
"buffer_load_dwordx4 v250, s[52:55], s65 offen lds\n"
"mA 88,52,80,88\n"
"dT 18,5,51200\n"
"mA 92,52,84,92\n"
"s_waitcnt lgkmcnt(8)\n"
"mA 96,56,72,96\n"
"dT 20,6,46592\n"
"mA 100,56,76,100\n"
"mA 104,56,80,104\n"
"dT 22,6,51200\n"
"mA 108,56,84,108\n"
"s_add_u32 m0, s87, 18432\n"
"s_mov_b32 s65, s60\n"
"s_mul_i32 s65, s65, 165888\n"
"s_add_u32 s65, s65, 36864\n"
"buffer_load_dwordx4 v248, s[52:55], s65 offen lds\n"
"mA 112,60,72,112\n"
"dT 24,5,46848\n"
"mA 116,60,76,116\n"
"mA 120,60,80,120\n"
"dT 26,5,51456\n"
"mA 124,60,84,124\n"
"mA 128,64,72,128\n"
"dT 28,6,46848\n"
"mA 132,64,76,132\n"
"s_add_u32 m0, s87, 22528\n"
"s_mov_b32 s65, s60\n"
"s_mul_i32 s65, s65, 165888\n"
"s_add_u32 s65, s65, 36864\n"
"buffer_load_dwordx4 v249, s[52:55], s65 offen lds\n"
"s_mul_i32 s65, s88, 26624\n"
"s_add_u32 m0, s65, s89\n"
"s_mov_b32 s65, s60\n"
"s_mul_i32 s65, s65, 165888\n"
"s_add_u32 s65, s65, 36864\n"
"buffer_load_dwordx4 v250, s[52:55], s65 offen lds\n"
"mA 136,64,80,136\n"
"dT 30,6,51456\n"
"mA 140,64,84,140\n"
"mA 144,68,72,144\n"
"dT 32,8,46080\n"
"mA 148,68,76,148\n"
"mA 152,68,80,152\n"
"dT 34,8,50688\n"
"mA 156,68,84,156\n"
"s_add_u32 m0, s87, 27648\n"
"s_mov_b32 s65, s60\n"
"s_mul_i32 s65, s65, 165888\n"
"s_add_u32 s65, s65, 73728\n"
"buffer_load_dwordx4 v248, s[52:55], s65 offen lds\n"
"s_waitcnt vmcnt(30)\n"
"mA 16,0,88,16\n"
"ds_read_b64_tr_b16 a[36:37], v9\n"
"s_waitcnt vmcnt(29)\n"
"mA 20,0,92,20\n"
"s_waitcnt vmcnt(28)\n"
"mA 24,0,96,24\n"
"dT 38,9,4608\n"
"s_waitcnt vmcnt(27)\n"
"mA 28,0,100,28\n"
"s_waitcnt lgkmcnt(7)\n"
"mA 32,4,88,32\n"
"ds_read_b64_tr_b16 a[40:41], v10\n"
"mA 36,4,92,36\n"
"s_add_u32 m0, s87, 31744\n"
"s_mov_b32 s65, s60\n"
"s_mul_i32 s65, s65, 165888\n"
"s_add_u32 s65, s65, 73728\n"
"buffer_load_dwordx4 v249, s[52:55], s65 offen lds\n"
"s_mul_i32 s65, s88, 35840\n"
"s_add_u32 m0, s65, s89\n"
"s_mov_b32 s65, s60\n"
"s_mul_i32 s65, s65, 165888\n"
"s_add_u32 s65, s65, 73728\n"
"buffer_load_dwordx4 v250, s[52:55], s65 offen lds\n"
"mA 40,4,96,40\n"
"dT 42,10,4608\n"
"mA 44,4,100,44\n"
"mA 48,8,88,48\n"
"dT 44,9,256\n"
"mA 52,8,92,52\n"
"mA 56,8,96,56\n"
"dT 46,9,4864\n"
"mA 60,8,100,60\n"
"s_add_u32 m0, s87, 36864\n"
"s_mov_b32 s65, s60\n"
"s_mul_i32 s65, s65, 165888\n"
"s_add_u32 s65, s65, 110592\n"
"buffer_load_dwordx4 v248, s[52:55], s65 offen lds\n"
"mA 64,12,88,64\n"
"dT 48,10,256\n"
"mA 68,12,92,68\n"
"mA 72,12,96,72\n"
"dT 50,10,4864\n"
"mA 76,12,100,76\n"
"mA 80,16,88,80\n"
"dT 52,9,512\n"
"mA 84,16,92,84\n"
"s_add_u32 m0, s87, 40960\n"
"s_mov_b32 s65, s60\n"
"s_mul_i32 s65, s65, 165888\n"
"s_add_u32 s65, s65, 110592\n"
"buffer_load_dwordx4 v249, s[52:55], s65 offen lds\n"
"s_mul_i32 s65, s88, 45056\n"
"s_add_u32 m0, s65, s89\n"
"s_mov_b32 s65, s60\n"
"s_mul_i32 s65, s65, 165888\n"
"s_add_u32 s65, s65, 110592\n"
"buffer_load_dwordx4 v250, s[52:55], s65 offen lds\n"
"mA 88,16,96,88\n"
"dT 54,9,5120\n"
"mA 92,16,100,92\n"
"mA 96,20,88,96\n"
"dT 56,10,512\n"
"mA 100,20,92,100\n"
"mA 104,20,96,104\n"
"dT 58,10,5120\n"
"mA 108,20,100,108\n"
"s_add_u32 m0, s87, 46080\n"
"s_cmp_le_u32 s60, 0\n"
"s_cselect_b32 s65, 1, 0\n"
"s_mul_i32 s65, s65, 165888\n"
"s_add_u32 s65, s65, 0\n"
"buffer_load_dwordx4 v248, s[52:55], s65 offen lds\n"
"s_waitcnt lgkmcnt(6)\n"
"mA 112,24,88,112\n"
"dT 60,9,768\n"
"mA 116,24,92,116\n"
"mA 120,24,96,120\n"
"dT 62,9,5376\n"
"mA 124,24,100,124\n"
"mA 128,28,88,128\n"
"dT 64,10,768\n"
"mA 132,28,92,132\n"
"s_add_u32 m0, s87, 50176\n"
"s_cmp_le_u32 s60, 0\n"
"s_cselect_b32 s65, 1, 0\n"
"s_mul_i32 s65, s65, 165888\n"
"s_add_u32 s65, s65, 0\n"
"buffer_load_dwordx4 v249, s[52:55], s65 offen lds\n"
"s_mul_i32 s65, s88, 54272\n"
"s_add_u32 m0, s65, s89\n"
"s_cmp_le_u32 s60, 0\n"
"s_cselect_b32 s65, 1, 0\n"
"s_mul_i32 s65, s65, 165888\n"
"s_add_u32 s65, s65, 0\n"
"buffer_load_dwordx4 v250, s[52:55], s65 offen lds\n"
"mA 136,28,96,136\n"
"dT 66,10,5376\n"
"mA 140,28,100,140\n"
"mA 144,32,88,144\n"
"ds_read_b64_tr_b16 a[68:69], v11\n"
"mA 148,32,92,148\n"
"mA 152,32,96,152\n"
"dT 70,11,4608\n"
"mA 156,32,100,156\n"
"s_add_u32 m0, s87, 55296\n"
"s_cmp_le_u32 s60, 0\n"
"s_cselect_b32 s65, 1, 0\n"
"s_mul_i32 s65, s65, 165888\n"
"s_add_u32 s65, s65, 36864\n"
"buffer_load_dwordx4 v248, s[52:55], s65 offen lds\n"
"s_waitcnt lgkmcnt(0)\n"
"s_barrier\n"
"s_waitcnt vmcnt(35)\n"
"mA 16,36,104,16\n"
"dT 0,9,9216\n"
"s_waitcnt vmcnt(34)\n"
"mA 20,36,108,20\n"
"s_waitcnt vmcnt(33)\n"
"mA 24,36,112,24\n"
"dT 2,9,13824\n"
"s_waitcnt vmcnt(32)\n"
"mA 28,36,116,28\n"
"mA 32,40,104,32\n"
"dT 4,9,18432\n"
"mA 36,40,108,36\n"
"s_add_u32 m0, s87, 59392\n"
"s_cmp_le_u32 s60, 0\n"
"s_cselect_b32 s65, 1, 0\n"
"s_mul_i32 s65, s65, 165888\n"
"s_add_u32 s65, s65, 36864\n"
"buffer_load_dwordx4 v249, s[52:55], s65 offen lds\n"
"s_mul_i32 s65, s88, 63488\n"
"s_add_u32 m0, s65, s89\n"
"s_cmp_le_u32 s60, 0\n"
"s_cselect_b32 s65, 1, 0\n"
"s_mul_i32 s65, s65, 165888\n"
"s_add_u32 s65, s65, 36864\n"
"buffer_load_dwordx4 v250, s[52:55], s65 offen lds\n"
"mA 40,40,112,40\n"
"dT 6,9,23040\n"
"mA 44,40,116,44\n"
"mA 48,44,104,48\n"
"dT 8,9,27648\n"
"mA 52,44,108,52\n"
"mA 56,44,112,56\n"
"dT 10,9,32256\n"
"mA 60,44,116,60\n"
"s_add_u32 m0, s87, 64512\n"
"s_cmp_le_u32 s60, 0\n"
"s_cselect_b32 s65, 1, 0\n"
"s_mul_i32 s65, s65, 165888\n"
"s_add_u32 s65, s65, 73728\n"
"buffer_load_dwordx4 v248, s[52:55], s65 offen lds\n"
"mA 64,48,104,64\n"
"ds_read_b64_tr_b16 a[12:13], v2\n"
"mA 68,48,108,68\n"
"mA 72,48,112,72\n"
"dT 14,2,4608\n"
"mA 76,48,116,76\n"
"mA 80,52,104,80\n"
"mA 84,52,108,84\n"
"s_add_u32 m0, s87, 68608\n"
"s_cmp_le_u32 s60, 0\n"
"s_cselect_b32 s65, 1, 0\n"
"s_mul_i32 s65, s65, 165888\n"
"s_add_u32 s65, s65, 73728\n"
"buffer_load_dwordx4 v249, s[52:55], s65 offen lds\n"
"s_mul_i32 s65, s88, 72704\n"
"s_add_u32 m0, s65, s89\n"
"s_cmp_le_u32 s60, 0\n"
"s_cselect_b32 s65, 1, 0\n"
"s_mul_i32 s65, s65, 165888\n"
"s_add_u32 s65, s65, 73728\n"
"buffer_load_dwordx4 v250, s[52:55], s65 offen lds\n"
"mA 88,52,112,88\n"
"mA 92,52,116,92\n"
"mA 96,56,104,96\n"
"mA 100,56,108,100\n"
"mA 104,56,112,104\n"
"mA 108,56,116,108\n"
"s_add_u32 m0, s87, 73728\n"
"s_cmp_le_u32 s60, 0\n"
"s_cselect_b32 s65, 1, 0\n"
"s_mul_i32 s65, s65, 165888\n"
"s_add_u32 s65, s65, 110592\n"
"buffer_load_dwordx4 v248, s[52:55], s65 offen lds\n"
"mA 112,60,104,112\n"
"mA 116,60,108,116\n"
"mA 120,60,112,120\n"
"mA 124,60,116,124\n"
"mA 128,64,104,128\n"
"mA 132,64,108,132\n"
"s_add_u32 m0, s87, 77824\n"
"s_cmp_le_u32 s60, 0\n"
"s_cselect_b32 s65, 1, 0\n"
"s_mul_i32 s65, s65, 165888\n"
"s_add_u32 s65, s65, 110592\n"
"buffer_load_dwordx4 v249, s[52:55], s65 offen lds\n"
"s_mul_i32 s65, s88, 81920\n"
"s_add_u32 m0, s65, s89\n"
"s_cmp_le_u32 s60, 0\n"
"s_cselect_b32 s65, 1, 0\n"
"s_mul_i32 s65, s65, 165888\n"
"s_add_u32 s65, s65, 110592\n"
"buffer_load_dwordx4 v250, s[52:55], s65 offen lds\n"
"mA 136,64,112,136\n"
"mA 140,64,116,140\n"
"mA 144,68,104,144\n"
"mA 148,68,108,148\n"
"mA 152,68,112,152\n"
"mA 156,68,116,156\n"
"s_add_u32 m0, s87, 82944\n"
"s_cmp_le_u32 s60, 1\n"
"s_cselect_b32 s65, 2, 1\n"
"s_mul_i32 s65, s65, 165888\n"
"s_add_u32 s65, s65, 0\n"
"buffer_load_dwordx4 v248, s[52:55], s65 offen lds\n"
"s_add_u32 m0, s87, 87040\n"
"s_cmp_le_u32 s60, 1\n"
"s_cselect_b32 s65, 2, 1\n"
"s_mul_i32 s65, s65, 165888\n"
"s_add_u32 s65, s65, 0\n"
"buffer_load_dwordx4 v249, s[52:55], s65 offen lds\n"
"s_mul_i32 s65, s88, 91136\n"
"s_add_u32 m0, s65, s89\n"
"s_cmp_le_u32 s60, 1\n"
"s_cselect_b32 s65, 2, 1\n"
"s_mul_i32 s65, s65, 165888\n"
"s_add_u32 s65, s65, 0\n"
"buffer_load_dwordx4 v250, s[52:55], s65 offen lds\n"
"s_waitcnt vmcnt(42) lgkmcnt(0)\n"
"mA 16,0,120,16\n"
"dT 36,10,9216\n"
"s_waitcnt vmcnt(38)\n"
"mA 16,4,136,16\n"
"s_add_u32 m0, s87, 92160\n"
"s_cmp_le_u32 s60, 1\n"
"s_cselect_b32 s65, 2, 1\n"
"s_mul_i32 s65, s65, 165888\n"
"s_add_u32 s65, s65, 36864\n"
"buffer_load_dwordx4 v248, s[52:55], s65 offen lds\n"
"s_waitcnt vmcnt(35)\n"
"mA 16,8,152,16\n"
"dT 38,10,13824\n"
"s_waitcnt vmcnt(31)\n"
"mA 16,12,168,16\n"
"mA 20,0,124,20\n"
"dT 40,10,18432\n"
"mA 20,4,140,20\n"
"s_add_u32 m0, s87, 96256\n"
"s_cmp_le_u32 s60, 1\n"
"s_cselect_b32 s65, 2, 1\n"
"s_mul_i32 s65, s65, 165888\n"
"s_add_u32 s65, s65, 36864\n"
"buffer_load_dwordx4 v249, s[52:55], s65 offen lds\n"
"mA 20,8,156,20\n"
"dT 42,10,23040\n"
"s_waitcnt vmcnt(31)\n"
"mA 20,12,172,20\n"
"mA 24,0,128,24\n"
"dT 44,10,27648\n"
"mA 24,4,144,24\n"
"mA 24,8,160,24\n"
"dT 46,10,32256\n"
"s_waitcnt vmcnt(30)\n"
"mA 24,12,176,24\n"
"mA 28,0,132,28\n"
"ds_read_b64_tr_b16 a[48:49], v3\n"
"mA 28,4,148,28\n"
"mA 28,8,164,28\n"
"dT 50,3,4608\n"
"s_waitcnt vmcnt(29)\n"
"mA 28,12,180,28\n"
"s_waitcnt lgkmcnt(6)\n"
"mA 32,36,120,32\n"
"dT 0,9,9472\n"
"s_waitcnt lgkmcnt(5)\n"
"mA 32,40,136,32\n"
"s_mul_i32 s65, s88, 100352\n"
"s_add_u32 m0, s65, s89\n"
"s_cmp_le_u32 s60, 1\n"
"s_cselect_b32 s65, 2, 1\n"
"s_mul_i32 s65, s65, 165888\n"
"s_add_u32 s65, s65, 36864\n"
"buffer_load_dwordx4 v250, s[52:55], s65 offen lds\n"
"v_mul_f32 v224, s92, v16\n"
"v_mul_f32 v225, s92, v17\n"
"v_mul_f32 v226, s92, v18\n"
"v_mul_f32 v227, s92, v19\n"
"v_mul_f32 v228, s92, v20\n"
"v_mul_f32 v229, s92, v21\n"
"s_waitcnt lgkmcnt(3)\n"
"mA 32,44,152,32\n"
"dT 2,9,14080\n"
"v_mul_f32 v230, s92, v22\n"
"v_mul_f32 v231, s92, v23\n"
"eX 224,224\n"
"eX 225,225\n"
"eX 226,226\n"
"eX 227,227\n"
"s_waitcnt lgkmcnt(2)\n"
"mA 32,48,168,32\n"
"eX 228,228\n"
"eX 229,229\n"
"eX 230,230\n"
"eX 231,231\n"
"v_add_f32 v224, 1.0, v224\n"
"v_add_f32 v225, 1.0, v225\n"
"mA 36,36,124,36\n"
"dT 4,9,18688\n"
"v_add_f32 v226, 1.0, v226\n"
"v_add_f32 v227, 1.0, v227\n"
"v_add_f32 v228, 1.0, v228\n"
"v_add_f32 v229, 1.0, v229\n"
"v_add_f32 v230, 1.0, v230\n"
"v_add_f32 v231, 1.0, v231\n"
"mA 36,40,140,36\n"
"s_add_u32 m0, s87, 101376\n"
"s_cmp_le_u32 s60, 1\n"
"s_cselect_b32 s65, 2, 1\n"
"s_mul_i32 s65, s65, 165888\n"
"s_add_u32 s65, s65, 73728\n"
"buffer_load_dwordx4 v248, s[52:55], s65 offen lds\n"
"v_rcp_f32 v224, v224\n"
"v_rcp_f32 v225, v225\n"
"v_rcp_f32 v226, v226\n"
"v_rcp_f32 v227, v227\n"
"v_rcp_f32 v228, v228\n"
"v_rcp_f32 v229, v229\n"
"mA 36,44,156,36\n"
"dT 6,9,23296\n"
"v_rcp_f32 v230, v230\n"
"v_rcp_f32 v231, v231\n"
"mU 16,16,224\n"
"mU 17,17,225\n"
"mU 18,18,226\n"
"mU 19,19,227\n"
"mA 36,48,172,36\n"
"mU 20,20,228\n"
"mU 21,21,229\n"
"mU 22,22,230\n"
"mU 23,23,231\n"
"cP 160,16,17\n"
"cP 161,18,19\n"
"mA 40,36,128,40\n"
"dT 8,9,27904\n"
"cP 164,20,21\n"
"cP 165,22,23\n"
"v_mul_f32 v224, s92, v24\n"
"v_mul_f32 v225, s92, v25\n"
"v_mul_f32 v226, s92, v26\n"
"v_mul_f32 v227, s92, v27\n"
"mA 40,40,144,40\n"
"v_mul_f32 v228, s92, v28\n"
"v_mul_f32 v229, s92, v29\n"
"v_mul_f32 v230, s92, v30\n"
"v_mul_f32 v231, s92, v31\n"
"eX 224,224\n"
"eX 225,225\n"
"mA 40,44,160,40\n"
"dT 10,9,32512\n"
"eX 226,226\n"
"eX 227,227\n"
"eX 228,228\n"
"eX 229,229\n"
"eX 230,230\n"
"eX 231,231\n"
"mA 40,48,176,40\n"
"v_add_f32 v224, 1.0, v224\n"
"v_add_f32 v225, 1.0, v225\n"
"v_add_f32 v226, 1.0, v226\n"
"v_add_f32 v227, 1.0, v227\n"
"v_add_f32 v228, 1.0, v228\n"
"v_add_f32 v229, 1.0, v229\n"
"mA 44,36,132,44\n"
"dT 12,2,256\n"
"v_add_f32 v230, 1.0, v230\n"
"v_add_f32 v231, 1.0, v231\n"
"v_rcp_f32 v224, v224\n"
"v_rcp_f32 v225, v225\n"
"v_rcp_f32 v226, v226\n"
"v_rcp_f32 v227, v227\n"
"mA 44,40,148,44\n"
"v_rcp_f32 v228, v228\n"
"v_rcp_f32 v229, v229\n"
"v_rcp_f32 v230, v230\n"
"v_rcp_f32 v231, v231\n"
"mU 24,24,224\n"
"mU 25,25,225\n"
"mA 44,44,164,44\n"
"dT 14,2,4864\n"
"mU 26,26,226\n"
"mU 27,27,227\n"
"mU 28,28,228\n"
"mU 29,29,229\n"
"mU 30,30,230\n"
"mU 31,31,231\n"
"mA 44,48,180,44\n"
"cP 168,24,25\n"
"cP 169,26,27\n"
"cP 172,28,29\n"
"cP 173,30,31\n"
"s_waitcnt lgkmcnt(2)\n"
"mA 48,0,120,48\n"
"dT 36,10,9472\n"
"mA 48,4,136,48\n"
"s_add_u32 m0, s87, 105472\n"
"s_cmp_le_u32 s60, 1\n"
"s_cselect_b32 s65, 2, 1\n"
"s_mul_i32 s65, s65, 165888\n"
"s_add_u32 s65, s65, 73728\n"
"buffer_load_dwordx4 v249, s[52:55], s65 offen lds\n"
"v_mul_f32 v224, s92, v32\n"
"v_mul_f32 v225, s92, v33\n"
"v_mul_f32 v226, s92, v34\n"
"v_mul_f32 v227, s92, v35\n"
"v_mul_f32 v228, s92, v36\n"
"v_mul_f32 v229, s92, v37\n"
"v_mul_f32 v230, s92, v38\n"
"mA 48,8,152,48\n"
"dT 38,10,14080\n"
"v_mul_f32 v231, s92, v39\n"
"eX 224,224\n"
"eX 225,225\n"
"eX 226,226\n"
"eX 227,227\n"
"eX 228,228\n"
"eX 229,229\n"
"s_waitcnt lgkmcnt(2)\n"
"mA 48,12,168,48\n"
"eX 230,230\n"
"eX 231,231\n"
"v_add_f32 v224, 1.0, v224\n"
"v_add_f32 v225, 1.0, v225\n"
"v_add_f32 v226, 1.0, v226\n"
"v_add_f32 v227, 1.0, v227\n"
"v_add_f32 v228, 1.0, v228\n"
"mA 52,0,124,52\n"
"dT 40,10,18688\n"
"v_add_f32 v229, 1.0, v229\n"
"v_add_f32 v230, 1.0, v230\n"
"v_add_f32 v231, 1.0, v231\n"
"v_rcp_f32 v224, v224\n"
"v_rcp_f32 v225, v225\n"
"v_rcp_f32 v226, v226\n"
"v_rcp_f32 v227, v227\n"
"mA 52,4,140,52\n"
"s_mul_i32 s65, s88, 109568\n"
"s_add_u32 m0, s65, s89\n"
"s_cmp_le_u32 s60, 1\n"
"s_cselect_b32 s65, 2, 1\n"
"s_mul_i32 s65, s65, 165888\n"
"s_add_u32 s65, s65, 73728\n"
"buffer_load_dwordx4 v250, s[52:55], s65 offen lds\n"
"v_rcp_f32 v228, v228\n"
"v_rcp_f32 v229, v229\n"
"v_rcp_f32 v230, v230\n"
"v_rcp_f32 v231, v231\n"
"mU 32,32,224\n"
"mU 33,33,225\n"
"mU 34,34,226\n"
"mA 52,8,156,52\n"
"dT 42,10,23296\n"
"mU 35,35,227\n"
"mU 36,36,228\n"
"mU 37,37,229\n"
"mU 38,38,230\n"
"mU 39,39,231\n"
"cP 162,32,33\n"
"cP 163,34,35\n"
"mA 52,12,172,52\n"
"buffer_store_dwordx4 v[160:163], v7, s[40:43], s73 offen sc1\n"
"cP 166,36,37\n"
"cP 167,38,39\n"
"buffer_store_dwordx4 v[164:167], v7, s[40:43], s73 offen offset:2048 sc1\n"
"v_mul_f32 v224, s92, v40\n"
"v_mul_f32 v225, s92, v41\n"
"v_mul_f32 v226, s92, v42\n"
"mA 56,0,128,56\n"
"dT 44,10,27904\n"
"v_mul_f32 v227, s92, v43\n"
"v_mul_f32 v228, s92, v44\n"
"v_mul_f32 v229, s92, v45\n"
"v_mul_f32 v230, s92, v46\n"
"v_mul_f32 v231, s92, v47\n"
"eX 224,224\n"
"eX 225,225\n"
"mA 56,4,144,56\n"
"eX 226,226\n"
"eX 227,227\n"
"eX 228,228\n"
"eX 229,229\n"
"eX 230,230\n"
"eX 231,231\n"
"v_add_f32 v224, 1.0, v224\n"
"mA 56,8,160,56\n"
"dT 46,10,32512\n"
"v_add_f32 v225, 1.0, v225\n"
"v_add_f32 v226, 1.0, v226\n"
"v_add_f32 v227, 1.0, v227\n"
"v_add_f32 v228, 1.0, v228\n"
"v_add_f32 v229, 1.0, v229\n"
"v_add_f32 v230, 1.0, v230\n"
"v_add_f32 v231, 1.0, v231\n"
"mA 56,12,176,56\n"
"v_rcp_f32 v224, v224\n"
"v_rcp_f32 v225, v225\n"
"v_rcp_f32 v226, v226\n"
"v_rcp_f32 v227, v227\n"
"v_rcp_f32 v228, v228\n"
"v_rcp_f32 v229, v229\n"
"v_rcp_f32 v230, v230\n"
"mA 60,0,132,60\n"
"dT 48,3,256\n"
"v_rcp_f32 v231, v231\n"
"mU 40,40,224\n"
"mU 41,41,225\n"
"mU 42,42,226\n"
"mU 43,43,227\n"
"mU 44,44,228\n"
"mU 45,45,229\n"
"mA 60,4,148,60\n"
"mU 46,46,230\n"
"mU 47,47,231\n"
"cP 170,40,41\n"
"cP 171,42,43\n"
"buffer_store_dwordx4 v[168:171], v7, s[40:43], s74 offen sc1\n"
"cP 174,44,45\n"
"cP 175,46,47\n"
"mA 60,8,164,60\n"
"dT 50,3,4864\n"
"buffer_store_dwordx4 v[172:175], v7, s[40:43], s74 offen offset:2048 sc1\n"
"mA 60,12,180,60\n"
"s_waitcnt lgkmcnt(3)\n"
"mA 64,36,120,64\n"
"dT 0,9,9728\n"
"mA 64,40,136,64\n"
"s_add_u32 m0, s87, 110592\n"
"s_cmp_le_u32 s60, 1\n"
"s_cselect_b32 s65, 2, 1\n"
"s_mul_i32 s65, s65, 165888\n"
"s_add_u32 s65, s65, 110592\n"
"buffer_load_dwordx4 v248, s[52:55], s65 offen lds\n"
"v_mul_f32 v224, s92, v48\n"
"v_mul_f32 v225, s92, v49\n"
"v_mul_f32 v226, s92, v50\n"
"v_mul_f32 v227, s92, v51\n"
"v_mul_f32 v228, s92, v52\n"
"v_mul_f32 v229, s92, v53\n"
"s_waitcnt lgkmcnt(3)\n"
"mA 64,44,152,64\n"
"dT 2,9,14336\n"
"v_mul_f32 v230, s92, v54\n"
"v_mul_f32 v231, s92, v55\n"
"eX 224,224\n"
"eX 225,225\n"
"eX 226,226\n"
"eX 227,227\n"
"s_waitcnt lgkmcnt(2)\n"
"mA 64,48,168,64\n"
"eX 228,228\n"
"eX 229,229\n"
"eX 230,230\n"
"eX 231,231\n"
"v_add_f32 v224, 1.0, v224\n"
"v_add_f32 v225, 1.0, v225\n"
"mA 68,36,124,68\n"
"dT 4,9,18944\n"
"v_add_f32 v226, 1.0, v226\n"
"v_add_f32 v227, 1.0, v227\n"
"v_add_f32 v228, 1.0, v228\n"
"v_add_f32 v229, 1.0, v229\n"
"v_add_f32 v230, 1.0, v230\n"
"v_add_f32 v231, 1.0, v231\n"
"mA 68,40,140,68\n"
"v_rcp_f32 v224, v224\n"
"v_rcp_f32 v225, v225\n"
"v_rcp_f32 v226, v226\n"
"v_rcp_f32 v227, v227\n"
"v_rcp_f32 v228, v228\n"
"v_rcp_f32 v229, v229\n"
"mA 68,44,156,68\n"
"dT 6,9,23552\n"
"v_rcp_f32 v230, v230\n"
"v_rcp_f32 v231, v231\n"
"mU 48,48,224\n"
"mU 49,49,225\n"
"mU 50,50,226\n"
"mU 51,51,227\n"
"mA 68,48,172,68\n"
"mU 52,52,228\n"
"mU 53,53,229\n"
"mU 54,54,230\n"
"mU 55,55,231\n"
"cP 176,48,49\n"
"cP 177,50,51\n"
"mA 72,36,128,72\n"
"dT 8,9,28160\n"
"cP 180,52,53\n"
"cP 181,54,55\n"
"v_mul_f32 v224, s92, v56\n"
"v_mul_f32 v225, s92, v57\n"
"v_mul_f32 v226, s92, v58\n"
"v_mul_f32 v227, s92, v59\n"
"mA 72,40,144,72\n"
"v_mul_f32 v228, s92, v60\n"
"v_mul_f32 v229, s92, v61\n"
"v_mul_f32 v230, s92, v62\n"
"v_mul_f32 v231, s92, v63\n"
"eX 224,224\n"
"eX 225,225\n"
"mA 72,44,160,72\n"
"dT 10,9,32768\n"
"eX 226,226\n"
"eX 227,227\n"
"eX 228,228\n"
"eX 229,229\n"
"eX 230,230\n"
"eX 231,231\n"
"mA 72,48,176,72\n"
"v_add_f32 v224, 1.0, v224\n"
"v_add_f32 v225, 1.0, v225\n"
"v_add_f32 v226, 1.0, v226\n"
"v_add_f32 v227, 1.0, v227\n"
"v_add_f32 v228, 1.0, v228\n"
"v_add_f32 v229, 1.0, v229\n"
"mA 76,36,132,76\n"
"dT 12,2,512\n"
"v_add_f32 v230, 1.0, v230\n"
"v_add_f32 v231, 1.0, v231\n"
"v_rcp_f32 v224, v224\n"
"v_rcp_f32 v225, v225\n"
"v_rcp_f32 v226, v226\n"
"v_rcp_f32 v227, v227\n"
"mA 76,40,148,76\n"
"v_rcp_f32 v228, v228\n"
"v_rcp_f32 v229, v229\n"
"v_rcp_f32 v230, v230\n"
"v_rcp_f32 v231, v231\n"
"mU 56,56,224\n"
"mU 57,57,225\n"
"mA 76,44,164,76\n"
"dT 14,2,5120\n"
"mU 58,58,226\n"
"mU 59,59,227\n"
"mU 60,60,228\n"
"mU 61,61,229\n"
"mU 62,62,230\n"
"mU 63,63,231\n"
"mA 76,48,180,76\n"
"cP 184,56,57\n"
"cP 185,58,59\n"
"cP 188,60,61\n"
"cP 189,62,63\n"
"s_waitcnt lgkmcnt(2)\n"
"mA 80,0,120,80\n"
"dT 36,10,9728\n"
"mA 80,4,136,80\n"
"s_add_u32 m0, s87, 114688\n"
"s_cmp_le_u32 s60, 1\n"
"s_cselect_b32 s65, 2, 1\n"
"s_mul_i32 s65, s65, 165888\n"
"s_add_u32 s65, s65, 110592\n"
"buffer_load_dwordx4 v249, s[52:55], s65 offen lds\n"
"v_mul_f32 v224, s92, v64\n"
"v_mul_f32 v225, s92, v65\n"
"v_mul_f32 v226, s92, v66\n"
"v_mul_f32 v227, s92, v67\n"
"v_mul_f32 v228, s92, v68\n"
"v_mul_f32 v229, s92, v69\n"
"v_mul_f32 v230, s92, v70\n"
"mA 80,8,152,80\n"
"dT 38,10,14336\n"
"v_mul_f32 v231, s92, v71\n"
"eX 224,224\n"
"eX 225,225\n"
"eX 226,226\n"
"eX 227,227\n"
"eX 228,228\n"
"eX 229,229\n"
"s_waitcnt lgkmcnt(2)\n"
"mA 80,12,168,80\n"
"eX 230,230\n"
"eX 231,231\n"
"v_add_f32 v224, 1.0, v224\n"
"v_add_f32 v225, 1.0, v225\n"
"v_add_f32 v226, 1.0, v226\n"
"v_add_f32 v227, 1.0, v227\n"
"v_add_f32 v228, 1.0, v228\n"
"mA 84,0,124,84\n"
"dT 40,10,18944\n"
"v_add_f32 v229, 1.0, v229\n"
"v_add_f32 v230, 1.0, v230\n"
"v_add_f32 v231, 1.0, v231\n"
"v_rcp_f32 v224, v224\n"
"v_rcp_f32 v225, v225\n"
"v_rcp_f32 v226, v226\n"
"v_rcp_f32 v227, v227\n"
"mA 84,4,140,84\n"
"v_rcp_f32 v228, v228\n"
"v_rcp_f32 v229, v229\n"
"v_rcp_f32 v230, v230\n"
"v_rcp_f32 v231, v231\n"
"mU 64,64,224\n"
"mU 65,65,225\n"
"mU 66,66,226\n"
"mA 84,8,156,84\n"
"dT 42,10,23552\n"
"mU 67,67,227\n"
"mU 68,68,228\n"
"mU 69,69,229\n"
"mU 70,70,230\n"
"mU 71,71,231\n"
"cP 178,64,65\n"
"cP 179,66,67\n"
"mA 84,12,172,84\n"
"buffer_store_dwordx4 v[176:179], v7, s[40:43], s75 offen sc1\n"
"cP 182,68,69\n"
"cP 183,70,71\n"
"buffer_store_dwordx4 v[180:183], v7, s[40:43], s75 offen offset:2048 sc1\n"
"v_mul_f32 v224, s92, v72\n"
"v_mul_f32 v225, s92, v73\n"
"v_mul_f32 v226, s92, v74\n"
"mA 88,0,128,88\n"
"dT 44,10,28160\n"
"v_mul_f32 v227, s92, v75\n"
"v_mul_f32 v228, s92, v76\n"
"v_mul_f32 v229, s92, v77\n"
"v_mul_f32 v230, s92, v78\n"
"v_mul_f32 v231, s92, v79\n"
"eX 224,224\n"
"eX 225,225\n"
"mA 88,4,144,88\n"
"eX 226,226\n"
"eX 227,227\n"
"eX 228,228\n"
"eX 229,229\n"
"eX 230,230\n"
"eX 231,231\n"
"v_add_f32 v224, 1.0, v224\n"
"mA 88,8,160,88\n"
"dT 46,10,32768\n"
"v_add_f32 v225, 1.0, v225\n"
"v_add_f32 v226, 1.0, v226\n"
"v_add_f32 v227, 1.0, v227\n"
"v_add_f32 v228, 1.0, v228\n"
"v_add_f32 v229, 1.0, v229\n"
"v_add_f32 v230, 1.0, v230\n"
"v_add_f32 v231, 1.0, v231\n"
"mA 88,12,176,88\n"
"v_rcp_f32 v224, v224\n"
"v_rcp_f32 v225, v225\n"
"v_rcp_f32 v226, v226\n"
"v_rcp_f32 v227, v227\n"
"v_rcp_f32 v228, v228\n"
"v_rcp_f32 v229, v229\n"
"v_rcp_f32 v230, v230\n"
"mA 92,0,132,92\n"
"dT 48,3,512\n"
"v_rcp_f32 v231, v231\n"
"mU 72,72,224\n"
"mU 73,73,225\n"
"mU 74,74,226\n"
"mU 75,75,227\n"
"mU 76,76,228\n"
"mU 77,77,229\n"
"mA 92,4,148,92\n"
"mU 78,78,230\n"
"mU 79,79,231\n"
"cP 186,72,73\n"
"cP 187,74,75\n"
"buffer_store_dwordx4 v[184:187], v7, s[40:43], s76 offen sc1\n"
"cP 190,76,77\n"
"cP 191,78,79\n"
"mA 92,8,164,92\n"
"dT 50,3,5120\n"
"buffer_store_dwordx4 v[188:191], v7, s[40:43], s76 offen offset:2048 sc1\n"
"mA 92,12,180,92\n"
"s_waitcnt lgkmcnt(3)\n"
"mA 96,36,120,96\n"
"dT 0,9,9984\n"
"mA 96,40,136,96\n"
"s_mul_i32 s65, s88, 118784\n"
"s_add_u32 m0, s65, s89\n"
"s_cmp_le_u32 s60, 1\n"
"s_cselect_b32 s65, 2, 1\n"
"s_mul_i32 s65, s65, 165888\n"
"s_add_u32 s65, s65, 110592\n"
"buffer_load_dwordx4 v250, s[52:55], s65 offen lds\n"
"v_mul_f32 v224, s92, v80\n"
"v_mul_f32 v225, s92, v81\n"
"v_mul_f32 v226, s92, v82\n"
"v_mul_f32 v227, s92, v83\n"
"v_mul_f32 v228, s92, v84\n"
"v_mul_f32 v229, s92, v85\n"
"s_waitcnt lgkmcnt(3)\n"
"mA 96,44,152,96\n"
"dT 2,9,14592\n"
"v_mul_f32 v230, s92, v86\n"
"v_mul_f32 v231, s92, v87\n"
"eX 224,224\n"
"eX 225,225\n"
"eX 226,226\n"
"eX 227,227\n"
"s_waitcnt lgkmcnt(2)\n"
"mA 96,48,168,96\n"
"eX 228,228\n"
"eX 229,229\n"
"eX 230,230\n"
"eX 231,231\n"
"v_add_f32 v224, 1.0, v224\n"
"v_add_f32 v225, 1.0, v225\n"
"mA 100,36,124,100\n"
"dT 4,9,19200\n"
"v_add_f32 v226, 1.0, v226\n"
"v_add_f32 v227, 1.0, v227\n"
"v_add_f32 v228, 1.0, v228\n"
"v_add_f32 v229, 1.0, v229\n"
"v_add_f32 v230, 1.0, v230\n"
"v_add_f32 v231, 1.0, v231\n"
"mA 100,40,140,100\n"
"v_rcp_f32 v224, v224\n"
"v_rcp_f32 v225, v225\n"
"v_rcp_f32 v226, v226\n"
"v_rcp_f32 v227, v227\n"
"v_rcp_f32 v228, v228\n"
"v_rcp_f32 v229, v229\n"
"mA 100,44,156,100\n"
"dT 6,9,23808\n"
"v_rcp_f32 v230, v230\n"
"v_rcp_f32 v231, v231\n"
"mU 80,80,224\n"
"mU 81,81,225\n"
"mU 82,82,226\n"
"mU 83,83,227\n"
"mA 100,48,172,100\n"
"mU 84,84,228\n"
"mU 85,85,229\n"
"mU 86,86,230\n"
"mU 87,87,231\n"
"cP 192,80,81\n"
"cP 193,82,83\n"
"mA 104,36,128,104\n"
"dT 8,9,28416\n"
"cP 196,84,85\n"
"cP 197,86,87\n"
"v_mul_f32 v224, s92, v88\n"
"v_mul_f32 v225, s92, v89\n"
"v_mul_f32 v226, s92, v90\n"
"v_mul_f32 v227, s92, v91\n"
"mA 104,40,144,104\n"
"v_mul_f32 v228, s92, v92\n"
"v_mul_f32 v229, s92, v93\n"
"v_mul_f32 v230, s92, v94\n"
"v_mul_f32 v231, s92, v95\n"
"eX 224,224\n"
"eX 225,225\n"
"mA 104,44,160,104\n"
"dT 10,9,33024\n"
"eX 226,226\n"
"eX 227,227\n"
"eX 228,228\n"
"eX 229,229\n"
"eX 230,230\n"
"eX 231,231\n"
"mA 104,48,176,104\n"
"v_add_f32 v224, 1.0, v224\n"
"v_add_f32 v225, 1.0, v225\n"
"v_add_f32 v226, 1.0, v226\n"
"v_add_f32 v227, 1.0, v227\n"
"v_add_f32 v228, 1.0, v228\n"
"v_add_f32 v229, 1.0, v229\n"
"mA 108,36,132,108\n"
"dT 12,2,768\n"
"v_add_f32 v230, 1.0, v230\n"
"v_add_f32 v231, 1.0, v231\n"
"v_rcp_f32 v224, v224\n"
"v_rcp_f32 v225, v225\n"
"v_rcp_f32 v226, v226\n"
"v_rcp_f32 v227, v227\n"
"mA 108,40,148,108\n"
"v_rcp_f32 v228, v228\n"
"v_rcp_f32 v229, v229\n"
"v_rcp_f32 v230, v230\n"
"v_rcp_f32 v231, v231\n"
"mU 88,88,224\n"
"mU 89,89,225\n"
"mA 108,44,164,108\n"
"dT 14,2,5376\n"
"mU 90,90,226\n"
"mU 91,91,227\n"
"mU 92,92,228\n"
"mU 93,93,229\n"
"mU 94,94,230\n"
"mU 95,95,231\n"
"mA 108,48,180,108\n"
"cP 200,88,89\n"
"cP 201,90,91\n"
"cP 204,92,93\n"
"cP 205,94,95\n"
"s_waitcnt lgkmcnt(2)\n"
"mA 112,0,120,112\n"
"dT 36,10,9984\n"
"mA 112,4,136,112\n"
"s_add_u32 m0, s87, 147456\n"
"s_cmp_le_u32 s60, 2\n"
"s_cselect_b32 s65, 3, 2\n"
"s_mul_i32 s65, s65, 165888\n"
"s_add_u32 s65, s65, 0\n"
"buffer_load_dwordx4 v248, s[52:55], s65 offen lds\n"
"v_mul_f32 v224, s92, v96\n"
"v_mul_f32 v225, s92, v97\n"
"v_mul_f32 v226, s92, v98\n"
"v_mul_f32 v227, s92, v99\n"
"v_mul_f32 v228, s92, v100\n"
"v_mul_f32 v229, s92, v101\n"
"v_mul_f32 v230, s92, v102\n"
"mA 112,8,152,112\n"
"dT 38,10,14592\n"
"v_mul_f32 v231, s92, v103\n"
"eX 224,224\n"
"eX 225,225\n"
"eX 226,226\n"
"eX 227,227\n"
"eX 228,228\n"
"eX 229,229\n"
"s_waitcnt lgkmcnt(2)\n"
"mA 112,12,168,112\n"
"eX 230,230\n"
"eX 231,231\n"
"v_add_f32 v224, 1.0, v224\n"
"v_add_f32 v225, 1.0, v225\n"
"v_add_f32 v226, 1.0, v226\n"
"v_add_f32 v227, 1.0, v227\n"
"v_add_f32 v228, 1.0, v228\n"
"mA 116,0,124,116\n"
"dT 40,10,19200\n"
"v_add_f32 v229, 1.0, v229\n"
"v_add_f32 v230, 1.0, v230\n"
"v_add_f32 v231, 1.0, v231\n"
"v_rcp_f32 v224, v224\n"
"v_rcp_f32 v225, v225\n"
"v_rcp_f32 v226, v226\n"
"v_rcp_f32 v227, v227\n"
"mA 116,4,140,116\n"
"v_rcp_f32 v228, v228\n"
"v_rcp_f32 v229, v229\n"
"v_rcp_f32 v230, v230\n"
"v_rcp_f32 v231, v231\n"
"mU 96,96,224\n"
"mU 97,97,225\n"
"mU 98,98,226\n"
"mA 116,8,156,116\n"
"dT 42,10,23808\n"
"mU 99,99,227\n"
"mU 100,100,228\n"
"mU 101,101,229\n"
"mU 102,102,230\n"
"mU 103,103,231\n"
"cP 194,96,97\n"
"cP 195,98,99\n"
"mA 116,12,172,116\n"
"buffer_store_dwordx4 v[192:195], v7, s[40:43], s77 offen sc1\n"
"cP 198,100,101\n"
"cP 199,102,103\n"
"buffer_store_dwordx4 v[196:199], v7, s[40:43], s77 offen offset:2048 sc1\n"
"v_mul_f32 v224, s92, v104\n"
"v_mul_f32 v225, s92, v105\n"
"v_mul_f32 v226, s92, v106\n"
"mA 120,0,128,120\n"
"dT 44,10,28416\n"
"v_mul_f32 v227, s92, v107\n"
"v_mul_f32 v228, s92, v108\n"
"v_mul_f32 v229, s92, v109\n"
"v_mul_f32 v230, s92, v110\n"
"v_mul_f32 v231, s92, v111\n"
"eX 224,224\n"
"eX 225,225\n"
"mA 120,4,144,120\n"
"eX 226,226\n"
"eX 227,227\n"
"eX 228,228\n"
"eX 229,229\n"
"eX 230,230\n"
"eX 231,231\n"
"v_add_f32 v224, 1.0, v224\n"
"mA 120,8,160,120\n"
"dT 46,10,33024\n"
"v_add_f32 v225, 1.0, v225\n"
"v_add_f32 v226, 1.0, v226\n"
"v_add_f32 v227, 1.0, v227\n"
"v_add_f32 v228, 1.0, v228\n"
"v_add_f32 v229, 1.0, v229\n"
"v_add_f32 v230, 1.0, v230\n"
"v_add_f32 v231, 1.0, v231\n"
"mA 120,12,176,120\n"
"v_rcp_f32 v224, v224\n"
"v_rcp_f32 v225, v225\n"
"v_rcp_f32 v226, v226\n"
"v_rcp_f32 v227, v227\n"
"v_rcp_f32 v228, v228\n"
"v_rcp_f32 v229, v229\n"
"v_rcp_f32 v230, v230\n"
"mA 124,0,132,124\n"
"dT 48,3,768\n"
"v_rcp_f32 v231, v231\n"
"mU 104,104,224\n"
"mU 105,105,225\n"
"mU 106,106,226\n"
"mU 107,107,227\n"
"mU 108,108,228\n"
"mU 109,109,229\n"
"mA 124,4,148,124\n"
"mU 110,110,230\n"
"mU 111,111,231\n"
"cP 202,104,105\n"
"cP 203,106,107\n"
"buffer_store_dwordx4 v[200:203], v7, s[40:43], s78 offen sc1\n"
"cP 206,108,109\n"
"cP 207,110,111\n"
"mA 124,8,164,124\n"
"dT 50,3,5376\n"
"buffer_store_dwordx4 v[204:207], v7, s[40:43], s78 offen offset:2048 sc1\n"
"mA 124,12,180,124\n"
"s_waitcnt lgkmcnt(3)\n"
"mA 128,36,120,128\n"
"dT 0,11,9216\n"
"mA 128,40,136,128\n"
"s_add_u32 m0, s87, 151552\n"
"s_cmp_le_u32 s60, 2\n"
"s_cselect_b32 s65, 3, 2\n"
"s_mul_i32 s65, s65, 165888\n"
"s_add_u32 s65, s65, 0\n"
"buffer_load_dwordx4 v249, s[52:55], s65 offen lds\n"
"v_mul_f32 v224, s92, v112\n"
"v_mul_f32 v225, s92, v113\n"
"v_mul_f32 v226, s92, v114\n"
"v_mul_f32 v227, s92, v115\n"
"v_mul_f32 v228, s92, v116\n"
"v_mul_f32 v229, s92, v117\n"
"s_waitcnt lgkmcnt(3)\n"
"mA 128,44,152,128\n"
"dT 2,11,13824\n"
"v_mul_f32 v230, s92, v118\n"
"v_mul_f32 v231, s92, v119\n"
"eX 224,224\n"
"eX 225,225\n"
"eX 226,226\n"
"eX 227,227\n"
"s_waitcnt lgkmcnt(2)\n"
"mA 128,48,168,128\n"
"eX 228,228\n"
"eX 229,229\n"
"eX 230,230\n"
"eX 231,231\n"
"v_add_f32 v224, 1.0, v224\n"
"v_add_f32 v225, 1.0, v225\n"
"mA 132,36,124,132\n"
"dT 4,11,18432\n"
"v_add_f32 v226, 1.0, v226\n"
"v_add_f32 v227, 1.0, v227\n"
"v_add_f32 v228, 1.0, v228\n"
"v_add_f32 v229, 1.0, v229\n"
"v_add_f32 v230, 1.0, v230\n"
"v_add_f32 v231, 1.0, v231\n"
"mA 132,40,140,132\n"
"v_rcp_f32 v224, v224\n"
"v_rcp_f32 v225, v225\n"
"v_rcp_f32 v226, v226\n"
"v_rcp_f32 v227, v227\n"
"v_rcp_f32 v228, v228\n"
"v_rcp_f32 v229, v229\n"
"mA 132,44,156,132\n"
"dT 6,11,23040\n"
"v_rcp_f32 v230, v230\n"
"v_rcp_f32 v231, v231\n"
"mU 112,112,224\n"
"mU 113,113,225\n"
"mU 114,114,226\n"
"mU 115,115,227\n"
"mA 132,48,172,132\n"
"mU 116,116,228\n"
"mU 117,117,229\n"
"mU 118,118,230\n"
"mU 119,119,231\n"
"cP 208,112,113\n"
"cP 209,114,115\n"
"mA 136,36,128,136\n"
"dT 8,11,27648\n"
"cP 212,116,117\n"
"cP 213,118,119\n"
"v_mul_f32 v224, s92, v120\n"
"v_mul_f32 v225, s92, v121\n"
"v_mul_f32 v226, s92, v122\n"
"v_mul_f32 v227, s92, v123\n"
"mA 136,40,144,136\n"
"v_mul_f32 v228, s92, v124\n"
"v_mul_f32 v229, s92, v125\n"
"v_mul_f32 v230, s92, v126\n"
"v_mul_f32 v231, s92, v127\n"
"eX 224,224\n"
"eX 225,225\n"
"mA 136,44,160,136\n"
"dT 10,11,32256\n"
"eX 226,226\n"
"eX 227,227\n"
"eX 228,228\n"
"eX 229,229\n"
"eX 230,230\n"
"eX 231,231\n"
"mA 136,48,176,136\n"
"v_add_f32 v224, 1.0, v224\n"
"v_add_f32 v225, 1.0, v225\n"
"v_add_f32 v226, 1.0, v226\n"
"v_add_f32 v227, 1.0, v227\n"
"v_add_f32 v228, 1.0, v228\n"
"v_add_f32 v229, 1.0, v229\n"
"mA 140,36,132,140\n"
"ds_read_b64_tr_b16 a[12:13], v4\n"
"v_add_f32 v230, 1.0, v230\n"
"v_add_f32 v231, 1.0, v231\n"
"v_rcp_f32 v224, v224\n"
"v_rcp_f32 v225, v225\n"
"v_rcp_f32 v226, v226\n"
"v_rcp_f32 v227, v227\n"
"mA 140,40,148,140\n"
"v_rcp_f32 v228, v228\n"
"v_rcp_f32 v229, v229\n"
"v_rcp_f32 v230, v230\n"
"v_rcp_f32 v231, v231\n"
"mU 120,120,224\n"
"mU 121,121,225\n"
"mA 140,44,164,140\n"
"dT 14,4,4608\n"
"mU 122,122,226\n"
"mU 123,123,227\n"
"mU 124,124,228\n"
"mU 125,125,229\n"
"mU 126,126,230\n"
"mU 127,127,231\n"
"mA 140,48,180,140\n"
"cP 216,120,121\n"
"cP 217,122,123\n"
"cP 220,124,125\n"
"cP 221,126,127\n"
"s_waitcnt lgkmcnt(2)\n"
"mA 144,0,120,144\n"
"mA 144,4,136,144\n"
"s_mul_i32 s65, s88, 155648\n"
"s_add_u32 m0, s65, s89\n"
"s_cmp_le_u32 s60, 2\n"
"s_cselect_b32 s65, 3, 2\n"
"s_mul_i32 s65, s65, 165888\n"
"s_add_u32 s65, s65, 0\n"
"buffer_load_dwordx4 v250, s[52:55], s65 offen lds\n"
"v_mul_f32 v224, s92, v128\n"
"v_mul_f32 v225, s92, v129\n"
"v_mul_f32 v226, s92, v130\n"
"v_mul_f32 v227, s92, v131\n"
"v_mul_f32 v228, s92, v132\n"
"v_mul_f32 v229, s92, v133\n"
"v_mul_f32 v230, s92, v134\n"
"mA 144,8,152,144\n"
"v_mul_f32 v231, s92, v135\n"
"eX 224,224\n"
"eX 225,225\n"
"eX 226,226\n"
"eX 227,227\n"
"eX 228,228\n"
"eX 229,229\n"
"s_waitcnt lgkmcnt(0)\n"
"mA 144,12,168,144\n"
"eX 230,230\n"
"eX 231,231\n"
"v_add_f32 v224, 1.0, v224\n"
"v_add_f32 v225, 1.0, v225\n"
"v_add_f32 v226, 1.0, v226\n"
"v_add_f32 v227, 1.0, v227\n"
"v_add_f32 v228, 1.0, v228\n"
"mA 148,0,124,148\n"
"v_add_f32 v229, 1.0, v229\n"
"v_add_f32 v230, 1.0, v230\n"
"v_add_f32 v231, 1.0, v231\n"
"v_rcp_f32 v224, v224\n"
"v_rcp_f32 v225, v225\n"
"v_rcp_f32 v226, v226\n"
"v_rcp_f32 v227, v227\n"
"mA 148,4,140,148\n"
"v_rcp_f32 v228, v228\n"
"v_rcp_f32 v229, v229\n"
"v_rcp_f32 v230, v230\n"
"v_rcp_f32 v231, v231\n"
"mU 128,128,224\n"
"mU 129,129,225\n"
"mU 130,130,226\n"
"mA 148,8,156,148\n"
"mU 131,131,227\n"
"mU 132,132,228\n"
"mU 133,133,229\n"
"mU 134,134,230\n"
"mU 135,135,231\n"
"cP 210,128,129\n"
"cP 211,130,131\n"
"mA 148,12,172,148\n"
"buffer_store_dwordx4 v[208:211], v7, s[40:43], s79 offen sc1\n"
"cP 214,132,133\n"
"cP 215,134,135\n"
"buffer_store_dwordx4 v[212:215], v7, s[40:43], s79 offen offset:2048 sc1\n"
"v_mul_f32 v224, s92, v136\n"
"v_mul_f32 v225, s92, v137\n"
"v_mul_f32 v226, s92, v138\n"
"mA 152,0,128,152\n"
"v_mul_f32 v227, s92, v139\n"
"v_mul_f32 v228, s92, v140\n"
"v_mul_f32 v229, s92, v141\n"
"v_mul_f32 v230, s92, v142\n"
"v_mul_f32 v231, s92, v143\n"
"eX 224,224\n"
"eX 225,225\n"
"mA 152,4,144,152\n"
"eX 226,226\n"
"eX 227,227\n"
"eX 228,228\n"
"eX 229,229\n"
"eX 230,230\n"
"eX 231,231\n"
"v_add_f32 v224, 1.0, v224\n"
"mA 152,8,160,152\n"
"v_add_f32 v225, 1.0, v225\n"
"v_add_f32 v226, 1.0, v226\n"
"v_add_f32 v227, 1.0, v227\n"
"v_add_f32 v228, 1.0, v228\n"
"v_add_f32 v229, 1.0, v229\n"
"v_add_f32 v230, 1.0, v230\n"
"v_add_f32 v231, 1.0, v231\n"
"mA 152,12,176,152\n"
"v_rcp_f32 v224, v224\n"
"v_rcp_f32 v225, v225\n"
"v_rcp_f32 v226, v226\n"
"v_rcp_f32 v227, v227\n"
"v_rcp_f32 v228, v228\n"
"v_rcp_f32 v229, v229\n"
"v_rcp_f32 v230, v230\n"
"mA 156,0,132,156\n"
"v_rcp_f32 v231, v231\n"
"mU 136,136,224\n"
"mU 137,137,225\n"
"mU 138,138,226\n"
"mU 139,139,227\n"
"mU 140,140,228\n"
"mU 141,141,229\n"
"mA 156,4,148,156\n"
"mU 142,142,230\n"
"mU 143,143,231\n"
"cP 218,136,137\n"
"cP 219,138,139\n"
"buffer_store_dwordx4 v[216:219], v7, s[40:43], s80 offen sc1\n"
"cP 222,140,141\n"
"cP 223,142,143\n"
"mA 156,8,164,156\n"
"buffer_store_dwordx4 v[220:223], v7, s[40:43], s80 offen offset:2048 sc1\n"
"mA 156,12,180,156\n"
"v_mul_f32 v224, s92, v144\n"
"v_mul_f32 v225, s92, v145\n"
"v_mul_f32 v226, s92, v146\n"
"v_mul_f32 v227, s92, v147\n"
"v_mul_f32 v228, s92, v148\n"
"v_mul_f32 v229, s92, v149\n"
"v_mul_f32 v230, s92, v150\n"
"v_mul_f32 v231, s92, v151\n"
"eX 224,224\n"
"eX 225,225\n"
"eX 226,226\n"
"eX 227,227\n"
"eX 228,228\n"
"eX 229,229\n"
"eX 230,230\n"
"eX 231,231\n"
"v_add_f32 v224, 1.0, v224\n"
"v_add_f32 v225, 1.0, v225\n"
"v_add_f32 v226, 1.0, v226\n"
"v_add_f32 v227, 1.0, v227\n"
"v_add_f32 v228, 1.0, v228\n"
"v_add_f32 v229, 1.0, v229\n"
"v_add_f32 v230, 1.0, v230\n"
"v_add_f32 v231, 1.0, v231\n"
"v_rcp_f32 v224, v224\n"
"v_rcp_f32 v225, v225\n"
"v_rcp_f32 v226, v226\n"
"v_rcp_f32 v227, v227\n"
"v_rcp_f32 v228, v228\n"
"v_rcp_f32 v229, v229\n"
"v_rcp_f32 v230, v230\n"
"v_rcp_f32 v231, v231\n"
"mU 144,144,224\n"
"mU 145,145,225\n"
"mU 146,146,226\n"
"mU 147,147,227\n"
"mU 148,148,228\n"
"mU 149,149,229\n"
"mU 150,150,230\n"
"mU 151,151,231\n"
"cP 144,144,145\n"
"cP 145,146,147\n"
"buffer_store_dwordx2 v[144:145], v12, s[40:43], s81 offen sc1\n"
"cP 148,148,149\n"
"cP 149,150,151\n"
"buffer_store_dwordx2 v[148:149], v12, s[40:43], s81 offen offset:2048 sc1\n"
"v_mul_f32 v224, s92, v152\n"
"v_mul_f32 v225, s92, v153\n"
"v_mul_f32 v226, s92, v154\n"
"v_mul_f32 v227, s92, v155\n"
"v_mul_f32 v228, s92, v156\n"
"v_mul_f32 v229, s92, v157\n"
"v_mul_f32 v230, s92, v158\n"
"v_mul_f32 v231, s92, v159\n"
"eX 224,224\n"
"eX 225,225\n"
"eX 226,226\n"
"eX 227,227\n"
"eX 228,228\n"
"eX 229,229\n"
"eX 230,230\n"
"eX 231,231\n"
"v_add_f32 v224, 1.0, v224\n"
"v_add_f32 v225, 1.0, v225\n"
"v_add_f32 v226, 1.0, v226\n"
"v_add_f32 v227, 1.0, v227\n"
"v_add_f32 v228, 1.0, v228\n"
"v_add_f32 v229, 1.0, v229\n"
"v_add_f32 v230, 1.0, v230\n"
"v_add_f32 v231, 1.0, v231\n"
"v_rcp_f32 v224, v224\n"
"v_rcp_f32 v225, v225\n"
"v_rcp_f32 v226, v226\n"
"v_rcp_f32 v227, v227\n"
"v_rcp_f32 v228, v228\n"
"v_rcp_f32 v229, v229\n"
"v_rcp_f32 v230, v230\n"
"v_rcp_f32 v231, v231\n"
"mU 152,152,224\n"
"mU 153,153,225\n"
"mU 154,154,226\n"
"mU 155,155,227\n"
"mU 156,156,228\n"
"mU 157,157,229\n"
"mU 158,158,230\n"
"mU 159,159,231\n"
"cP 152,152,153\n"
"cP 153,154,155\n"
"buffer_store_dwordx2 v[152:153], v12, s[40:43], s83 offen sc1\n"
"cP 156,156,157\n"
"cP 157,158,159\n"
"buffer_store_dwordx2 v[156:157], v12, s[40:43], s83 offen offset:2048 sc1\n"
"s_waitcnt vmcnt(56)\n"
"s_waitcnt vmcnt(53)\n"
"s_waitcnt vmcnt(50)\n"
"s_waitcnt vmcnt(47)\n"
"s_barrier\n"
"dT 0,2,9216\n"
"dT 2,2,13824\n"
"dT 4,3,9216\n"
"dT 6,3,13824\n"
"dT 8,2,9472\n"
"dT 10,2,14080\n"
"dT 12,3,9472\n"
"dT 14,3,14080\n"
"dT 16,2,9728\n"
"dT 18,2,14336\n"
"dT 20,3,9728\n"
"dT 22,3,14336\n"
"dT 24,2,9984\n"
"dT 26,2,14592\n"
"dT 28,3,9984\n"
"dT 30,3,14592\n"
"dT 32,4,9216\n"
"dT 34,4,13824\n"
"s_waitcnt lgkmcnt(15)\n"
"v_mfma_f32_16x16x32_bf16 v[16:19], a[0:3], v[160:163], 0\n"
"dT 36,2,18432\n"
"v_mfma_f32_16x16x32_bf16 v[20:23], a[0:3], v[164:167], 0\n"
"v_mfma_f32_16x16x32_bf16 v[24:27], a[0:3], v[168:171], 0\n"
"dT 38,2,23040\n"
"v_mfma_f32_16x16x32_bf16 v[28:31], a[0:3], v[172:175], 0\n"
"s_waitcnt lgkmcnt(15)\n"
"v_mfma_f32_16x16x32_bf16 v[32:35], a[4:7], v[160:163], 0\n"
"dT 40,3,18432\n"
"v_mfma_f32_16x16x32_bf16 v[36:39], a[4:7], v[164:167], 0\n"
"v_mfma_f32_16x16x32_bf16 v[40:43], a[4:7], v[168:171], 0\n"
"dT 42,3,23040\n"
"v_mfma_f32_16x16x32_bf16 v[44:47], a[4:7], v[172:175], 0\n"
"s_waitcnt lgkmcnt(15)\n"
"v_mfma_f32_16x16x32_bf16 v[48:51], a[8:11], v[160:163], 0\n"
"dT 44,2,18688\n"
"v_mfma_f32_16x16x32_bf16 v[52:55], a[8:11], v[164:167], 0\n"
"v_mfma_f32_16x16x32_bf16 v[56:59], a[8:11], v[168:171], 0\n"
"dT 46,2,23296\n"
"v_mfma_f32_16x16x32_bf16 v[60:63], a[8:11], v[172:175], 0\n"
"s_waitcnt lgkmcnt(15)\n"
"v_mfma_f32_16x16x32_bf16 v[64:67], a[12:15], v[160:163], 0\n"
"dT 48,3,18688\n"
"v_mfma_f32_16x16x32_bf16 v[68:71], a[12:15], v[164:167], 0\n"
"v_mfma_f32_16x16x32_bf16 v[72:75], a[12:15], v[168:171], 0\n"
"dT 50,3,23296\n"
"v_mfma_f32_16x16x32_bf16 v[76:79], a[12:15], v[172:175], 0\n"
"s_waitcnt lgkmcnt(15)\n"
"v_mfma_f32_16x16x32_bf16 v[80:83], a[16:19], v[160:163], 0\n"
"dT 52,2,18944\n"
"v_mfma_f32_16x16x32_bf16 v[84:87], a[16:19], v[164:167], 0\n"
"v_mfma_f32_16x16x32_bf16 v[88:91], a[16:19], v[168:171], 0\n"
"dT 54,2,23552\n"
"v_mfma_f32_16x16x32_bf16 v[92:95], a[16:19], v[172:175], 0\n"
"s_waitcnt lgkmcnt(15)\n"
"v_mfma_f32_16x16x32_bf16 v[96:99], a[20:23], v[160:163], 0\n"
"dT 56,3,18944\n"
"v_mfma_f32_16x16x32_bf16 v[100:103], a[20:23], v[164:167], 0\n"
"v_mfma_f32_16x16x32_bf16 v[104:107], a[20:23], v[168:171], 0\n"
"dT 58,3,23552\n"
"v_mfma_f32_16x16x32_bf16 v[108:111], a[20:23], v[172:175], 0\n"
"s_waitcnt lgkmcnt(12)\n"
"v_mfma_f32_16x16x32_bf16 v[112:115], a[24:27], v[160:163], 0\n"
"dT 60,2,19200\n"
"v_mfma_f32_16x16x32_bf16 v[116:119], a[24:27], v[164:167], 0\n"
"v_mfma_f32_16x16x32_bf16 v[120:123], a[24:27], v[168:171], 0\n"
"dT 62,2,23808\n"
"v_mfma_f32_16x16x32_bf16 v[124:127], a[24:27], v[172:175], 0\n"
"v_mfma_f32_16x16x32_bf16 v[128:131], a[28:31], v[160:163], 0\n"
"dT 64,3,19200\n"
"v_mfma_f32_16x16x32_bf16 v[132:135], a[28:31], v[164:167], 0\n"
"v_mfma_f32_16x16x32_bf16 v[136:139], a[28:31], v[168:171], 0\n"
"dT 66,3,23808\n"
"v_mfma_f32_16x16x32_bf16 v[140:143], a[28:31], v[172:175], 0\n"
"v_mfma_f32_16x16x32_bf16 v[144:147], a[32:35], v[160:163], 0\n"
"dT 68,4,18432\n"
"v_mfma_f32_16x16x32_bf16 v[148:151], a[32:35], v[164:167], 0\n"
"v_mfma_f32_16x16x32_bf16 v[152:155], a[32:35], v[168:171], 0\n"
"dT 70,4,23040\n"
"v_mfma_f32_16x16x32_bf16 v[156:159], a[32:35], v[172:175], 0\n"
"s_lshl_b32 s62, s58, 8\n"
"s_add_u32 s62, s62, 73728\n"
"s_add_u32 s66, %[wslo], s62\n"
"s_addc_u32 s67, %[wshi], 0\n"
"s_waitcnt vmcnt(0) lgkmcnt(0)\n"
"s_barrier\n"
"s_cmp_eq_u32 s56, 0\n"
"s_cbranch_scc0 L_sg15_%=\n"
"s_lshl_b32 s62, s59, 2\n"
"s_add_u32 s62, s62, s60\n"
"s_lshl_b32 s62, s62, 3\n"
"v_mov_b32 v234, s62\n"
"v_mov_b32 v232, s98\n"
"v_mov_b32 v233, s99\n"
"s_mov_b64 exec, 1\n"
"global_store_dwordx2 v234, v[232:233], s[66:67] sc1\n"
"s_mov_b64 exec, -1\n"
"L_sg15_%=:\n"
"s_mul_i32 s62, s58, 2359296\n"
"s_mul_i32 s63, s59, 73728\n"
"s_add_u32 s62, s62, s63\n"
"s_add_u32 s36, %[wslo], 7340032\n"
"s_addc_u32 s37, %[wshi], 0\n"
"s_add_u32 s36, s36, s62\n"
"s_addc_u32 s37, s37, 0\n"
"s_and_b32 s37, s37, 0xffff\n"
"s_mov_b32 s38, 1843200\n"
"s_mov_b32 s39, 0x20000\n"
"s_mul_i32 s90, s56, 589824\n"
"s_mul_i32 s62, s58, 294912\n"
"s_mul_i32 s63, s59, 2359296\n"
"s_add_u32 s62, s62, s63\n"
"s_add_u32 s40, %[wslo], 26214400\n"
"s_addc_u32 s41, %[wshi], 0\n"
"s_add_u32 s40, s40, s62\n"
"s_addc_u32 s41, s41, 0\n"
"s_and_b32 s41, s41, 0xffff\n"
"s_mov_b32 s42, 294912\n"
"s_mov_b32 s43, 0x20000\n"
"s_lshr_b32 s62, s60, 1\n"
"s_mul_i32 s64, s62, 9\n"
"s_and_b32 s63, s60, 1\n"
"s_lshl_b32 s63, s63, 2\n"
"s_add_u32 s63, s63, s64\n"
"s_mul_i32 s65, s56, 73728\n"
"s_add_u32 s62, s63, 0\n"
"s_lshr_b32 s73, s62, 1\n"
"s_lshl_b32 s73, s73, 13\n"
"s_and_b32 s62, s62, 1\n"
"s_lshl_b32 s62, s62, 10\n"
"s_add_u32 s73, s73, s62\n"
"s_add_u32 s73, s73, s65\n"
"s_add_u32 s74, s73, 4096\n"
"s_add_u32 s62, s63, 1\n"
"s_lshr_b32 s75, s62, 1\n"
"s_lshl_b32 s75, s75, 13\n"
"s_and_b32 s62, s62, 1\n"
"s_lshl_b32 s62, s62, 10\n"
"s_add_u32 s75, s75, s62\n"
"s_add_u32 s75, s75, s65\n"
"s_add_u32 s76, s75, 4096\n"
"s_add_u32 s62, s63, 2\n"
"s_lshr_b32 s77, s62, 1\n"
"s_lshl_b32 s77, s77, 13\n"
"s_and_b32 s62, s62, 1\n"
"s_lshl_b32 s62, s62, 10\n"
"s_add_u32 s77, s77, s62\n"
"s_add_u32 s77, s77, s65\n"
"s_add_u32 s78, s77, 4096\n"
"s_add_u32 s62, s63, 3\n"
"s_lshr_b32 s79, s62, 1\n"
"s_lshl_b32 s79, s79, 13\n"
"s_and_b32 s62, s62, 1\n"
"s_lshl_b32 s62, s62, 10\n"
"s_add_u32 s79, s79, s62\n"
"s_add_u32 s79, s79, s65\n"
"s_add_u32 s80, s79, 4096\n"
"s_add_u32 s62, s64, 8\n"
"s_lshr_b32 s81, s62, 1\n"
"s_lshl_b32 s81, s81, 13\n"
"s_and_b32 s62, s62, 1\n"
"s_lshl_b32 s62, s62, 10\n"
"s_add_u32 s81, s81, s62\n"
"s_add_u32 s81, s81, s65\n"
"s_add_u32 s83, s81, 4096\n"
"mB 16,36,176,16\n"
"dT 0,2,27648\n"
"mB 20,36,180,20\n"
"s_add_u32 m0, s87, 119808\n"
"s_cmp_le_u32 s60, 2\n"
"s_cselect_b32 s65, 3, 2\n"
"s_mul_i32 s65, s65, 165888\n"
"s_add_u32 s65, s65, 36864\n"
"buffer_load_dwordx4 v248, s[52:55], s65 offen lds\n"
"mB 24,36,184,24\n"
"dT 2,2,32256\n"
"mB 28,36,188,28\n"
"mB 32,40,176,32\n"
"dT 4,3,27648\n"
"mB 36,40,180,36\n"
"mB 40,40,184,40\n"
"dT 6,3,32256\n"
"mB 44,40,188,44\n"
"s_add_u32 m0, s87, 123904\n"
"s_cmp_le_u32 s60, 2\n"
"s_cselect_b32 s65, 3, 2\n"
"s_mul_i32 s65, s65, 165888\n"
"s_add_u32 s65, s65, 36864\n"
"buffer_load_dwordx4 v249, s[52:55], s65 offen lds\n"
"mB 48,44,176,48\n"
"dT 8,2,27904\n"
"mB 52,44,180,52\n"
"mB 56,44,184,56\n"
"dT 10,2,32512\n"
"mB 60,44,188,60\n"
"mB 64,48,176,64\n"
"dT 12,3,27904\n"
"mB 68,48,180,68\n"
"s_mul_i32 s65, s88, 128000\n"
"s_add_u32 m0, s65, s89\n"
"s_cmp_le_u32 s60, 2\n"
"s_cselect_b32 s65, 3, 2\n"
"s_mul_i32 s65, s65, 165888\n"
"s_add_u32 s65, s65, 36864\n"
"buffer_load_dwordx4 v250, s[52:55], s65 offen lds\n"
"mB 72,48,184,72\n"
"dT 14,3,32512\n"
"mB 76,48,188,76\n"
"mB 80,52,176,80\n"
"dT 16,2,28160\n"
"mB 84,52,180,84\n"
"mB 88,52,184,88\n"
"dT 18,2,32768\n"
"mB 92,52,188,92\n"
"s_add_u32 m0, s87, 129024\n"
"s_cmp_le_u32 s60, 2\n"
"s_cselect_b32 s65, 3, 2\n"
"s_mul_i32 s65, s65, 165888\n"
"s_add_u32 s65, s65, 73728\n"
"buffer_load_dwordx4 v248, s[52:55], s65 offen lds\n"
"mB 96,56,176,96\n"
"dT 20,3,28160\n"
"mB 100,56,180,100\n"
"mB 104,56,184,104\n"
"dT 22,3,32768\n"
"mB 108,56,188,108\n"
"mB 112,60,176,112\n"
"dT 24,2,28416\n"
"mB 116,60,180,116\n"
"s_add_u32 m0, s87, 133120\n"
"s_cmp_le_u32 s60, 2\n"
"s_cselect_b32 s65, 3, 2\n"
"s_mul_i32 s65, s65, 165888\n"
"s_add_u32 s65, s65, 73728\n"
"buffer_load_dwordx4 v249, s[52:55], s65 offen lds\n"
"mB 120,60,184,120\n"
"dT 26,2,33024\n"
"mB 124,60,188,124\n"
"mB 128,64,176,128\n"
"dT 28,3,28416\n"
"mB 132,64,180,132\n"
"mB 136,64,184,136\n"
"dT 30,3,33024\n"
"mB 140,64,188,140\n"
"s_mul_i32 s65, s88, 137216\n"
"s_add_u32 m0, s65, s89\n"
"s_cmp_le_u32 s60, 2\n"
"s_cselect_b32 s65, 3, 2\n"
"s_mul_i32 s65, s65, 165888\n"
"s_add_u32 s65, s65, 73728\n"
"buffer_load_dwordx4 v250, s[52:55], s65 offen lds\n"
"mB 144,68,176,144\n"
"dT 32,4,27648\n"
"mB 148,68,180,148\n"
"mB 152,68,184,152\n"
"dT 34,4,32256\n"
"mB 156,68,188,156\n"
"s_add_u32 m0, s87, 138240\n"
"s_cmp_le_u32 s60, 2\n"
"s_cselect_b32 s65, 3, 2\n"
"s_mul_i32 s65, s65, 165888\n"
"s_add_u32 s65, s65, 110592\n"
"buffer_load_dwordx4 v248, s[52:55], s65 offen lds\n"
"s_add_u32 m0, s87, 142336\n"
"s_cmp_le_u32 s60, 2\n"
"s_cselect_b32 s65, 3, 2\n"
"s_mul_i32 s65, s65, 165888\n"
"s_add_u32 s65, s65, 110592\n"
"buffer_load_dwordx4 v249, s[52:55], s65 offen lds\n"
"s_mul_i32 s65, s88, 146432\n"
"s_add_u32 m0, s65, s89\n"
"s_cmp_le_u32 s60, 2\n"
"s_cselect_b32 s65, 3, 2\n"
"s_mul_i32 s65, s65, 165888\n"
"s_add_u32 s65, s65, 110592\n"
"buffer_load_dwordx4 v250, s[52:55], s65 offen lds\n"
"s_lshl_b32 s62, s58, 8\n"
"s_add_u32 s62, s62, 73728\n"
"s_add_u32 s66, %[wslo], s62\n"
"s_addc_u32 s67, %[wshi], 0\n"
"s_cmp_eq_u32 s56, 0\n"
"s_cbranch_scc0 L_wd16_%=\n"
"s_lshl_b32 s62, s59, 5\n"
"v_and_b32 v13, 3, v1\n"
"v_lshl_add_u32 v13, v13, 3, s62\n"
"s_mov_b32 s63, 131072\n"
"L_wp16_%=:\n"
"global_load_dwordx2 v[14:15], v13, s[66:67] sc1\n"
"s_waitcnt vmcnt(0)\n"
"v_cmp_eq_u64 vcc, s[98:99], v[14:15]\n"
"s_nop 1\n"
"s_cmp_eq_u64 vcc, -1\n"
"s_cbranch_scc1 L_wd16_%=\n"
"s_sleep 2\n"
"s_sub_u32 s63, s63, 1\n"
"s_cmp_lg_u32 s63, 0\n"
"s_cbranch_scc1 L_wp16_%=\n"
"L_wd16_%=:\n"
"s_barrier\n"
"s_cmp_le_u32 s60, 0\n"
"s_cselect_b32 s62, 1, 0\n"
"s_lshl_b32 s63, s62, 2\n"
"s_lshr_b32 s62, s62, 1\n"
"s_add_u32 s63, s63, s62\n"
"s_add_u32 s63, s63, 0\n"
"s_lshr_b32 s62, s63, 1\n"
"s_lshl_b32 s62, s62, 13\n"
"s_and_b32 s63, s63, 1\n"
"s_lshl_b32 s63, s63, 10\n"
"s_add_u32 s84, s90, s62\n"
"s_add_u32 s84, s84, s63\n"
"s_add_u32 s91, s84, 4096\n"
"buffer_load_dwordx4 a[136:139], v7, s[36:39], s84 offen sc1\n"
"buffer_load_dwordx4 a[140:143], v7, s[36:39], s84 offen offset:2048 sc1\n"
"buffer_load_dwordx4 a[144:147], v7, s[36:39], s91 offen sc1\n"
"buffer_load_dwordx4 a[148:151], v7, s[36:39], s91 offen offset:2048 sc1\n"
"s_cmp_le_u32 s60, 0\n"
"s_cselect_b32 s62, 1, 0\n"
"s_lshl_b32 s63, s62, 2\n"
"s_lshr_b32 s62, s62, 1\n"
"s_add_u32 s63, s63, s62\n"
"s_add_u32 s63, s63, 1\n"
"s_lshr_b32 s62, s63, 1\n"
"s_lshl_b32 s62, s62, 13\n"
"s_and_b32 s63, s63, 1\n"
"s_lshl_b32 s63, s63, 10\n"
"s_add_u32 s84, s90, s62\n"
"s_add_u32 s84, s84, s63\n"
"s_add_u32 s91, s84, 4096\n"
"buffer_load_dwordx4 a[152:155], v7, s[36:39], s84 offen sc1\n"
"buffer_load_dwordx4 a[156:159], v7, s[36:39], s84 offen offset:2048 sc1\n"
"buffer_load_dwordx4 a[160:163], v7, s[36:39], s91 offen sc1\n"
"buffer_load_dwordx4 a[164:167], v7, s[36:39], s91 offen offset:2048 sc1\n"
"s_waitcnt lgkmcnt(0)\n"
"mB 16,0,192,16\n"
"dT 36,2,36864\n"
"mB 20,0,196,20\n"
"s_cmp_le_u32 s60, 0\n"
"s_cselect_b32 s62, 1, 0\n"
"s_lshl_b32 s63, s62, 2\n"
"s_lshr_b32 s62, s62, 1\n"
"s_add_u32 s63, s63, s62\n"
"s_add_u32 s63, s63, 2\n"
"s_lshr_b32 s62, s63, 1\n"
"s_lshl_b32 s62, s62, 13\n"
"s_and_b32 s63, s63, 1\n"
"s_lshl_b32 s63, s63, 10\n"
"s_add_u32 s84, s90, s62\n"
"s_add_u32 s84, s84, s63\n"
"s_add_u32 s91, s84, 4096\n"
"buffer_load_dwordx4 a[168:171], v7, s[36:39], s84 offen sc1\n"
"mB 24,0,200,24\n"
"dT 38,2,41472\n"
"mB 28,0,204,28\n"
"mB 32,4,192,32\n"
"dT 40,3,36864\n"
"mB 36,4,196,36\n"
"mB 40,4,200,40\n"
"dT 42,3,41472\n"
"mB 44,4,204,44\n"
"buffer_load_dwordx4 a[172:175], v7, s[36:39], s84 offen offset:2048 sc1\n"
"mB 48,8,192,48\n"
"dT 44,2,37120\n"
"mB 52,8,196,52\n"
"mB 56,8,200,56\n"
"dT 46,2,41728\n"
"mB 60,8,204,60\n"
"mB 64,12,192,64\n"
"dT 48,3,37120\n"
"mB 68,12,196,68\n"
"buffer_load_dwordx4 a[176:179], v7, s[36:39], s91 offen sc1\n"
"mB 72,12,200,72\n"
"dT 50,3,41728\n"
"mB 76,12,204,76\n"
"mB 80,16,192,80\n"
"dT 52,2,37376\n"
"mB 84,16,196,84\n"
"mB 88,16,200,88\n"
"dT 54,2,41984\n"
"mB 92,16,204,92\n"
"buffer_load_dwordx4 a[180:183], v7, s[36:39], s91 offen offset:2048 sc1\n"
"mB 96,20,192,96\n"
"dT 56,3,37376\n"
"mB 100,20,196,100\n"
"mB 104,20,200,104\n"
"dT 58,3,41984\n"
"mB 108,20,204,108\n"
"mB 112,24,192,112\n"
"dT 60,2,37632\n"
"mB 116,24,196,116\n"
"s_add_u32 m0, s87, 0\n"
"s_mov_b32 s65, 147456\n"
"buffer_load_dwordx4 v248, s[52:55], s65 offen lds\n"
"mB 120,24,200,120\n"
"dT 62,2,42240\n"
"mB 124,24,204,124\n"
"mB 128,28,192,128\n"
"dT 64,3,37632\n"
"mB 132,28,196,132\n"
"mB 136,28,200,136\n"
"dT 66,3,42240\n"
"mB 140,28,204,140\n"
"s_add_u32 m0, s87, 4096\n"
"s_mov_b32 s65, 147456\n"
"buffer_load_dwordx4 v251, s[52:55], s65 offen lds\n"
"mB 144,32,192,144\n"
"dT 68,4,36864\n"
"mB 148,32,196,148\n"
"mB 152,32,200,152\n"
"dT 70,4,41472\n"
"mB 156,32,204,156\n"
"s_waitcnt lgkmcnt(11)\n"
"mB 16,36,208,16\n"
"dT 0,2,46080\n"
"mB 20,36,212,20\n"
"s_mul_i32 s65, s88, 8192\n"
"s_add_u32 m0, s65, s89\n"
"s_mov_b32 s65, 294912\n"
"buffer_load_dwordx4 v250, s[52:55], s65 offen lds\n"
"mB 24,36,216,24\n"
"dT 2,2,50688\n"
"mB 28,36,220,28\n"
"mB 32,40,208,32\n"
"dT 4,3,46080\n"
"mB 36,40,212,36\n"
"mB 40,40,216,40\n"
"dT 6,3,50688\n"
"mB 44,40,220,44\n"
"s_cmp_le_u32 s60, 0\n"
"s_cselect_b32 s62, 1, 0\n"
"s_lshl_b32 s63, s62, 2\n"
"s_lshr_b32 s62, s62, 1\n"
"s_add_u32 s63, s63, s62\n"
"s_add_u32 s63, s63, 3\n"
"s_lshr_b32 s62, s63, 1\n"
"s_lshl_b32 s62, s62, 13\n"
"s_and_b32 s63, s63, 1\n"
"s_lshl_b32 s63, s63, 10\n"
"s_add_u32 s84, s90, s62\n"
"s_add_u32 s84, s84, s63\n"
"s_add_u32 s91, s84, 4096\n"
"buffer_load_dwordx4 a[184:187], v7, s[36:39], s84 offen sc1\n"
"mB 48,44,208,48\n"
"dT 8,2,46336\n"
"mB 52,44,212,52\n"
"mB 56,44,216,56\n"
"dT 10,2,50944\n"
"mB 60,44,220,60\n"
"s_waitcnt lgkmcnt(7)\n"
"mB 64,48,208,64\n"
"dT 12,3,46336\n"
"mB 68,48,212,68\n"
"buffer_load_dwordx4 a[188:191], v7, s[36:39], s84 offen offset:2048 sc1\n"
"mB 72,48,216,72\n"
"dT 14,3,50944\n"
"mB 76,48,220,76\n"
"mB 80,52,208,80\n"
"dT 16,2,46592\n"
"mB 84,52,212,84\n"
"mB 88,52,216,88\n"
"dT 18,2,51200\n"
"mB 92,52,220,92\n"
"buffer_load_dwordx4 a[192:195], v7, s[36:39], s91 offen sc1\n"
"mB 96,56,208,96\n"
"dT 20,3,46592\n"
"mB 100,56,212,100\n"
"mB 104,56,216,104\n"
"dT 22,3,51200\n"
"mB 108,56,220,108\n"
"mB 112,60,208,112\n"
"dT 24,2,46848\n"
"mB 116,60,212,116\n"
"buffer_load_dwordx4 a[196:199], v7, s[36:39], s91 offen offset:2048 sc1\n"
"mB 120,60,216,120\n"
"dT 26,2,51456\n"
"mB 124,60,220,124\n"
"mB 128,64,208,128\n"
"dT 28,3,46848\n"
"mB 132,64,212,132\n"
"mB 136,64,216,136\n"
"dT 30,3,51456\n"
"mB 140,64,220,140\n"
"s_add_u32 m0, s87, 9216\n"
"s_mov_b32 s65, 479232\n"
"buffer_load_dwordx4 v248, s[52:55], s65 offen lds\n"
"s_waitcnt lgkmcnt(11)\n"
"mB 144,68,208,144\n"
"dT 32,4,46080\n"
"mB 148,68,212,148\n"
"mB 152,68,216,152\n"
"dT 34,4,50688\n"
"mB 156,68,220,156\n"
"s_waitcnt vmcnt(16)\n"
"s_barrier\n"
"mA 16,0,136,16\n"
"ds_read_b64_tr_b16 a[36:37], v5\n"
"mA 20,0,140,20\n"
"s_add_u32 m0, s87, 13312\n"
"s_mov_b32 s65, 479232\n"
"buffer_load_dwordx4 v251, s[52:55], s65 offen lds\n"
"mA 24,0,144,24\n"
"dT 38,5,4608\n"
"mA 28,0,148,28\n"
"mA 32,4,136,32\n"
"ds_read_b64_tr_b16 a[40:41], v6\n"
"mA 36,4,140,36\n"
"mA 40,4,144,40\n"
"dT 42,6,4608\n"
"mA 44,4,148,44\n"
"s_mul_i32 s65, s88, 17408\n"
"s_add_u32 m0, s65, s89\n"
"s_mov_b32 s65, 626688\n"
"buffer_load_dwordx4 v250, s[52:55], s65 offen lds\n"
"s_waitcnt lgkmcnt(9)\n"
"mA 48,8,136,48\n"
"dT 44,5,256\n"
"mA 52,8,140,52\n"
"mA 56,8,144,56\n"
"dT 46,5,4864\n"
"mA 60,8,148,60\n"
"mA 64,12,136,64\n"
"dT 48,6,256\n"
"mA 68,12,140,68\n"
"s_cmp_le_u32 s60, 1\n"
"s_cselect_b32 s62, 2, 1\n"
"s_lshl_b32 s63, s62, 2\n"
"s_lshr_b32 s62, s62, 1\n"
"s_add_u32 s63, s63, s62\n"
"s_add_u32 s63, s63, 0\n"
"s_lshr_b32 s62, s63, 1\n"
"s_lshl_b32 s62, s62, 13\n"
"s_and_b32 s63, s63, 1\n"
"s_lshl_b32 s63, s63, 10\n"
"s_add_u32 s84, s90, s62\n"
"s_add_u32 s84, s84, s63\n"
"s_add_u32 s91, s84, 4096\n"
"buffer_load_dwordx4 a[200:203], v7, s[36:39], s84 offen sc1\n"
"mA 72,12,144,72\n"
"dT 50,6,4864\n"
"mA 76,12,148,76\n"
"mA 80,16,136,80\n"
"dT 52,5,512\n"
"mA 84,16,140,84\n"
"mA 88,16,144,88\n"
"dT 54,5,5120\n"
"mA 92,16,148,92\n"
"buffer_load_dwordx4 a[204:207], v7, s[36:39], s84 offen offset:2048 sc1\n"
"mA 96,20,136,96\n"
"dT 56,6,512\n"
"mA 100,20,140,100\n"
"mA 104,20,144,104\n"
"dT 58,6,5120\n"
"mA 108,20,148,108\n"
"s_waitcnt lgkmcnt(8)\n"
"mA 112,24,136,112\n"
"dT 60,5,768\n"
"mA 116,24,140,116\n"
"buffer_load_dwordx4 a[208:211], v7, s[36:39], s91 offen sc1\n"
"mA 120,24,144,120\n"
"dT 62,5,5376\n"
"mA 124,24,148,124\n"
"mA 128,28,136,128\n"
"dT 64,6,768\n"
"mA 132,28,140,132\n"
"mA 136,28,144,136\n"
"dT 66,6,5376\n"
"mA 140,28,148,140\n"
"buffer_load_dwordx4 a[212:215], v7, s[36:39], s91 offen offset:2048 sc1\n"
"mA 144,32,136,144\n"
"ds_read_b64_tr_b16 a[68:69], v8\n"
"mA 148,32,140,148\n"
"mA 152,32,144,152\n"
"dT 70,8,4608\n"
"mA 156,32,148,156\n"
"s_waitcnt vmcnt(21)\n"
"mA 16,36,152,16\n"
"dT 0,5,9216\n"
"s_waitcnt vmcnt(20)\n"
"mA 20,36,156,20\n"
"s_cmp_le_u32 s60, 1\n"
"s_cselect_b32 s62, 2, 1\n"
"s_lshl_b32 s63, s62, 2\n"
"s_lshr_b32 s62, s62, 1\n"
"s_add_u32 s63, s63, s62\n"
"s_add_u32 s63, s63, 1\n"
"s_lshr_b32 s62, s63, 1\n"
"s_lshl_b32 s62, s62, 13\n"
"s_and_b32 s63, s63, 1\n"
"s_lshl_b32 s63, s63, 10\n"
"s_add_u32 s84, s90, s62\n"
"s_add_u32 s84, s84, s63\n"
"s_add_u32 s91, s84, 4096\n"
"buffer_load_dwordx4 a[216:219], v7, s[36:39], s84 offen sc1\n"
"s_waitcnt vmcnt(20)\n"
"mA 24,36,160,24\n"
"dT 2,5,13824\n"
"s_waitcnt vmcnt(19)\n"
"mA 28,36,164,28\n"
"mA 32,40,152,32\n"
"dT 4,6,9216\n"
"mA 36,40,156,36\n"
"mA 40,40,160,40\n"
"dT 6,6,13824\n"
"mA 44,40,164,44\n"
"buffer_load_dwordx4 a[220:223], v7, s[36:39], s84 offen offset:2048 sc1\n"
"s_waitcnt lgkmcnt(7)\n"
"mA 48,44,152,48\n"
"dT 8,5,9472\n"
"mA 52,44,156,52\n"
"mA 56,44,160,56\n"
"dT 10,5,14080\n"
"mA 60,44,164,60\n"
"mA 64,48,152,64\n"
"dT 12,6,9472\n"
"mA 68,48,156,68\n"
"buffer_load_dwordx4 a[224:227], v7, s[36:39], s91 offen sc1\n"
"mA 72,48,160,72\n"
"dT 14,6,14080\n"
"mA 76,48,164,76\n"
"mA 80,52,152,80\n"
"dT 16,5,9728\n"
"mA 84,52,156,84\n"
"mA 88,52,160,88\n"
"dT 18,5,14336\n"
"mA 92,52,164,92\n"
"buffer_load_dwordx4 a[228:231], v7, s[36:39], s91 offen offset:2048 sc1\n"
"mA 96,56,152,96\n"
"dT 20,6,9728\n"
"mA 100,56,156,100\n"
"mA 104,56,160,104\n"
"dT 22,6,14336\n"
"mA 108,56,164,108\n"
"mA 112,60,152,112\n"
"dT 24,5,9984\n"
"mA 116,60,156,116\n"
"s_cmp_le_u32 s60, 1\n"
"s_cselect_b32 s62, 2, 1\n"
"s_lshl_b32 s63, s62, 2\n"
"s_lshr_b32 s62, s62, 1\n"
"s_add_u32 s63, s63, s62\n"
"s_add_u32 s63, s63, 2\n"
"s_lshr_b32 s62, s63, 1\n"
"s_lshl_b32 s62, s62, 13\n"
"s_and_b32 s63, s63, 1\n"
"s_lshl_b32 s63, s63, 10\n"
"s_add_u32 s84, s90, s62\n"
"s_add_u32 s84, s84, s63\n"
"s_add_u32 s91, s84, 4096\n"
"buffer_load_dwordx4 a[232:235], v7, s[36:39], s84 offen sc1\n"
"mA 120,60,160,120\n"
"dT 26,5,14592\n"
"mA 124,60,164,124\n"
"s_waitcnt lgkmcnt(8)\n"
"mA 128,64,152,128\n"
"dT 28,6,9984\n"
"mA 132,64,156,132\n"
"mA 136,64,160,136\n"
"dT 30,6,14592\n"
"mA 140,64,164,140\n"
"buffer_load_dwordx4 a[236:239], v7, s[36:39], s84 offen offset:2048 sc1\n"
"mA 144,68,152,144\n"
"dT 32,8,9216\n"
"mA 148,68,156,148\n"
"mA 152,68,160,152\n"
"dT 34,8,13824\n"
"mA 156,68,164,156\n"
"s_waitcnt vmcnt(23)\n"
"mA 16,0,168,16\n"
"dT 36,5,18432\n"
"s_waitcnt vmcnt(22)\n"
"mA 20,0,172,20\n"
"buffer_load_dwordx4 a[240:243], v7, s[36:39], s91 offen sc1\n"
"s_waitcnt vmcnt(22)\n"
"mA 24,0,176,24\n"
"dT 38,5,23040\n"
"s_waitcnt vmcnt(21)\n"
"mA 28,0,180,28\n"
"mA 32,4,168,32\n"
"dT 40,6,18432\n"
"mA 36,4,172,36\n"
"mA 40,4,176,40\n"
"dT 42,6,23040\n"
"mA 44,4,180,44\n"
"buffer_load_dwordx4 a[244:247], v7, s[36:39], s91 offen offset:2048 sc1\n"
"mA 48,8,168,48\n"
"dT 44,5,18688\n"
"mA 52,8,172,52\n"
"mA 56,8,176,56\n"
"dT 46,5,23296\n"
"mA 60,8,180,60\n"
"s_waitcnt lgkmcnt(11)\n"
"mA 64,12,168,64\n"
"dT 48,6,18688\n"
"mA 68,12,172,68\n"
"s_cmp_le_u32 s60, 1\n"
"s_cselect_b32 s62, 2, 1\n"
"s_lshl_b32 s63, s62, 2\n"
"s_lshr_b32 s62, s62, 1\n"
"s_add_u32 s63, s63, s62\n"
"s_add_u32 s63, s63, 3\n"
"s_lshr_b32 s62, s63, 1\n"
"s_lshl_b32 s62, s62, 13\n"
"s_and_b32 s63, s63, 1\n"
"s_lshl_b32 s63, s63, 10\n"
"s_add_u32 s84, s90, s62\n"
"s_add_u32 s84, s84, s63\n"
"s_add_u32 s91, s84, 4096\n"
"buffer_load_dwordx4 a[72:75], v7, s[36:39], s84 offen sc1\n"
"mA 72,12,176,72\n"
"dT 50,6,23296\n"
"mA 76,12,180,76\n"
"mA 80,16,168,80\n"
"dT 52,5,18944\n"
"mA 84,16,172,84\n"
"mA 88,16,176,88\n"
"dT 54,5,23552\n"
"mA 92,16,180,92\n"
"buffer_load_dwordx4 a[76:79], v7, s[36:39], s84 offen offset:2048 sc1\n"
"mA 96,20,168,96\n"
"dT 56,6,18944\n"
"mA 100,20,172,100\n"
"mA 104,20,176,104\n"
"dT 58,6,23552\n"
"mA 108,20,180,108\n"
"s_waitcnt lgkmcnt(8)\n"
"mA 112,24,168,112\n"
"dT 60,5,19200\n"
"mA 116,24,172,116\n"
"buffer_load_dwordx4 a[80:83], v7, s[36:39], s91 offen sc1\n"
"mA 120,24,176,120\n"
"dT 62,5,23808\n"
"mA 124,24,180,124\n"
"mA 128,28,168,128\n"
"dT 64,6,19200\n"
"mA 132,28,172,132\n"
"mA 136,28,176,136\n"
"dT 66,6,23808\n"
"mA 140,28,180,140\n"
"buffer_load_dwordx4 a[84:87], v7, s[36:39], s91 offen offset:2048 sc1\n"
"mA 144,32,168,144\n"
"dT 68,8,18432\n"
"mA 148,32,172,148\n"
"mA 152,32,176,152\n"
"dT 70,8,23040\n"
"mA 156,32,180,156\n"
"s_waitcnt vmcnt(22)\n"
"mA 16,36,184,16\n"
"dT 0,5,27648\n"
"s_waitcnt vmcnt(21)\n"
"mA 20,36,188,20\n"
"s_cmp_le_u32 s60, 2\n"
"s_cselect_b32 s62, 3, 2\n"
"s_lshl_b32 s63, s62, 2\n"
"s_lshr_b32 s62, s62, 1\n"
"s_add_u32 s63, s63, s62\n"
"s_add_u32 s63, s63, 0\n"
"s_lshr_b32 s62, s63, 1\n"
"s_lshl_b32 s62, s62, 13\n"
"s_and_b32 s63, s63, 1\n"
"s_lshl_b32 s63, s63, 10\n"
"s_add_u32 s84, s90, s62\n"
"s_add_u32 s84, s84, s63\n"
"s_add_u32 s91, s84, 4096\n"
"buffer_load_dwordx4 a[88:91], v7, s[36:39], s84 offen sc1\n"
"s_waitcnt vmcnt(21)\n"
"mA 24,36,192,24\n"
"dT 2,5,32256\n"
"s_waitcnt vmcnt(20)\n"
"mA 28,36,196,28\n"
"mA 32,40,184,32\n"
"dT 4,6,27648\n"
"mA 36,40,188,36\n"
"mA 40,40,192,40\n"
"dT 6,6,32256\n"
"mA 44,40,196,44\n"
"buffer_load_dwordx4 a[92:95], v7, s[36:39], s84 offen offset:2048 sc1\n"
"s_waitcnt lgkmcnt(7)\n"
"mA 48,44,184,48\n"
"dT 8,5,27904\n"
"mA 52,44,188,52\n"
"mA 56,44,192,56\n"
"dT 10,5,32512\n"
"mA 60,44,196,60\n"
"mA 64,48,184,64\n"
"dT 12,6,27904\n"
"mA 68,48,188,68\n"
"buffer_load_dwordx4 a[96:99], v7, s[36:39], s91 offen sc1\n"
"mA 72,48,192,72\n"
"dT 14,6,32512\n"
"mA 76,48,196,76\n"
"mA 80,52,184,80\n"
"dT 16,5,28160\n"
"mA 84,52,188,84\n"
"mA 88,52,192,88\n"
"dT 18,5,32768\n"
"mA 92,52,196,92\n"
"buffer_load_dwordx4 a[100:103], v7, s[36:39], s91 offen offset:2048 sc1\n"
"mA 96,56,184,96\n"
"dT 20,6,28160\n"
"mA 100,56,188,100\n"
"mA 104,56,192,104\n"
"dT 22,6,32768\n"
"mA 108,56,196,108\n"
"mA 112,60,184,112\n"
"dT 24,5,28416\n"
"mA 116,60,188,116\n"
"s_cmp_le_u32 s60, 2\n"
"s_cselect_b32 s62, 3, 2\n"
"s_lshl_b32 s63, s62, 2\n"
"s_lshr_b32 s62, s62, 1\n"
"s_add_u32 s63, s63, s62\n"
"s_add_u32 s63, s63, 1\n"
"s_lshr_b32 s62, s63, 1\n"
"s_lshl_b32 s62, s62, 13\n"
"s_and_b32 s63, s63, 1\n"
"s_lshl_b32 s63, s63, 10\n"
"s_add_u32 s84, s90, s62\n"
"s_add_u32 s84, s84, s63\n"
"s_add_u32 s91, s84, 4096\n"
"buffer_load_dwordx4 a[104:107], v7, s[36:39], s84 offen sc1\n"
"mA 120,60,192,120\n"
"dT 26,5,33024\n"
"mA 124,60,196,124\n"
"s_waitcnt lgkmcnt(8)\n"
"mA 128,64,184,128\n"
"dT 28,6,28416\n"
"mA 132,64,188,132\n"
"mA 136,64,192,136\n"
"dT 30,6,33024\n"
"mA 140,64,196,140\n"
"buffer_load_dwordx4 a[108:111], v7, s[36:39], s84 offen offset:2048 sc1\n"
"mA 144,68,184,144\n"
"dT 32,8,27648\n"
"mA 148,68,188,148\n"
"mA 152,68,192,152\n"
"dT 34,8,32256\n"
"mA 156,68,196,156\n"
"s_waitcnt vmcnt(21)\n"
"mA 16,0,200,16\n"
"dT 36,5,36864\n"
"s_waitcnt vmcnt(20)\n"
"mA 20,0,204,20\n"
"buffer_load_dwordx4 a[112:115], v7, s[36:39], s91 offen sc1\n"
"s_waitcnt vmcnt(20)\n"
"mA 24,0,208,24\n"
"dT 38,5,41472\n"
"s_waitcnt vmcnt(19)\n"
"mA 28,0,212,28\n"
"mA 32,4,200,32\n"
"dT 40,6,36864\n"
"mA 36,4,204,36\n"
"mA 40,4,208,40\n"
"dT 42,6,41472\n"
"mA 44,4,212,44\n"
"buffer_load_dwordx4 a[116:119], v7, s[36:39], s91 offen offset:2048 sc1\n"
"mA 48,8,200,48\n"
"dT 44,5,37120\n"
"mA 52,8,204,52\n"
"mA 56,8,208,56\n"
"dT 46,5,41728\n"
"mA 60,8,212,60\n"
"s_waitcnt lgkmcnt(11)\n"
"mA 64,12,200,64\n"
"dT 48,6,37120\n"
"mA 68,12,204,68\n"
"s_cmp_le_u32 s60, 2\n"
"s_cselect_b32 s62, 3, 2\n"
"s_lshl_b32 s63, s62, 2\n"
"s_lshr_b32 s62, s62, 1\n"
"s_add_u32 s63, s63, s62\n"
"s_add_u32 s63, s63, 2\n"
"s_lshr_b32 s62, s63, 1\n"
"s_lshl_b32 s62, s62, 13\n"
"s_and_b32 s63, s63, 1\n"
"s_lshl_b32 s63, s63, 10\n"
"s_add_u32 s84, s90, s62\n"
"s_add_u32 s84, s84, s63\n"
"s_add_u32 s91, s84, 4096\n"
"buffer_load_dwordx4 a[120:123], v7, s[36:39], s84 offen sc1\n"
"mA 72,12,208,72\n"
"dT 50,6,41728\n"
"mA 76,12,212,76\n"
"mA 80,16,200,80\n"
"dT 52,5,37376\n"
"mA 84,16,204,84\n"
"mA 88,16,208,88\n"
"dT 54,5,41984\n"
"mA 92,16,212,92\n"
"buffer_load_dwordx4 a[124:127], v7, s[36:39], s84 offen offset:2048 sc1\n"
"mA 96,20,200,96\n"
"dT 56,6,37376\n"
"mA 100,20,204,100\n"
"mA 104,20,208,104\n"
"dT 58,6,41984\n"
"mA 108,20,212,108\n"
"s_waitcnt lgkmcnt(8)\n"
"mA 112,24,200,112\n"
"dT 60,5,37632\n"
"mA 116,24,204,116\n"
"buffer_load_dwordx4 a[128:131], v7, s[36:39], s91 offen sc1\n"
"mA 120,24,208,120\n"
"dT 62,5,42240\n"
"mA 124,24,212,124\n"
"mA 128,28,200,128\n"
"dT 64,6,37632\n"
"mA 132,28,204,132\n"
"mA 136,28,208,136\n"
"dT 66,6,42240\n"
"mA 140,28,212,140\n"
"buffer_load_dwordx4 a[132:135], v7, s[36:39], s91 offen offset:2048 sc1\n"
"mA 144,32,200,144\n"
"dT 68,8,36864\n"
"mA 148,32,204,148\n"
"mA 152,32,208,152\n"
"dT 70,8,41472\n"
"mA 156,32,212,156\n"
"s_waitcnt vmcnt(23)\n"
"mA 16,36,216,16\n"
"dT 0,5,46080\n"
"s_waitcnt vmcnt(22)\n"
"mA 20,36,220,20\n"
"s_cmp_le_u32 s60, 2\n"
"s_cselect_b32 s62, 3, 2\n"
"s_lshl_b32 s63, s62, 2\n"
"s_lshr_b32 s62, s62, 1\n"
"s_add_u32 s63, s63, s62\n"
"s_add_u32 s63, s63, 3\n"
"s_lshr_b32 s62, s63, 1\n"
"s_lshl_b32 s62, s62, 13\n"
"s_and_b32 s63, s63, 1\n"
"s_lshl_b32 s63, s63, 10\n"
"s_add_u32 s84, s90, s62\n"
"s_add_u32 s84, s84, s63\n"
"s_add_u32 s91, s84, 4096\n"
"buffer_load_dwordx4 a[136:139], v7, s[36:39], s84 offen sc1\n"
"s_waitcnt vmcnt(22)\n"
"mA 24,36,224,24\n"
"dT 2,5,50688\n"
"s_waitcnt vmcnt(21)\n"
"mA 28,36,228,28\n"
"mA 32,40,216,32\n"
"dT 4,6,46080\n"
"mA 36,40,220,36\n"
"mA 40,40,224,40\n"
"dT 6,6,50688\n"
"mA 44,40,228,44\n"
"buffer_load_dwordx4 a[140:143], v7, s[36:39], s84 offen offset:2048 sc1\n"
"s_waitcnt lgkmcnt(7)\n"
"mA 48,44,216,48\n"
"dT 8,5,46336\n"
"mA 52,44,220,52\n"
"mA 56,44,224,56\n"
"dT 10,5,50944\n"
"mA 60,44,228,60\n"
"mA 64,48,216,64\n"
"dT 12,6,46336\n"
"mA 68,48,220,68\n"
"buffer_load_dwordx4 a[144:147], v7, s[36:39], s91 offen sc1\n"
"mA 72,48,224,72\n"
"dT 14,6,50944\n"
"mA 76,48,228,76\n"
"mA 80,52,216,80\n"
"dT 16,5,46592\n"
"mA 84,52,220,84\n"
"mA 88,52,224,88\n"
"dT 18,5,51200\n"
"mA 92,52,228,92\n"
"buffer_load_dwordx4 a[148:151], v7, s[36:39], s91 offen offset:2048 sc1\n"
"mA 96,56,216,96\n"
"dT 20,6,46592\n"
"mA 100,56,220,100\n"
"mA 104,56,224,104\n"
"dT 22,6,51200\n"
"mA 108,56,228,108\n"
"mA 112,60,216,112\n"
"dT 24,5,46848\n"
"mA 116,60,220,116\n"
"s_add_u32 s84, s90, 32768\n"
"s_add_u32 s91, s84, 4096\n"
"buffer_load_dwordx4 a[152:155], v7, s[36:39], s84 offen sc1\n"
"mA 120,60,224,120\n"
"dT 26,5,51456\n"
"mA 124,60,228,124\n"
"s_waitcnt lgkmcnt(11)\n"
"mA 128,64,216,128\n"
"dT 28,6,46848\n"
"mA 132,64,220,132\n"
"mA 136,64,224,136\n"
"dT 30,6,51456\n"
"mA 140,64,228,140\n"
"buffer_load_dwordx4 a[156:159], v7, s[36:39], s84 offen offset:2048 sc1\n"
"mA 144,68,216,144\n"
"dT 32,8,46080\n"
"mA 148,68,220,148\n"
"mA 152,68,224,152\n"
"dT 34,8,50688\n"
"mA 156,68,228,156\n"
"s_waitcnt vmcnt(25)\n"
"mA 16,0,232,16\n"
"ds_read_b64_tr_b16 a[36:37], v9\n"
"s_waitcnt vmcnt(24)\n"
"mA 20,0,236,20\n"
"buffer_load_dwordx4 a[160:163], v7, s[36:39], s91 offen sc1\n"
"s_waitcnt vmcnt(24)\n"
"mA 24,0,240,24\n"
"dT 38,9,4608\n"
"s_waitcnt vmcnt(23)\n"
"mA 28,0,244,28\n"
"s_waitcnt lgkmcnt(10)\n"
"mA 32,4,232,32\n"
"ds_read_b64_tr_b16 a[40:41], v10\n"
"mA 36,4,236,36\n"
"mA 40,4,240,40\n"
"dT 42,10,4608\n"
"mA 44,4,244,44\n"
"buffer_load_dwordx4 a[164:167], v7, s[36:39], s91 offen offset:2048 sc1\n"
"mA 48,8,232,48\n"
"dT 44,9,256\n"
"mA 52,8,236,52\n"
"mA 56,8,240,56\n"
"dT 46,9,4864\n"
"mA 60,8,244,60\n"
"mA 64,12,232,64\n"
"dT 48,10,256\n"
"mA 68,12,236,68\n"
"s_add_u32 s84, s90, 66560\n"
"s_add_u32 s91, s84, 4096\n"
"buffer_load_dwordx4 a[168:171], v7, s[36:39], s84 offen sc1\n"
"mA 72,12,240,72\n"
"dT 50,10,4864\n"
"mA 76,12,244,76\n"
"mA 80,16,232,80\n"
"dT 52,9,512\n"
"mA 84,16,236,84\n"
"mA 88,16,240,88\n"
"dT 54,9,5120\n"
"mA 92,16,244,92\n"
"buffer_load_dwordx4 a[172:175], v7, s[36:39], s84 offen offset:2048 sc1\n"
"s_waitcnt lgkmcnt(10)\n"
"mA 96,20,232,96\n"
"dT 56,10,512\n"
"mA 100,20,236,100\n"
"mA 104,20,240,104\n"
"dT 58,10,5120\n"
"mA 108,20,244,108\n"
"mA 112,24,232,112\n"
"dT 60,9,768\n"
"mA 116,24,236,116\n"
"buffer_load_dwordx4 a[176:179], v7, s[36:39], s91 offen sc1\n"
"mA 120,24,240,120\n"
"dT 62,9,5376\n"
"mA 124,24,244,124\n"
"mA 128,28,232,128\n"
"dT 64,10,768\n"
"mA 132,28,236,132\n"
"mA 136,28,240,136\n"
"dT 66,10,5376\n"
"mA 140,28,244,140\n"
"buffer_load_dwordx4 a[180:183], v7, s[36:39], s91 offen offset:2048 sc1\n"
"mA 144,32,232,144\n"
"ds_read_b64_tr_b16 a[68:69], v11\n"
"mA 148,32,236,148\n"
"mA 152,32,240,152\n"
"dT 70,11,4608\n"
"mA 156,32,244,156\n"
"s_barrier\n"
"s_waitcnt lgkmcnt(0)\n"
"s_barrier\n"
"s_waitcnt vmcnt(27)\n"
"mA 16,36,72,16\n"
"dT 0,9,36864\n"
"s_waitcnt vmcnt(26)\n"
"mA 20,36,76,20\n"
"s_waitcnt vmcnt(25)\n"
"mA 24,36,80,24\n"
"dT 2,9,41472\n"
"s_waitcnt vmcnt(24)\n"
"mA 28,36,84,28\n"
"mA 32,40,72,32\n"
"dT 4,9,9216\n"
"mA 36,40,76,36\n"
"mA 40,40,80,40\n"
"dT 6,9,13824\n"
"mA 44,40,84,44\n"
"mA 48,44,72,48\n"
"dT 8,9,18432\n"
"mA 52,44,76,52\n"
"mA 56,44,80,56\n"
"dT 10,9,23040\n"
"mA 60,44,84,60\n"
"mA 64,48,72,64\n"
"dT 12,9,27648\n"
"mA 68,48,76,68\n"
"mA 72,48,80,72\n"
"dT 14,9,32256\n"
"mA 76,48,84,76\n"
"mA 80,52,72,80\n"
"ds_read_b64_tr_b16 a[16:17], v2\n"
"mA 84,52,76,84\n"
"mA 88,52,80,88\n"
"dT 18,2,4608\n"
"mA 92,52,84,92\n"
"mA 96,56,72,96\n"
"dT 20,2,9216\n"
"mA 100,56,76,100\n"
"mA 104,56,80,104\n"
"dT 22,2,13824\n"
"mA 108,56,84,108\n"
"mA 112,60,72,112\n"
"mA 116,60,76,116\n"
"mA 120,60,80,120\n"
"mA 124,60,84,124\n"
"mA 128,64,72,128\n"
"mA 132,64,76,132\n"
"mA 136,64,80,136\n"
"mA 140,64,84,140\n"
"mA 144,68,72,144\n"
"mA 148,68,76,148\n"
"mA 152,68,80,152\n"
"mA 156,68,84,156\n"
"s_waitcnt vmcnt(23) lgkmcnt(9)\n"
"mA 16,0,88,16\n"
"dT 36,10,36864\n"
"s_waitcnt vmcnt(19) lgkmcnt(9)\n"
"mA 16,4,104,16\n"
"s_waitcnt vmcnt(15) lgkmcnt(7)\n"
"mA 16,8,120,16\n"
"dT 38,10,41472\n"
"s_waitcnt vmcnt(11) lgkmcnt(6)\n"
"mA 16,12,136,16\n"
"s_waitcnt vmcnt(7) lgkmcnt(4)\n"
"mA 16,16,152,16\n"
"dT 40,10,9216\n"
"s_waitcnt vmcnt(3) lgkmcnt(3)\n"
"mA 16,20,168,16\n"
"mA 20,0,92,20\n"
"dT 42,10,13824\n"
"mA 20,4,108,20\n"
"mA 20,8,124,20\n"
"dT 44,10,18432\n"
"mA 20,12,140,20\n"
"mA 20,16,156,20\n"
"dT 46,10,23040\n"
"s_waitcnt vmcnt(2)\n"
"mA 20,20,172,20\n"
"mA 24,0,96,24\n"
"dT 48,10,27648\n"
"mA 24,4,112,24\n"
"mA 24,8,128,24\n"
"dT 50,10,32256\n"
"mA 24,12,144,24\n"
"mA 24,16,160,24\n"
"ds_read_b64_tr_b16 a[52:53], v3\n"
"s_waitcnt vmcnt(1)\n"
"mA 24,20,176,24\n"
"mA 28,0,100,28\n"
"dT 54,3,4608\n"
"mA 28,4,116,28\n"
"mA 28,8,132,28\n"
"dT 56,3,9216\n"
"mA 28,12,148,28\n"
"mA 28,16,164,28\n"
"dT 58,3,13824\n"
"s_waitcnt vmcnt(0)\n"
"mA 28,20,180,28\n"
"s_waitcnt lgkmcnt(10)\n"
"mA 32,36,88,32\n"
"dT 0,9,37120\n"
"s_waitcnt lgkmcnt(9)\n"
"mA 32,40,104,32\n"
"cP 16,16,17\n"
"s_waitcnt lgkmcnt(7)\n"
"mA 32,44,120,32\n"
"dT 2,9,41728\n"
"cP 17,18,19\n"
"s_waitcnt lgkmcnt(6)\n"
"mA 32,48,136,32\n"
"cP 20,20,21\n"
"s_waitcnt lgkmcnt(4)\n"
"mA 32,52,152,32\n"
"dT 4,9,9472\n"
"cP 21,22,23\n"
"s_waitcnt lgkmcnt(3)\n"
"mA 32,56,168,32\n"
"cP 24,24,25\n"
"mA 36,36,92,36\n"
"dT 6,9,14080\n"
"cP 25,26,27\n"
"mA 36,40,108,36\n"
"cP 28,28,29\n"
"mA 36,44,124,36\n"
"dT 8,9,18688\n"
"cP 29,30,31\n"
"mA 36,48,140,36\n"
"mA 36,52,156,36\n"
"dT 10,9,23296\n"
"mA 36,56,172,36\n"
"mA 40,36,96,40\n"
"dT 12,9,27904\n"
"mA 40,40,112,40\n"
"mA 40,44,128,40\n"
"dT 14,9,32512\n"
"mA 40,48,144,40\n"
"mA 40,52,160,40\n"
"dT 16,2,256\n"
"mA 40,56,176,40\n"
"mA 44,36,100,44\n"
"dT 18,2,4864\n"
"mA 44,40,116,44\n"
"mA 44,44,132,44\n"
"dT 20,2,9472\n"
"mA 44,48,148,44\n"
"mA 44,52,164,44\n"
"dT 22,2,14080\n"
"mA 44,56,180,44\n"
"s_waitcnt lgkmcnt(10)\n"
"mA 48,0,88,48\n"
"dT 36,10,37120\n"
"s_waitcnt lgkmcnt(9)\n"
"mA 48,4,104,48\n"
"cP 18,32,33\n"
"s_waitcnt lgkmcnt(7)\n"
"mA 48,8,120,48\n"
"dT 38,10,41728\n"
"cP 19,34,35\n"
"s_waitcnt lgkmcnt(6)\n"
"mA 48,12,136,48\n"
"buffer_store_dwordx4 v[16:19], v7, s[40:43], s73 offen sc1\n"
"s_waitcnt lgkmcnt(4)\n"
"mA 48,16,152,48\n"
"dT 40,10,9472\n"
"cP 22,36,37\n"
"s_waitcnt lgkmcnt(3)\n"
"mA 48,20,168,48\n"
"cP 23,38,39\n"
"mA 52,0,92,52\n"
"dT 42,10,14080\n"
"buffer_store_dwordx4 v[20:23], v7, s[40:43], s73 offen offset:2048 sc1\n"
"mA 52,4,108,52\n"
"cP 26,40,41\n"
"mA 52,8,124,52\n"
"dT 44,10,18688\n"
"cP 27,42,43\n"
"mA 52,12,140,52\n"
"buffer_store_dwordx4 v[24:27], v7, s[40:43], s74 offen sc1\n"
"mA 52,16,156,52\n"
"dT 46,10,23296\n"
"cP 30,44,45\n"
"mA 52,20,172,52\n"
"cP 31,46,47\n"
"mA 56,0,96,56\n"
"dT 48,10,27904\n"
"buffer_store_dwordx4 v[28:31], v7, s[40:43], s74 offen offset:2048 sc1\n"
"mA 56,4,112,56\n"
"mA 56,8,128,56\n"
"dT 50,10,32512\n"
"mA 56,12,144,56\n"
"mA 56,16,160,56\n"
"dT 52,3,256\n"
"mA 56,20,176,56\n"
"mA 60,0,100,60\n"
"dT 54,3,4864\n"
"mA 60,4,116,60\n"
"mA 60,8,132,60\n"
"dT 56,3,9472\n"
"mA 60,12,148,60\n"
"mA 60,16,164,60\n"
"dT 58,3,14080\n"
"mA 60,20,180,60\n"
"s_waitcnt lgkmcnt(10)\n"
"mA 64,36,88,64\n"
"dT 0,9,37376\n"
"s_waitcnt lgkmcnt(9)\n"
"mA 64,40,104,64\n"
"cP 48,48,49\n"
"s_waitcnt lgkmcnt(7)\n"
"mA 64,44,120,64\n"
"dT 2,9,41984\n"
"cP 49,50,51\n"
"s_waitcnt lgkmcnt(6)\n"
"mA 64,48,136,64\n"
"cP 52,52,53\n"
"s_waitcnt lgkmcnt(4)\n"
"mA 64,52,152,64\n"
"dT 4,9,9728\n"
"cP 53,54,55\n"
"s_waitcnt lgkmcnt(3)\n"
"mA 64,56,168,64\n"
"cP 56,56,57\n"
"mA 68,36,92,68\n"
"dT 6,9,14336\n"
"cP 57,58,59\n"
"mA 68,40,108,68\n"
"cP 60,60,61\n"
"mA 68,44,124,68\n"
"dT 8,9,18944\n"
"cP 61,62,63\n"
"mA 68,48,140,68\n"
"mA 68,52,156,68\n"
"dT 10,9,23552\n"
"mA 68,56,172,68\n"
"mA 72,36,96,72\n"
"dT 12,9,28160\n"
"mA 72,40,112,72\n"
"mA 72,44,128,72\n"
"dT 14,9,32768\n"
"mA 72,48,144,72\n"
"mA 72,52,160,72\n"
"dT 16,2,512\n"
"mA 72,56,176,72\n"
"mA 76,36,100,76\n"
"dT 18,2,5120\n"
"mA 76,40,116,76\n"
"mA 76,44,132,76\n"
"dT 20,2,9728\n"
"mA 76,48,148,76\n"
"mA 76,52,164,76\n"
"dT 22,2,14336\n"
"mA 76,56,180,76\n"
"s_waitcnt lgkmcnt(10)\n"
"mA 80,0,88,80\n"
"dT 36,10,37376\n"
"s_waitcnt lgkmcnt(9)\n"
"mA 80,4,104,80\n"
"cP 50,64,65\n"
"s_waitcnt lgkmcnt(7)\n"
"mA 80,8,120,80\n"
"dT 38,10,41984\n"
"cP 51,66,67\n"
"s_waitcnt lgkmcnt(6)\n"
"mA 80,12,136,80\n"
"buffer_store_dwordx4 v[48:51], v7, s[40:43], s75 offen sc1\n"
"s_waitcnt lgkmcnt(4)\n"
"mA 80,16,152,80\n"
"dT 40,10,9728\n"
"cP 54,68,69\n"
"s_waitcnt lgkmcnt(3)\n"
"mA 80,20,168,80\n"
"cP 55,70,71\n"
"mA 84,0,92,84\n"
"dT 42,10,14336\n"
"buffer_store_dwordx4 v[52:55], v7, s[40:43], s75 offen offset:2048 sc1\n"
"mA 84,4,108,84\n"
"cP 58,72,73\n"
"mA 84,8,124,84\n"
"dT 44,10,18944\n"
"cP 59,74,75\n"
"mA 84,12,140,84\n"
"buffer_store_dwordx4 v[56:59], v7, s[40:43], s76 offen sc1\n"
"mA 84,16,156,84\n"
"dT 46,10,23552\n"
"cP 62,76,77\n"
"mA 84,20,172,84\n"
"cP 63,78,79\n"
"mA 88,0,96,88\n"
"dT 48,10,28160\n"
"buffer_store_dwordx4 v[60:63], v7, s[40:43], s76 offen offset:2048 sc1\n"
"mA 88,4,112,88\n"
"mA 88,8,128,88\n"
"dT 50,10,32768\n"
"mA 88,12,144,88\n"
"mA 88,16,160,88\n"
"dT 52,3,512\n"
"mA 88,20,176,88\n"
"mA 92,0,100,92\n"
"dT 54,3,5120\n"
"mA 92,4,116,92\n"
"mA 92,8,132,92\n"
"dT 56,3,9728\n"
"mA 92,12,148,92\n"
"mA 92,16,164,92\n"
"dT 58,3,14336\n"
"mA 92,20,180,92\n"
"s_waitcnt lgkmcnt(10)\n"
"mA 96,36,88,96\n"
"dT 0,9,37632\n"
"s_waitcnt lgkmcnt(9)\n"
"mA 96,40,104,96\n"
"cP 80,80,81\n"
"s_waitcnt lgkmcnt(7)\n"
"mA 96,44,120,96\n"
"dT 2,9,42240\n"
"cP 81,82,83\n"
"s_waitcnt lgkmcnt(6)\n"
"mA 96,48,136,96\n"
"cP 84,84,85\n"
"s_waitcnt lgkmcnt(4)\n"
"mA 96,52,152,96\n"
"dT 4,9,9984\n"
"cP 85,86,87\n"
"s_waitcnt lgkmcnt(3)\n"
"mA 96,56,168,96\n"
"cP 88,88,89\n"
"mA 100,36,92,100\n"
"dT 6,9,14592\n"
"cP 89,90,91\n"
"mA 100,40,108,100\n"
"cP 92,92,93\n"
"mA 100,44,124,100\n"
"dT 8,9,19200\n"
"cP 93,94,95\n"
"mA 100,48,140,100\n"
"mA 100,52,156,100\n"
"dT 10,9,23808\n"
"mA 100,56,172,100\n"
"mA 104,36,96,104\n"
"dT 12,9,28416\n"
"mA 104,40,112,104\n"
"mA 104,44,128,104\n"
"dT 14,9,33024\n"
"mA 104,48,144,104\n"
"mA 104,52,160,104\n"
"dT 16,2,768\n"
"mA 104,56,176,104\n"
"mA 108,36,100,108\n"
"dT 18,2,5376\n"
"mA 108,40,116,108\n"
"mA 108,44,132,108\n"
"dT 20,2,9984\n"
"mA 108,48,148,108\n"
"mA 108,52,164,108\n"
"dT 22,2,14592\n"
"mA 108,56,180,108\n"
"s_waitcnt lgkmcnt(10)\n"
"mA 112,0,88,112\n"
"dT 36,10,37632\n"
"s_waitcnt lgkmcnt(9)\n"
"mA 112,4,104,112\n"
"cP 82,96,97\n"
"s_waitcnt lgkmcnt(7)\n"
"mA 112,8,120,112\n"
"dT 38,10,42240\n"
"cP 83,98,99\n"
"s_waitcnt lgkmcnt(6)\n"
"mA 112,12,136,112\n"
"buffer_store_dwordx4 v[80:83], v7, s[40:43], s77 offen sc1\n"
"s_waitcnt lgkmcnt(4)\n"
"mA 112,16,152,112\n"
"dT 40,10,9984\n"
"cP 86,100,101\n"
"s_waitcnt lgkmcnt(3)\n"
"mA 112,20,168,112\n"
"cP 87,102,103\n"
"mA 116,0,92,116\n"
"dT 42,10,14592\n"
"buffer_store_dwordx4 v[84:87], v7, s[40:43], s77 offen offset:2048 sc1\n"
"mA 116,4,108,116\n"
"cP 90,104,105\n"
"mA 116,8,124,116\n"
"dT 44,10,19200\n"
"cP 91,106,107\n"
"mA 116,12,140,116\n"
"buffer_store_dwordx4 v[88:91], v7, s[40:43], s78 offen sc1\n"
"mA 116,16,156,116\n"
"dT 46,10,23808\n"
"cP 94,108,109\n"
"mA 116,20,172,116\n"
"cP 95,110,111\n"
"mA 120,0,96,120\n"
"dT 48,10,28416\n"
"buffer_store_dwordx4 v[92:95], v7, s[40:43], s78 offen offset:2048 sc1\n"
"mA 120,4,112,120\n"
"mA 120,8,128,120\n"
"dT 50,10,33024\n"
"mA 120,12,144,120\n"
"mA 120,16,160,120\n"
"dT 52,3,768\n"
"mA 120,20,176,120\n"
"mA 124,0,100,124\n"
"dT 54,3,5376\n"
"mA 124,4,116,124\n"
"mA 124,8,132,124\n"
"dT 56,3,9984\n"
"mA 124,12,148,124\n"
"mA 124,16,164,124\n"
"dT 58,3,14592\n"
"mA 124,20,180,124\n"
"s_waitcnt lgkmcnt(10)\n"
"mA 128,36,88,128\n"
"dT 0,11,36864\n"
"s_waitcnt lgkmcnt(9)\n"
"mA 128,40,104,128\n"
"cP 112,112,113\n"
"s_waitcnt lgkmcnt(7)\n"
"mA 128,44,120,128\n"
"dT 2,11,41472\n"
"cP 113,114,115\n"
"s_waitcnt lgkmcnt(6)\n"
"mA 128,48,136,128\n"
"cP 116,116,117\n"
"s_waitcnt lgkmcnt(4)\n"
"mA 128,52,152,128\n"
"dT 4,11,9216\n"
"cP 117,118,119\n"
"s_waitcnt lgkmcnt(3)\n"
"mA 128,56,168,128\n"
"cP 120,120,121\n"
"mA 132,36,92,132\n"
"dT 6,11,13824\n"
"cP 121,122,123\n"
"mA 132,40,108,132\n"
"cP 124,124,125\n"
"mA 132,44,124,132\n"
"dT 8,11,18432\n"
"cP 125,126,127\n"
"mA 132,48,140,132\n"
"mA 132,52,156,132\n"
"dT 10,11,23040\n"
"mA 132,56,172,132\n"
"mA 136,36,96,136\n"
"dT 12,11,27648\n"
"mA 136,40,112,136\n"
"mA 136,44,128,136\n"
"dT 14,11,32256\n"
"mA 136,48,144,136\n"
"mA 136,52,160,136\n"
"ds_read_b64_tr_b16 a[16:17], v4\n"
"mA 136,56,176,136\n"
"mA 140,36,100,140\n"
"dT 18,4,4608\n"
"mA 140,40,116,140\n"
"mA 140,44,132,140\n"
"dT 20,4,9216\n"
"mA 140,48,148,140\n"
"mA 140,52,164,140\n"
"dT 22,4,13824\n"
"mA 140,56,180,140\n"
"s_waitcnt lgkmcnt(10)\n"
"mA 144,0,88,144\n"
"s_waitcnt lgkmcnt(8)\n"
"mA 144,4,104,144\n"
"cP 114,128,129\n"
"s_waitcnt lgkmcnt(6)\n"
"mA 144,8,120,144\n"
"cP 115,130,131\n"
"s_waitcnt lgkmcnt(4)\n"
"mA 144,12,136,144\n"
"buffer_store_dwordx4 v[112:115], v7, s[40:43], s79 offen sc1\n"
"s_waitcnt lgkmcnt(2)\n"
"mA 144,16,152,144\n"
"cP 118,132,133\n"
"s_waitcnt lgkmcnt(0)\n"
"mA 144,20,168,144\n"
"cP 119,134,135\n"
"mA 148,0,92,148\n"
"buffer_store_dwordx4 v[116:119], v7, s[40:43], s79 offen offset:2048 sc1\n"
"mA 148,4,108,148\n"
"cP 122,136,137\n"
"mA 148,8,124,148\n"
"cP 123,138,139\n"
"mA 148,12,140,148\n"
"buffer_store_dwordx4 v[120:123], v7, s[40:43], s80 offen sc1\n"
"mA 148,16,156,148\n"
"cP 126,140,141\n"
"mA 148,20,172,148\n"
"cP 127,142,143\n"
"mA 152,0,96,152\n"
"buffer_store_dwordx4 v[124:127], v7, s[40:43], s80 offen offset:2048 sc1\n"
"mA 152,4,112,152\n"
"mA 152,8,128,152\n"
"mA 152,12,144,152\n"
"mA 152,16,160,152\n"
"mA 152,20,176,152\n"
"mA 156,0,100,156\n"
"mA 156,4,116,156\n"
"mA 156,8,132,156\n"
"mA 156,12,148,156\n"
"mA 156,16,164,156\n"
"mA 156,20,180,156\n"
"cP 144,144,145\n"
"cP 145,146,147\n"
"buffer_store_dwordx2 v[144:145], v12, s[40:43], s81 offen sc1\n"
"cP 148,148,149\n"
"cP 149,150,151\n"
"buffer_store_dwordx2 v[148:149], v12, s[40:43], s81 offen offset:2048 sc1\n"
"cP 152,152,153\n"
"cP 153,154,155\n"
"buffer_store_dwordx2 v[152:153], v12, s[40:43], s83 offen sc1\n"
"cP 156,156,157\n"
"cP 157,158,159\n"
"buffer_store_dwordx2 v[156:157], v12, s[40:43], s83 offen offset:2048 sc1\n"
"s_add_u32 s62, s57, 158228\n"
"v_mov_b32 v13, s62\n"
"v_mov_b32 v14, 0\n"
"ds_write_b32 v13, v14\n"
"s_lshl_b32 s62, s58, 8\n"
"s_add_u32 s62, s62, 75776\n"
"s_add_u32 s66, %[wslo], s62\n"
"s_addc_u32 s67, %[wshi], 0\n"
"s_waitcnt vmcnt(0) lgkmcnt(0)\n"
"s_barrier\n"
"s_cmp_eq_u32 s56, 0\n"
"s_cbranch_scc0 L_sg17_%=\n"
"s_lshl_b32 s62, s59, 2\n"
"s_add_u32 s62, s62, s60\n"
"s_lshl_b32 s62, s62, 3\n"
"v_mov_b32 v234, s62\n"
"v_mov_b32 v232, s98\n"
"v_mov_b32 v233, s99\n"
"s_mov_b64 exec, 1\n"
"global_store_dwordx2 v234, v[232:233], s[66:67] sc1\n"
"s_mov_b64 exec, -1\n"
"L_sg17_%=:\n"
"v_and_b32 v36, 7, v1\n"
"v_lshrrev_b32 v37, 4, v1\n"
"v_bfe_u32 v38, v1, 3, 1\n"
"v_lshl_add_u32 v16, v37, 4, v36\n"
"v_lshlrev_b32 v16, 4, v16\n"
"v_lshl_add_u32 v16, v38, 10, v16\n"
"s_lshr_b32 s62, s61, 3\n"
"s_mul_i32 s93, s62, 73728\n"
"s_bfe_u32 s62, s61, 0x20001\n"
"s_lshl_b32 s62, s62, 11\n"
"s_add_u32 s93, s93, s62\n"
"s_and_b32 s62, s61, 1\n"
"s_lshl_b32 s62, s62, 7\n"
"s_add_u32 s93, s93, s62\n"
"s_lshl_b32 s62, s56, 13\n"
"s_add_u32 s93, s93, s62\n"
"s_mul_i32 s63, s58, 294912\n"
"s_add_u32 s44, %[wslo], 26214400\n"
"s_addc_u32 s45, %[wshi], 0\n"
"s_add_u32 s44, s44, s63\n"
"s_addc_u32 s45, s45, 0\n"
"s_and_b32 s45, s45, 0xffff\n"
"s_mov_b32 s46, 16809984\n"
"s_mov_b32 s47, 0x20000\n"
"s_add_u32 s62, s57, 158228\n"
"v_mov_b32 v13, s62\n"
"s_cmp_eq_u32 s56, 3\n"
"s_cbranch_scc1 L_r318_%=\n"
"s_lshl_b32 s62, s56, 1\n"
"v_add_u32 v39, s62, v38\n"
"v_lshlrev_b32 v17, 7, v39\n"
"v_lshl_add_u32 v17, v37, 5, v17\n"
"s_add_u32 s40, %[wslo], 0\n"
"s_addc_u32 s41, %[wshi], 0\n"
"s_and_b32 s41, s41, 0xffff\n"
"s_mov_b32 s42, 13824\n"
"s_mov_b32 s43, 0x20000\n"
"s_mov_b32 s62, 11520\n"
"buffer_load_dwordx4 v[164:167], v17, s[40:43], s62 offen sc1\n"
"buffer_load_dwordx4 v[168:171], v17, s[40:43], s62 offen offset:16 sc1\n"
"buffer_load_dwordx4 v[172:175], v17, s[40:43], s62 offen offset:768 sc1\n"
"buffer_load_dwordx4 v[176:179], v17, s[40:43], s62 offen offset:784 sc1\n"
"buffer_load_dwordx4 v[180:183], v17, s[40:43], s62 offen offset:1536 sc1\n"
"buffer_load_dwordx4 v[184:187], v17, s[40:43], s62 offen offset:1552 sc1\n"
"s_lshl_b32 s62, s58, 5\n"
"s_add_u32 s62, s62, s61\n"
"s_mul_i32 s62, s62, 9216\n"
"s_add_u32 s36, %[outlo], 0\n"
"s_addc_u32 s37, %[outhi], 0\n"
"s_add_u32 s36, s36, s62\n"
"s_addc_u32 s37, s37, 0\n"
"s_and_b32 s37, s37, 0xffff\n"
"s_mov_b32 s38, 9216\n"
"s_mov_b32 s39, 0x20000\n"
"v_bfe_u32 v32, v1, 2, 2\n"
"v_cmp_eq_u32 vcc, v32, v37\n"
"v_and_b32 v33, 1, v1\n"
"v_lshlrev_b32 v33, 4, v33\n"
"v_mov_b32 v34, 0x3f80\n"
"v_lshlrev_b32 v34, v33, v34\n"
"v_cndmask_b32 v34, 0, v34, vcc\n"
"v_and_b32 v32, 2, v1\n"
"v_cmp_eq_u32 vcc, 0, v32\n"
"s_nop 1\n"
"v_cndmask_b32 v22, 0, v34, vcc\n"
"v_sub_u32 v23, v34, v22\n"
"v_mov_b32 v20, 0\n"
"v_mov_b32 v21, 0\n"
"v_mov_b32 v24, 0\n"
"v_mov_b32 v25, 0\n"
"v_mul_u32_u24 v36, 1152, v36\n"
"v_lshl_add_u32 v36, v37, 3, v36\n"
"v_add_u32 v32, 0, v39\n"
"v_cmp_lt_u32 vcc, 8, v32\n"
"v_mov_b32 v34, 9\n"
"s_nop 0\n"
"v_cndmask_b32 v33, 0, v34, vcc\n"
"v_sub_u32 v32, v32, v33\n"
"v_lshlrev_b32 v26, 6, v33\n"
"v_lshl_add_u32 v26, v32, 6, v26\n"
"v_cmp_lt_u32 vcc, 3, v32\n"
"v_mov_b32 v34, 32\n"
"s_nop 0\n"
"v_cndmask_b32 v34, 0, v34, vcc\n"
"v_add_u32 v26, v26, v34\n"
"v_cmp_eq_u32 vcc, 8, v32\n"
"v_mov_b32 v34, 288\n"
"s_nop 0\n"
"v_cndmask_b32 v34, 0, v34, vcc\n"
"v_sub_u32 v26, v26, v34\n"
"v_add_u32 v26, v26, v36\n"
"v_mov_b32 v34, 288\n"
"v_cndmask_b32 v34, 32, v34, vcc\n"
"v_add_u32 v27, v26, v34\n"
"v_add_u32 v32, 6, v39\n"
"v_cmp_lt_u32 vcc, 8, v32\n"
"v_mov_b32 v34, 9\n"
"s_nop 0\n"
"v_cndmask_b32 v33, 0, v34, vcc\n"
"v_sub_u32 v32, v32, v33\n"
"v_lshlrev_b32 v28, 6, v33\n"
"v_lshl_add_u32 v28, v32, 6, v28\n"
"v_cmp_lt_u32 vcc, 3, v32\n"
"v_mov_b32 v34, 32\n"
"s_nop 0\n"
"v_cndmask_b32 v34, 0, v34, vcc\n"
"v_add_u32 v28, v28, v34\n"
"v_cmp_eq_u32 vcc, 8, v32\n"
"v_mov_b32 v34, 288\n"
"s_nop 0\n"
"v_cndmask_b32 v34, 0, v34, vcc\n"
"v_sub_u32 v28, v28, v34\n"
"v_add_u32 v28, v28, v36\n"
"v_mov_b32 v34, 288\n"
"v_cndmask_b32 v34, 32, v34, vcc\n"
"v_add_u32 v29, v28, v34\n"
"v_add_u32 v32, 12, v39\n"
"v_cmp_lt_u32 vcc, 8, v32\n"
"v_mov_b32 v34, 9\n"
"s_nop 0\n"
"v_cndmask_b32 v33, 0, v34, vcc\n"
"v_sub_u32 v32, v32, v33\n"
"v_lshlrev_b32 v30, 6, v33\n"
"v_lshl_add_u32 v30, v32, 6, v30\n"
"v_cmp_lt_u32 vcc, 3, v32\n"
"v_mov_b32 v34, 32\n"
"s_nop 0\n"
"v_cndmask_b32 v34, 0, v34, vcc\n"
"v_add_u32 v30, v30, v34\n"
"v_cmp_eq_u32 vcc, 8, v32\n"
"v_mov_b32 v34, 288\n"
"s_nop 0\n"
"v_cndmask_b32 v34, 0, v34, vcc\n"
"v_sub_u32 v30, v30, v34\n"
"v_add_u32 v30, v30, v36\n"
"v_mov_b32 v34, 288\n"
"v_cndmask_b32 v34, 32, v34, vcc\n"
"v_add_u32 v31, v30, v34\n"
"s_mov_b32 s68, 0\n"
"L_el18_%=:\n"
"ds_read_b32 v14, v13\n"
"s_waitcnt lgkmcnt(0)\n"
"v_readfirstlane_b32 s69, v14\n"
"s_nop 1\n"
"s_andn2_b32 s69, s69, s68\n"
"s_cbranch_scc0 L_el18_%=\n"
"s_or_b32 s68, s68, s69\n"
"s_bitcmp1_b32 s69, 0\n"
"s_cbranch_scc0 L_es18_0_%=\n"
"s_add_u32 s62, s93, 0\n"
"buffer_load_dwordx4 v[56:59], v16, s[44:47], s62 offen sc1\n"
"s_add_u32 s62, s93, 24576\n"
"buffer_load_dwordx4 v[88:91], v16, s[44:47], s62 offen sc1\n"
"s_add_u32 s62, s93, 49152\n"
"buffer_load_dwordx4 v[120:123], v16, s[44:47], s62 offen sc1\n"
"L_es18_0_%=:\n"
"s_bitcmp1_b32 s69, 4\n"
"s_cbranch_scc0 L_es18_1_%=\n"
"s_add_u32 s62, s93, 2359296\n"
"buffer_load_dwordx4 v[60:63], v16, s[44:47], s62 offen sc1\n"
"s_add_u32 s62, s93, 2383872\n"
"buffer_load_dwordx4 v[92:95], v16, s[44:47], s62 offen sc1\n"
"s_add_u32 s62, s93, 2408448\n"
"buffer_load_dwordx4 v[124:127], v16, s[44:47], s62 offen sc1\n"
"L_es18_1_%=:\n"
"s_bitcmp1_b32 s69, 8\n"
"s_cbranch_scc0 L_es18_2_%=\n"
"s_add_u32 s62, s93, 4718592\n"
"buffer_load_dwordx4 v[64:67], v16, s[44:47], s62 offen sc1\n"
"s_add_u32 s62, s93, 4743168\n"
"buffer_load_dwordx4 v[96:99], v16, s[44:47], s62 offen sc1\n"
"s_add_u32 s62, s93, 4767744\n"
"buffer_load_dwordx4 v[128:131], v16, s[44:47], s62 offen sc1\n"
"L_es18_2_%=:\n"
"s_bitcmp1_b32 s69, 12\n"
"s_cbranch_scc0 L_es18_3_%=\n"
"s_add_u32 s62, s93, 7077888\n"
"buffer_load_dwordx4 v[68:71], v16, s[44:47], s62 offen sc1\n"
"s_add_u32 s62, s93, 7102464\n"
"buffer_load_dwordx4 v[100:103], v16, s[44:47], s62 offen sc1\n"
"s_add_u32 s62, s93, 7127040\n"
"buffer_load_dwordx4 v[132:135], v16, s[44:47], s62 offen sc1\n"
"L_es18_3_%=:\n"
"s_bitcmp1_b32 s69, 16\n"
"s_cbranch_scc0 L_es18_4_%=\n"
"s_add_u32 s62, s93, 9437184\n"
"buffer_load_dwordx4 v[72:75], v16, s[44:47], s62 offen sc1\n"
"s_add_u32 s62, s93, 9461760\n"
"buffer_load_dwordx4 v[104:107], v16, s[44:47], s62 offen sc1\n"
"s_add_u32 s62, s93, 9486336\n"
"buffer_load_dwordx4 v[136:139], v16, s[44:47], s62 offen sc1\n"
"L_es18_4_%=:\n"
"s_bitcmp1_b32 s69, 20\n"
"s_cbranch_scc0 L_es18_5_%=\n"
"s_add_u32 s62, s93, 11796480\n"
"buffer_load_dwordx4 v[76:79], v16, s[44:47], s62 offen sc1\n"
"s_add_u32 s62, s93, 11821056\n"
"buffer_load_dwordx4 v[108:111], v16, s[44:47], s62 offen sc1\n"
"s_add_u32 s62, s93, 11845632\n"
"buffer_load_dwordx4 v[140:143], v16, s[44:47], s62 offen sc1\n"
"L_es18_5_%=:\n"
"s_bitcmp1_b32 s69, 24\n"
"s_cbranch_scc0 L_es18_6_%=\n"
"s_add_u32 s62, s93, 14155776\n"
"buffer_load_dwordx4 v[80:83], v16, s[44:47], s62 offen sc1\n"
"s_add_u32 s62, s93, 14180352\n"
"buffer_load_dwordx4 v[112:115], v16, s[44:47], s62 offen sc1\n"
"s_add_u32 s62, s93, 14204928\n"
"buffer_load_dwordx4 v[144:147], v16, s[44:47], s62 offen sc1\n"
"L_es18_6_%=:\n"
"s_bitcmp1_b32 s69, 28\n"
"s_cbranch_scc0 L_es18_7_%=\n"
"s_add_u32 s62, s93, 16515072\n"
"buffer_load_dwordx4 v[84:87], v16, s[44:47], s62 offen sc1\n"
"s_add_u32 s62, s93, 16539648\n"
"buffer_load_dwordx4 v[116:119], v16, s[44:47], s62 offen sc1\n"
"s_add_u32 s62, s93, 16564224\n"
"buffer_load_dwordx4 v[148:151], v16, s[44:47], s62 offen sc1\n"
"L_es18_7_%=:\n"
"s_cmp_eq_u32 s68, 0x11111111\n"
"s_cbranch_scc0 L_el18_%=\n"
"s_waitcnt vmcnt(0)\n"
"v_mfma_f32_16x16x32_bf16 v[32:35], v[22:25], v[56:59], 0\n"
"v_mfma_f32_16x16x32_bf16 v[36:39], v[20:23], v[56:59], 0\n"
"v_mfma_f32_16x16x32_bf16 v[32:35], v[22:25], v[60:63], v[32:35]\n"
"v_mfma_f32_16x16x32_bf16 v[36:39], v[20:23], v[60:63], v[36:39]\n"
"v_mfma_f32_16x16x32_bf16 v[32:35], v[22:25], v[64:67], v[32:35]\n"
"v_mfma_f32_16x16x32_bf16 v[36:39], v[20:23], v[64:67], v[36:39]\n"
"v_mfma_f32_16x16x32_bf16 v[32:35], v[22:25], v[68:71], v[32:35]\n"
"v_mfma_f32_16x16x32_bf16 v[36:39], v[20:23], v[68:71], v[36:39]\n"
"v_mfma_f32_16x16x32_bf16 v[32:35], v[22:25], v[72:75], v[32:35]\n"
"v_mfma_f32_16x16x32_bf16 v[36:39], v[20:23], v[72:75], v[36:39]\n"
"v_mfma_f32_16x16x32_bf16 v[32:35], v[22:25], v[76:79], v[32:35]\n"
"v_mfma_f32_16x16x32_bf16 v[36:39], v[20:23], v[76:79], v[36:39]\n"
"v_mfma_f32_16x16x32_bf16 v[32:35], v[22:25], v[80:83], v[32:35]\n"
"v_mfma_f32_16x16x32_bf16 v[36:39], v[20:23], v[80:83], v[36:39]\n"
"v_mfma_f32_16x16x32_bf16 v[32:35], v[22:25], v[84:87], v[32:35]\n"
"v_mfma_f32_16x16x32_bf16 v[36:39], v[20:23], v[84:87], v[36:39]\n"
"v_mfma_f32_16x16x32_bf16 v[40:43], v[22:25], v[88:91], 0\n"
"v_mfma_f32_16x16x32_bf16 v[44:47], v[20:23], v[88:91], 0\n"
"v_mfma_f32_16x16x32_bf16 v[40:43], v[22:25], v[92:95], v[40:43]\n"
"v_mfma_f32_16x16x32_bf16 v[44:47], v[20:23], v[92:95], v[44:47]\n"
"v_mfma_f32_16x16x32_bf16 v[40:43], v[22:25], v[96:99], v[40:43]\n"
"v_mfma_f32_16x16x32_bf16 v[44:47], v[20:23], v[96:99], v[44:47]\n"
"v_mfma_f32_16x16x32_bf16 v[40:43], v[22:25], v[100:103], v[40:43]\n"
"v_mfma_f32_16x16x32_bf16 v[44:47], v[20:23], v[100:103], v[44:47]\n"
"v_mfma_f32_16x16x32_bf16 v[40:43], v[22:25], v[104:107], v[40:43]\n"
"v_mfma_f32_16x16x32_bf16 v[44:47], v[20:23], v[104:107], v[44:47]\n"
"v_mfma_f32_16x16x32_bf16 v[40:43], v[22:25], v[108:111], v[40:43]\n"
"v_mfma_f32_16x16x32_bf16 v[44:47], v[20:23], v[108:111], v[44:47]\n"
"v_mfma_f32_16x16x32_bf16 v[40:43], v[22:25], v[112:115], v[40:43]\n"
"v_mfma_f32_16x16x32_bf16 v[44:47], v[20:23], v[112:115], v[44:47]\n"
"v_mfma_f32_16x16x32_bf16 v[40:43], v[22:25], v[116:119], v[40:43]\n"
"v_mfma_f32_16x16x32_bf16 v[44:47], v[20:23], v[116:119], v[44:47]\n"
"v_mfma_f32_16x16x32_bf16 v[48:51], v[22:25], v[120:123], 0\n"
"v_mfma_f32_16x16x32_bf16 v[52:55], v[20:23], v[120:123], 0\n"
"v_mfma_f32_16x16x32_bf16 v[48:51], v[22:25], v[124:127], v[48:51]\n"
"v_mfma_f32_16x16x32_bf16 v[52:55], v[20:23], v[124:127], v[52:55]\n"
"v_mfma_f32_16x16x32_bf16 v[48:51], v[22:25], v[128:131], v[48:51]\n"
"v_mfma_f32_16x16x32_bf16 v[52:55], v[20:23], v[128:131], v[52:55]\n"
"v_mfma_f32_16x16x32_bf16 v[48:51], v[22:25], v[132:135], v[48:51]\n"
"v_mfma_f32_16x16x32_bf16 v[52:55], v[20:23], v[132:135], v[52:55]\n"
"v_mfma_f32_16x16x32_bf16 v[48:51], v[22:25], v[136:139], v[48:51]\n"
"v_mfma_f32_16x16x32_bf16 v[52:55], v[20:23], v[136:139], v[52:55]\n"
"v_mfma_f32_16x16x32_bf16 v[48:51], v[22:25], v[140:143], v[48:51]\n"
"v_mfma_f32_16x16x32_bf16 v[52:55], v[20:23], v[140:143], v[52:55]\n"
"v_mfma_f32_16x16x32_bf16 v[48:51], v[22:25], v[144:147], v[48:51]\n"
"v_mfma_f32_16x16x32_bf16 v[52:55], v[20:23], v[144:147], v[52:55]\n"
"v_mfma_f32_16x16x32_bf16 v[48:51], v[22:25], v[148:151], v[48:51]\n"
"v_mfma_f32_16x16x32_bf16 v[52:55], v[20:23], v[148:151], v[52:55]\n"
"mU 32,164,32\n"
"mU 33,165,33\n"
"mU 34,166,34\n"
"mU 35,167,35\n"
"mU 36,168,36\n"
"mU 37,169,37\n"
"mU 38,170,38\n"
"mU 39,171,39\n"
"cP 56,32,33\n"
"v_lshlrev_b32 v32, 16, v56\n"
"v_and_b32 v33, 0xffff0000, v56\n"
"cP 57,34,35\n"
"v_lshlrev_b32 v34, 16, v57\n"
"v_and_b32 v35, 0xffff0000, v57\n"
"cP 58,36,37\n"
"v_lshlrev_b32 v36, 16, v58\n"
"v_and_b32 v37, 0xffff0000, v58\n"
"cP 59,38,39\n"
"v_lshlrev_b32 v38, 16, v59\n"
"v_and_b32 v39, 0xffff0000, v59\n"
"v_lshlrev_b32 v60, 16, v236\n"
"v_and_b32 v61, 0xffff0000, v236\n"
"v_lshlrev_b32 v62, 16, v237\n"
"v_and_b32 v63, 0xffff0000, v237\n"
"v_lshlrev_b32 v64, 16, v238\n"
"v_and_b32 v65, 0xffff0000, v238\n"
"v_lshlrev_b32 v66, 16, v239\n"
"v_and_b32 v67, 0xffff0000, v239\n"
"aD 32,32,60\n"
"aD 33,33,61\n"
"aD 34,34,62\n"
"aD 35,35,63\n"
"aD 36,36,64\n"
"aD 37,37,65\n"
"aD 38,38,66\n"
"aD 39,39,67\n"
"cP 152,32,33\n"
"cP 153,34,35\n"
"cP 154,36,37\n"
"cP 155,38,39\n"
"buffer_store_dwordx2 v[152:153], v26, s[36:39], 0 offen\n"
"buffer_store_dwordx2 v[154:155], v27, s[36:39], 0 offen\n"
"mU 40,172,40\n"
"mU 41,173,41\n"
"mU 42,174,42\n"
"mU 43,175,43\n"
"mU 44,176,44\n"
"mU 45,177,45\n"
"mU 46,178,46\n"
"mU 47,179,47\n"
"cP 88,40,41\n"
"v_lshlrev_b32 v40, 16, v88\n"
"v_and_b32 v41, 0xffff0000, v88\n"
"cP 89,42,43\n"
"v_lshlrev_b32 v42, 16, v89\n"
"v_and_b32 v43, 0xffff0000, v89\n"
"cP 90,44,45\n"
"v_lshlrev_b32 v44, 16, v90\n"
"v_and_b32 v45, 0xffff0000, v90\n"
"cP 91,46,47\n"
"v_lshlrev_b32 v46, 16, v91\n"
"v_and_b32 v47, 0xffff0000, v91\n"
"v_lshlrev_b32 v92, 16, v240\n"
"v_and_b32 v93, 0xffff0000, v240\n"
"v_lshlrev_b32 v94, 16, v241\n"
"v_and_b32 v95, 0xffff0000, v241\n"
"v_lshlrev_b32 v96, 16, v242\n"
"v_and_b32 v97, 0xffff0000, v242\n"
"v_lshlrev_b32 v98, 16, v243\n"
"v_and_b32 v99, 0xffff0000, v243\n"
"aD 40,40,92\n"
"aD 41,41,93\n"
"aD 42,42,94\n"
"aD 43,43,95\n"
"aD 44,44,96\n"
"aD 45,45,97\n"
"aD 46,46,98\n"
"aD 47,47,99\n"
"cP 156,40,41\n"
"cP 157,42,43\n"
"cP 158,44,45\n"
"cP 159,46,47\n"
"buffer_store_dwordx2 v[156:157], v28, s[36:39], 0 offen\n"
"buffer_store_dwordx2 v[158:159], v29, s[36:39], 0 offen\n"
"mU 48,180,48\n"
"mU 49,181,49\n"
"mU 50,182,50\n"
"mU 51,183,51\n"
"mU 52,184,52\n"
"mU 53,185,53\n"
"mU 54,186,54\n"
"mU 55,187,55\n"
"cP 120,48,49\n"
"v_lshlrev_b32 v48, 16, v120\n"
"v_and_b32 v49, 0xffff0000, v120\n"
"cP 121,50,51\n"
"v_lshlrev_b32 v50, 16, v121\n"
"v_and_b32 v51, 0xffff0000, v121\n"
"cP 122,52,53\n"
"v_lshlrev_b32 v52, 16, v122\n"
"v_and_b32 v53, 0xffff0000, v122\n"
"cP 123,54,55\n"
"v_lshlrev_b32 v54, 16, v123\n"
"v_and_b32 v55, 0xffff0000, v123\n"
"v_lshlrev_b32 v124, 16, v244\n"
"v_and_b32 v125, 0xffff0000, v244\n"
"v_lshlrev_b32 v126, 16, v245\n"
"v_and_b32 v127, 0xffff0000, v245\n"
"v_lshlrev_b32 v128, 16, v246\n"
"v_and_b32 v129, 0xffff0000, v246\n"
"v_lshlrev_b32 v130, 16, v247\n"
"v_and_b32 v131, 0xffff0000, v247\n"
"aD 48,48,124\n"
"aD 49,49,125\n"
"aD 50,50,126\n"
"aD 51,51,127\n"
"aD 52,52,128\n"
"aD 53,53,129\n"
"aD 54,54,130\n"
"aD 55,55,131\n"
"cP 160,48,49\n"
"cP 161,50,51\n"
"cP 162,52,53\n"
"cP 163,54,55\n"
"buffer_store_dwordx2 v[160:161], v30, s[36:39], 0 offen\n"
"buffer_store_dwordx2 v[162:163], v31, s[36:39], 0 offen\n"
"s_waitcnt vmcnt(0) lgkmcnt(0)\n"
"s_branch L_rj18_%=\n"
"L_r318_%=:\n"
"s_lshl_b32 s62, s58, 8\n"
"s_add_u32 s62, s62, 75776\n"
"s_add_u32 s66, %[wslo], s62\n"
"s_addc_u32 s67, %[wshi], 0\n"
"v_and_b32 v16, 31, v1\n"
"v_lshlrev_b32 v16, 3, v16\n"
"s_mov_b32 s63, 131072\n"
"L_p819_%=:\n"
"global_load_dwordx2 v[14:15], v16, s[66:67] sc1\n"
"s_waitcnt vmcnt(0)\n"
"v_cmp_eq_u64 vcc, s[98:99], v[14:15]\n"
"s_nop 1\n"
"s_lshr_b32 s62, vcc_lo, 1\n"
"s_and_b32 s62, s62, vcc_lo\n"
"s_lshr_b32 s64, s62, 2\n"
"s_and_b32 s62, s62, s64\n"
"s_and_b32 s62, s62, 0x11111111\n"
"s_sub_u32 s63, s63, 1\n"
"s_cmp_eq_u32 s63, 0\n"
"s_cselect_b32 s62, 0x11111111, s62\n"
"v_mov_b32 v14, s62\n"
"ds_write_b32 v13, v14\n"
"s_sleep 2\n"
"s_cmp_eq_u32 s62, 0x11111111\n"
"s_cbranch_scc0 L_p819_%=\n"
"L_rj18_%=:\n"
"s_nop 11\n"
"L_fin_%=:\n"
".purgem mA\n"
".purgem mZ\n"
".purgem mB\n"
".purgem dT\n"
".purgem dK\n"
".purgem eX\n"
".purgem cP\n"
".purgem aD\n"
".purgem mU\n"
    : : [tid]"v"(tid), [lds]"s"((unsigned)(size_t)lds), [bid]"s"(blockIdx.x), [taglo]"s"(taglo), [taghi]"s"(taghi), [xlo]"s"((unsigned)(size_t)x), [xhi]"s"((unsigned)((size_t)x >> 32)), [klo]"s"((unsigned)(size_t)k), [khi]"s"((unsigned)((size_t)k >> 32)), [vlo]"s"((unsigned)(size_t)v), [vhi]"s"((unsigned)((size_t)v >> 32)), [wqlo]"s"((unsigned)(size_t)wq), [wqhi]"s"((unsigned)((size_t)wq >> 32)), [wolo]"s"((unsigned)(size_t)wo), [wohi]"s"((unsigned)((size_t)wo >> 32)), [w1lo]"s"((unsigned)(size_t)w1), [w1hi]"s"((unsigned)((size_t)w1 >> 32)), [w2lo]"s"((unsigned)(size_t)w2), [w2hi]"s"((unsigned)((size_t)w2 >> 32)), [outlo]"s"((unsigned)(size_t)out), [outhi]"s"((unsigned)((size_t)out >> 32)), [wslo]"s"((unsigned)(size_t)ws), [wshi]"s"((unsigned)((size_t)ws >> 32)), [clo]"s"((unsigned)(size_t)c), [chi]"s"((unsigned)((size_t)c >> 32)), [wmlo]"s"((unsigned)(size_t)wm), [wmhi]"s"((unsigned)((size_t)wm >> 32))
    : "v1","v2","v3","v4","v5","v6","v7","v8","v9","v10","v11","v12","v13","v14","v15","v16","v17","v18","v19","v20","v21","v22","v23","v24","v25","v26","v27","v28","v29","v30","v31","v32","v33","v34","v35","v36","v37","v38","v39","v40","v41","v42","v43","v44","v45","v46","v47","v48","v49","v50","v51","v52","v53","v54","v55","v56","v57","v58","v59","v60","v61","v62","v63","v64","v65","v66","v67","v68","v69","v70","v71","v72","v73","v74","v75","v76","v77","v78","v79","v80","v81","v82","v83","v84","v85","v86","v87","v88","v89","v90","v91","v92","v93","v94","v95","v96","v97","v98","v99","v100","v101","v102","v103","v104","v105","v106","v107","v108","v109","v110","v111","v112","v113","v114","v115","v116","v117","v118","v119","v120","v121","v122","v123","v124","v125","v126","v127","v128","v129","v130","v131","v132","v133","v134","v135","v136","v137","v138","v139","v140","v141","v142","v143","v144","v145","v146","v147","v148","v149","v150","v151","v152","v153","v154","v155","v156","v157","v158","v159","v160","v161","v162","v163","v164","v165","v166","v167","v168","v169","v170","v171","v172","v173","v174","v175","v176","v177","v178","v179","v180","v181","v182","v183","v184","v185","v186","v187","v188","v189","v190","v191","v192","v193","v194","v195","v196","v197","v198","v199","v200","v201","v202","v203","v204","v205","v206","v207","v208","v209","v210","v211","v212","v213","v214","v215","v216","v217","v218","v219","v220","v221","v222","v223","v224","v225","v226","v227","v228","v229","v230","v231","v232","v233","v234","v235","v236","v237","v238","v239","v240","v241","v242","v243","v244","v245","v246","v247","v248","v249","v250","v251","v252","v253","v254","v255","a0","a1","a2","a3","a4","a5","a6","a7","a8","a9","a10","a11","a12","a13","a14","a15","a16","a17","a18","a19","a20","a21","a22","a23","a24","a25","a26","a27","a28","a29","a30","a31","a32","a33","a34","a35","a36","a37","a38","a39","a40","a41","a42","a43","a44","a45","a46","a47","a48","a49","a50","a51","a52","a53","a54","a55","a56","a57","a58","a59","a60","a61","a62","a63","a64","a65","a66","a67","a68","a69","a70","a71","a72","a73","a74","a75","a76","a77","a78","a79","a80","a81","a82","a83","a84","a85","a86","a87","a88","a89","a90","a91","a92","a93","a94","a95","a96","a97","a98","a99","a100","a101","a102","a103","a104","a105","a106","a107","a108","a109","a110","a111","a112","a113","a114","a115","a116","a117","a118","a119","a120","a121","a122","a123","a124","a125","a126","a127","a128","a129","a130","a131","a132","a133","a134","a135","a136","a137","a138","a139","a140","a141","a142","a143","a144","a145","a146","a147","a148","a149","a150","a151","a152","a153","a154","a155","a156","a157","a158","a159","a160","a161","a162","a163","a164","a165","a166","a167","a168","a169","a170","a171","a172","a173","a174","a175","a176","a177","a178","a179","a180","a181","a182","a183","a184","a185","a186","a187","a188","a189","a190","a191","a192","a193","a194","a195","a196","a197","a198","a199","a200","a201","a202","a203","a204","a205","a206","a207","a208","a209","a210","a211","a212","a213","a214","a215","a216","a217","a218","a219","a220","a221","a222","a223","a224","a225","a226","a227","a228","a229","a230","a231","a232","a233","a234","a235","a236","a237","a238","a239","a240","a241","a242","a243","a244","a245","a246","a247","a248","a249","a250","a251","a252","a253","a254","a255","s36","s37","s38","s39","s40","s41","s42","s43","s44","s45","s46","s47","s48","s49","s50","s51","s52","s53","s54","s55","s56","s57","s58","s59","s60","s61","s62","s63","s64","s65","s66","s67","s68","s69","s70","s71","s72","s73","s74","s75","s76","s77","s78","s79","s80","s81","s82","s83","s84","s85","s86","s87","s88","s89","s90","s91","s92","s93","s94","s95","s96","s97","s98","s99","s100","s101","memory","vcc","scc");
}

extern "C" void kernel_launch(void* const* d_in, const int* in_sizes, int n_in, void* d_out, int out_size, void* d_ws, size_t ws_size, hipStream_t stream) {
  k_main<<<256, 256, 0, stream>>>((const u16*)d_in[0], (const u16*)d_in[1], (const u16*)d_in[2], (const u16*)d_in[3], (const u16*)d_in[4], (const u16*)d_in[6], (const u16*)d_in[7],
                                  (const u16*)d_in[8], (const u16*)d_in[9], (u16*)d_out, (char*)d_ws);
}
```
